```python
import torch

def _softmax(x, axis=-1):
    return torch.softmax(x, dim=axis)
def _gelu(x):
    return torch.nn.functional.gelu(x, approximate="tanh")
def _silu(x):
    return torch.nn.functional.silu(x)
def _sigmoid(x):
    return torch.sigmoid(x)
def _layernorm(x, axis=-1, eps=1e-5):
    m = x.mean(dim=axis, keepdim=True)
    v = x.var(dim=axis, keepdim=True, unbiased=False)
    return (x - m) / torch.sqrt(v + eps)
def _scatter_add(tgt, idx, src, axis=0):
    return tgt.clone().index_add_(axis, idx, src)
def _segment_sum(src, seg, n):
    out = torch.zeros((n,) + src.shape[1:], dtype=src.dtype)
    return out.index_add_(0, seg, src)
def _pad(x, pw):
    flat = tuple(v for pair in reversed(pw) for v in pair)
    return torch.nn.functional.pad(x, flat)

kernel_name = 'interleave_layers__ssm_conv_h3__ssm_conv_s4__prompt'

def setup_inputs():

    _w = (lambda a: (lambda t: t.to(torch.bfloat16) if t.is_floating_point() else t)(torch.as_tensor(a)))
    def _A_setup():
        B, S, D = 1, 2048, 512
        ks = 4
        return {
            "q": _w((lambda *s: torch.randn(*s, dtype=torch.bfloat16) * 0.05)(B, S, D)),
            "k": _w((lambda *s: torch.randn(*s, dtype=torch.bfloat16) * 0.4)(B, S, D)),
            "v": _w((lambda *s: torch.randn(*s, dtype=torch.bfloat16) * 0.4)(B, S, D)),
            "shift_k": _w((lambda *s: torch.randn(*s, dtype=torch.bfloat16) * 0.4)(D, ks) * 0.5),
            "ssm_k": _w((lambda *s: torch.randn(*s, dtype=torch.bfloat16) * 0.4)(D, S) * (1.0 / S ** 0.5)),
            "ssm_D": _w((lambda *s: torch.randn(*s, dtype=torch.bfloat16) * 0.4)(D)),
        }
    _A_in = _A_setup()

    def _B_setup():
        B, L, D, N = 1, 2048, 512, 128
        A_re = -(lambda _x: torch.exp(torch.as_tensor(_x)))((lambda *s: torch.randn(*s, dtype=torch.bfloat16) * 6.4)(D, N))
        A_im = torch.pi * (lambda *s: torch.randn(*s, dtype=torch.bfloat16) * 0.05)(D, N)
        B_re = (lambda *s: torch.randn(*s, dtype=torch.bfloat16) * 0.1)(D, N)
        B_im = (lambda *s: torch.randn(*s, dtype=torch.bfloat16) * 0.1)(D, N)
        C_re = (lambda *s: torch.randn(*s, dtype=torch.bfloat16) * 0.1)(D, N)
        C_im = (lambda *s: torch.randn(*s, dtype=torch.bfloat16) * 0.1)(D, N)
        log_dt = (lambda *s: torch.randn(*s, dtype=torch.bfloat16) * 0.05)(D) * 0.5 - 4.0
        Dskip = (lambda *s: torch.randn(*s, dtype=torch.bfloat16) * 0.05)(D)
        return {
            "u": _w((lambda *s: torch.randn(*s, dtype=torch.bfloat16) * 0.05)(B, L, D)),
            "A_re": _w(A_re),
            "A_im": _w(A_im),
            "B_re": _w(B_re),
            "B_im": _w(B_im),
            "C_re": _w(C_re),
            "C_im": _w(C_im),
            "log_dt": _w(log_dt),
            "Dskip": _w(Dskip),
        }
    _B_in = _B_setup()

    return {
        "A_q": _A_in["q"],
        "A_k": _A_in["k"],
        "A_v": _A_in["v"],
        "A_shift_k": _A_in["shift_k"],
        "A_ssm_k": _A_in["ssm_k"],
        "A_ssm_D": _A_in["ssm_D"],
        "B_u": _B_in["u"],
        "B_A_re": _B_in["A_re"],
        "B_A_im": _B_in["A_im"],
        "B_B_re": _B_in["B_re"],
        "B_B_im": _B_in["B_im"],
        "B_C_re": _B_in["C_re"],
        "B_C_im": _B_in["C_im"],
        "B_log_dt": _B_in["log_dt"],
        "B_Dskip": _B_in["Dskip"],
    }

def reference(A_q, A_k, A_v, A_shift_k, A_ssm_k, A_ssm_D, B_u, B_A_re, B_A_im, B_B_re, B_B_im, B_C_re, B_C_im, B_log_dt, B_Dskip):
    def _A_ref(q, k, v, shift_k, ssm_k, ssm_D):
        B, S, D = q.shape
        ks = shift_k.shape[-1]
        k_pad = _pad(k, ((0, 0), (ks - 1, 0), (0, 0)))
        k_shift = torch.zeros_like(k)
        for t in range(ks):
            k_shift = k_shift + k_pad[:, t:t + S, :] * shift_k[:, ks - 1 - t][None, None, :]
        kv = k_shift * v
        L = 2 * S
        kv_pad = _pad(kv, ((0, 0), (0, S), (0, 0)))
        kf_pad = _pad(ssm_k, ((0, 0), (0, S)))
        kv_f = (lambda x, axes=(-2,-1): torch.fft.rfft2(x.float(), dim=axes))(kv_pad[:, None, :, :], axes=(1, 2))
        kf_f = (lambda x, axes=(-2,-1): torch.fft.rfft2(x.float(), dim=axes))(kf_pad[:, None, :], axes=(1, 2))
        kf_f = (lambda x, *d: x.permute(*d))(kf_f, 1, 2, 0)
        y_f = kv_f * kf_f[None, :, :, :]
        y = (lambda x, s, axes=(-2,-1): torch.fft.irfft2(x, s=s, dim=axes).to(torch.bfloat16))(y_f, (1, L), axes=(1, 2))[:, 0, :S, :]
        y = y + kv * ssm_D[None, None, :]
        return q * y

    def _B_ref(u, A_re, A_im, B_re, B_im, C_re, C_im, log_dt, Dskip):
        Bsz, L, D = u.shape
        dt = torch.exp(log_dt)[:, None]
        A = A_re + 1j * A_im
        Bc = B_re + 1j * B_im
        Cc = C_re + 1j * C_im
        logdA = dt * A
        dB = dt * Bc
        l = (lambda x, dt: x.to(dt))(torch.arange(L), A_re.dtype)
        P = torch.exp(logdA[:, :, None] * l[None, None, :])
        K = torch.einsum("dn,dnl,dn->dl", Cc, P, dB).real
        L2 = 2 * L
        Kp = _pad(K, ((0, 0), (0, L)))
        up = (lambda x, *d: x.permute(*d))(u, 0, 2, 1)
        up = _pad(up, ((0, 0), (0, 0), (0, L)))
        Kf = (lambda x, axes=(-2,-1): torch.fft.rfft2(x.float(), dim=axes))(Kp[:, None, :], axes=(-2, -1))
        Uf = (lambda x, axes=(-2,-1): torch.fft.rfft2(x.float(), dim=axes))(up[:, :, None, :], axes=(-2, -1))
        Yf = Uf * Kf[None, :, :, :]
        y = (lambda x, s, axes=(-2,-1): torch.fft.irfft2(x, s=s, dim=axes).to(torch.bfloat16))(Yf, (1, L2), axes=(-2, -1))[:, :, 0, :L]
        y = (lambda x, *d: x.permute(*d))(y, 0, 2, 1) + u * Dskip[None, None, :]
        return y

    x = A_q
    x = x + _A_ref(_layernorm(x, axis=-1), A_k, A_v, A_shift_k, A_ssm_k, A_ssm_D)
    x = x + _B_ref(_layernorm(x, axis=-1), B_A_re, B_A_im, B_B_re, B_B_im, B_C_re, B_C_im, B_log_dt, B_Dskip)
    x = x + _A_ref(_layernorm(x, axis=-1), A_k, A_v, A_shift_k, A_ssm_k, A_ssm_D)
    x = x + _B_ref(_layernorm(x, axis=-1), B_A_re, B_A_im, B_B_re, B_B_im, B_C_re, B_C_im, B_log_dt, B_Dskip)
    return x
```

```cpp
#include <hip/hip_runtime.h>
#include <hip/hip_bf16.h>
#include <stdint.h>

#define S_ 2048
#define D_ 512
#define N_ 128
typedef unsigned short u16;

__device__ __forceinline__ float bf2f(u16 x) { return __uint_as_float(((unsigned)x) << 16); }
__device__ __forceinline__ u16 f2bf(float f) { unsigned u = __float_as_uint(f); return (u16)((u + 0x7FFFu + ((u >> 16) & 1u)) >> 16); }
__device__ __forceinline__ float rbf(float f) { return bf2f(f2bf(f)); }

__global__ void k_kv(const u16* k, const u16* v, const u16* sk, u16* kvT) {
    int d = threadIdx.x + blockIdx.x * 256 % D_;
    int idx = blockIdx.x * 256 + threadIdx.x; d = idx % D_; int t = idx / D_;
    float acc = 0.f;
    for (int j = 0; j < 4; ++j) {
        int tt = t - 3 + j;
        float kk = tt >= 0 ? bf2f(k[tt * D_ + d]) : 0.f;
        acc = rbf(acc + rbf(kk * bf2f(sk[d * 4 + 3 - j])));
    }
    kvT[d * S_ + t] = f2bf(acc * bf2f(v[t * D_ + d]));
}
__global__ void k_K(const u16* Are, const u16* Aim, const u16* Bre, const u16* Bim, const u16* Cre, const u16* Cim, const u16* logdt, float* K, int rnd) {
    int idx = blockIdx.x * 256 + threadIdx.x; int d = idx / S_, l = idx % S_;
    float dt = rbf(expf(bf2f(logdt[d])));
    float lb = rbf((float)l);
    float acc = 0.f;
    for (int n = 0; n < N_; ++n) {
        float ar = dt * bf2f(Are[d * N_ + n]), ai = dt * bf2f(Aim[d * N_ + n]);
        float br = dt * bf2f(Bre[d * N_ + n]), bi = dt * bf2f(Bim[d * N_ + n]);
        float cr = bf2f(Cre[d * N_ + n]), ci = bf2f(Cim[d * N_ + n]);
        float e = expf(ar * lb), s, c; sincosf(ai * lb, &s, &c);
        float pr = e * c, pi = e * s;
        float wr = cr * br - ci * bi, wi = cr * bi + ci * br;
        acc += wr * pr - wi * pi;
    }
    K[idx] = rnd ? rbf(acc) : acc;
}
__global__ void k_cvt(const u16* a, float* o) { int idx = blockIdx.x * 256 + threadIdx.x; o[idx] = bf2f(a[idx]); }
__global__ void k_conv(const float* Kf, const u16* uT, u16* yT) {
    __shared__ float sK[S_]; __shared__ float su[S_];
    int d = blockIdx.x;
    for (int i = threadIdx.x; i < S_; i += 256) { sK[i] = Kf[d * S_ + i]; su[i] = bf2f(uT[d * S_ + i]); }
    __syncthreads();
    for (int t = threadIdx.x; t < S_; t += 256) {
        float acc = 0.f;
        for (int s = 0; s <= t; ++s) acc += sK[t - s] * su[s];
        yT[d * S_ + t] = f2bf(acc);
    }
}
__device__ __forceinline__ float wsum(float v) { for (int o = 32; o; o >>= 1) v += __shfl_xor(v, o); return v; }
__device__ __forceinline__ void ln8(const float* x, float* o) {
    float s = 0; for (int j = 0; j < 8; ++j) s += x[j];
    float m = wsum(s) * (1.f / D_);
    float q = 0; for (int j = 0; j < 8; ++j) q += (x[j] - m) * (x[j] - m);
    float var = wsum(q) * (1.f / D_);
    float mb = rbf(m), vb = rbf(var);
    float sd = rbf(sqrtf(rbf(vb + 1e-5f)));
    for (int j = 0; j < 8; ++j) o[j] = rbf(rbf(x[j] - mb) / sd);
}
__global__ void k_row1(const u16* q, const u16* yT, const u16* kvT, const u16* ssmD, u16* yA, u16* x1o, u16* u1T) {
    int t = blockIdx.x, l = threadIdx.x;
    float x[8], n[8], y[8];
    for (int j = 0; j < 8; ++j) { int d = l * 8 + j; x[j] = bf2f(q[t * D_ + d]);
        y[j] = rbf(bf2f(yT[d * S_ + t]) + rbf(bf2f(kvT[d * S_ + t]) * bf2f(ssmD[d]))); yA[t * D_ + d] = f2bf(y[j]); }
    ln8(x, n);
    for (int j = 0; j < 8; ++j) x[j] = rbf(x[j] + rbf(n[j] * y[j]));
    ln8(x, n);
    for (int j = 0; j < 8; ++j) { int d = l * 8 + j; x1o[t * D_ + d] = f2bf(x[j]); u1T[d * S_ + t] = f2bf(n[j]); }
}
__global__ void k_row2(const u16* x1, const u16* u1T, const u16* c1T, const u16* Dskip, const u16* yA, u16* x3o, u16* u2T) {
    int t = blockIdx.x, l = threadIdx.x;
    float x[8], n[8];
    for (int j = 0; j < 8; ++j) { int d = l * 8 + j;
        float yy = rbf(bf2f(c1T[d * S_ + t]) + rbf(bf2f(u1T[d * S_ + t]) * bf2f(Dskip[d])));
        x[j] = rbf(bf2f(x1[t * D_ + d]) + yy); }
    ln8(x, n);
    for (int j = 0; j < 8; ++j) { int d = l * 8 + j; x[j] = rbf(x[j] + rbf(n[j] * bf2f(yA[t * D_ + d]))); }
    ln8(x, n);
    for (int j = 0; j < 8; ++j) { int d = l * 8 + j; x3o[t * D_ + d] = f2bf(x[j]); u2T[d * S_ + t] = f2bf(n[j]); }
}
__global__ void k_row3(const u16* x3, const u16* u2T, const u16* c2T, const u16* Dskip, u16* out) {
    int idx = blockIdx.x * 256 + threadIdx.x; int d = idx % D_, t = idx / D_;
    float yy = rbf(bf2f(c2T[d * S_ + t]) + rbf(bf2f(u2T[d * S_ + t]) * bf2f(Dskip[d])));
    out[idx] = f2bf(bf2f(x3[idx]) + yy);
}

#define RNDK 1
extern "C" void kernel_launch(void* const* d_in, const int* in_sizes, int n_in,
                              void* d_out, int out_size, void* d_ws, size_t ws_size,
                              hipStream_t stream) {
    const u16* q = (const u16*)d_in[0]; const u16* k = (const u16*)d_in[1]; const u16* v = (const u16*)d_in[2];
    const u16* sk = (const u16*)d_in[3]; const u16* ssmk = (const u16*)d_in[4]; const u16* ssmD = (const u16*)d_in[5];
    const u16* Are = (const u16*)d_in[7]; const u16* Aim = (const u16*)d_in[8]; const u16* Bre = (const u16*)d_in[9];
    const u16* Bim = (const u16*)d_in[10]; const u16* Cre = (const u16*)d_in[11]; const u16* Cim = (const u16*)d_in[12];
    const u16* logdt = (const u16*)d_in[13]; const u16* Dskip = (const u16*)d_in[14];
    char* ws = (char*)d_ws; const size_t M = 1 << 20;
    u16* kvT = (u16*)(ws); float* Kf = (float*)(ws + 2 * M); float* Sf = (float*)(ws + 6 * M);
    u16* yT = (u16*)(ws + 10 * M); u16* yA = (u16*)(ws + 12 * M); u16* x1 = (u16*)(ws + 14 * M); u16* uT = (u16*)(ws + 16 * M);
    u16* cT = (u16*)(ws + 18 * M); u16* x3 = (u16*)(ws + 20 * M); u16* u2T = (u16*)(ws + 22 * M);
    const int NB = S_ * D_ / 256;
    k_kv<<<NB, 256, 0, stream>>>(k, v, sk, kvT);
    k_K<<<NB, 256, 0, stream>>>(Are, Aim, Bre, Bim, Cre, Cim, logdt, Kf, RNDK);
    k_cvt<<<NB, 256, 0, stream>>>(ssmk, Sf);
    k_conv<<<D_, 256, 0, stream>>>(Sf, kvT, yT);
    k_row1<<<S_, 64, 0, stream>>>(q, yT, kvT, ssmD, yA, x1, uT);
    k_conv<<<D_, 256, 0, stream>>>(Kf, uT, cT);
    k_row2<<<S_, 64, 0, stream>>>(x1, uT, cT, Dskip, yA, x3, u2T);
    k_conv<<<D_, 256, 0, stream>>>(Kf, u2T, cT);
    k_row3<<<NB, 256, 0, stream>>>(x3, u2T, cT, Dskip, (u16*)d_out);
}
```

```cpp
#include <hip/hip_runtime.h>
#include <stdint.h>

typedef unsigned short u16;
typedef unsigned u32;
typedef u32 v4u __attribute__((ext_vector_type(4)));
typedef float v2f __attribute__((ext_vector_type(2)));
typedef float v4f __attribute__((ext_vector_type(4)));
typedef __bf16 v2bf __attribute__((ext_vector_type(2)));
typedef __bf16 v8bf __attribute__((ext_vector_type(8)));

constexpr int S_ = 2048, D_ = 512, NS_ = 128;
constexpr u32 POISON = 0xFFFFFFFFu;

constexpr u32 WS_EPOCH = 0;
constexpr u32 WS_FLAGS = 4096;
constexpr u32 KV_PITCH = 4096 + 128;
constexpr u32 WS_KV = 8192;
constexpr u32 C_PITCH = 32768 + 128;
constexpr u32 C_SIZE = 64 * C_PITCH;
constexpr u32 WS_C = WS_KV + 512 * KV_PITCH;
constexpr u32 U_PITCH = 8192 + 128;
constexpr u32 U_SIZE = 256 * U_PITCH;
constexpr u32 WS_U = WS_C + 3 * C_SIZE;
constexpr u32 WS_TOTAL = WS_U + 2 * U_SIZE;

constexpr int UR_PITCH = 4608;
constexpr int L_UR = 0;
constexpr int PART_CH = 2048 + 16;
constexpr int PART_TILE = 2 * PART_CH;
constexpr int L_PART = L_UR + 2 * UR_PITCH;
constexpr int KC_COPY = 4160;
constexpr int KC_CH = 2 * KC_COPY;
constexpr int L_KA = L_PART + 8 * PART_TILE;
constexpr int L_KB = L_KA + 2 * KC_CH;
constexpr int KT_CH = 648 * 4;
constexpr int L_KT = L_KB + 2 * KC_CH;
constexpr int L_PT = L_KT + 2 * KT_CH;
constexpr int L_T = L_PT + 4096;
constexpr int L_RED = L_T + 8192;
constexpr int L_SYNC = L_RED + 2 * 512;
constexpr int SY_INTILE = L_SYNC;
constexpr int SY_PART = L_SYNC + 32;
constexpr int SY_KV = L_SYNC + 64;
constexpr int SY_GO = L_SYNC + 68;
constexpr int SY_KT = L_SYNC + 72;
constexpr int SY_KREADY = L_SYNC + 76;
constexpr int SY_ABORT = L_SYNC + 80;
constexpr int LDS_BYTES = L_SYNC + 96;
static_assert(LDS_BYTES > 82 * 1024 && LDS_BYTES <= 160 * 1024, "one workgroup per CU");

constexpr u32 LDS_SPIN_CAP = 1u << 20;
constexpr u32 GLB_SPIN_CAP = 1u << 16;

__device__ __forceinline__ float bf_lo(u32 x) { return __builtin_bit_cast(float, x << 16); }
__device__ __forceinline__ float bf_hi(u32 x) { return __builtin_bit_cast(float, x & 0xFFFF0000u); }
__device__ __forceinline__ float bf2f(u16 x) { return __builtin_bit_cast(float, ((u32)x) << 16); }
__device__ __forceinline__ u32 pack_bf(float lo, float hi) {
    v2f f = {lo, hi};
    return __builtin_bit_cast(u32, __builtin_convertvector(f, v2bf));
}
__device__ __forceinline__ u32 no_poison(u32 x) { return x < 0xFFFFFFFEu ? x : 0xFFFFFFFEu; }
template <int CTRL> __device__ __forceinline__ float dppf(float x) {
    return __builtin_bit_cast(float, __builtin_amdgcn_mov_dpp(__builtin_bit_cast(int, x), CTRL, 0xf, 0xf, true));
}
template <int CTRL> __device__ __forceinline__ u32 dppu(u32 x) { return (u32)__builtin_amdgcn_mov_dpp((int)x, CTRL, 0xf, 0xf, true); }

#define STAMP(n) do {} while (0)
__device__ __forceinline__ u32 lds_ld(unsigned char* lds, int off) {
    return __hip_atomic_load((u32*)(lds + off), __ATOMIC_RELAXED, __HIP_MEMORY_SCOPE_WORKGROUP);
}
__device__ __forceinline__ void lds_st(unsigned char* lds, int off, u32 v) {
    __hip_atomic_store((u32*)(lds + off), v, __ATOMIC_RELAXED, __HIP_MEMORY_SCOPE_WORKGROUP);
}
__device__ __forceinline__ void lds_inc(unsigned char* lds, int off) {
    (void)__hip_atomic_fetch_add((u32*)(lds + off), 1u, __ATOMIC_RELAXED, __HIP_MEMORY_SCOPE_WORKGROUP);
}
__device__ __forceinline__ void lds_signal_inc(unsigned char* lds, int off, int lane) {
    asm volatile("s_waitcnt lgkmcnt(0)" ::: "memory");
    if (lane == 0) lds_inc(lds, off);
}
__device__ __forceinline__ void lds_signal_set(unsigned char* lds, int off, u32 v, int lane) {
    asm volatile("s_waitcnt lgkmcnt(0)" ::: "memory");
    if (lane == 0) lds_st(lds, off, v);
}
__device__ __forceinline__ void lds_wait_ge(unsigned char* lds, int off, u32 target) {
    for (u32 it = 0;; ++it) {
        const u32 v = __builtin_amdgcn_readfirstlane(lds_ld(lds, off));
        if (v >= target) break;
        if ((it & 63u) == 63u) {
            if (__builtin_amdgcn_readfirstlane(lds_ld(lds, SY_ABORT))) break;
            if (it > LDS_SPIN_CAP) { lds_st(lds, SY_ABORT, 1u); break; }
        }
        __builtin_amdgcn_s_sleep(1);
    }
    asm volatile("" ::: "memory");
}
__device__ __forceinline__ bool valid16(v4u r) {
    u32 m = r.x > r.y ? r.x : r.y;
    m = m > r.z ? m : r.z;
    m = m > r.w ? m : r.w;
    return m != POISON;
}
__device__ __forceinline__ v4u poll16(__amdgpu_buffer_rsrc_t rs, u32 voff, u32 soff, unsigned char* lds) {
    v4u r = __builtin_amdgcn_raw_buffer_load_b128(rs, voff, soff, 16);
    bool ok = valid16(r);
    for (u32 it = 0; !__all(ok); ++it) {
        if ((it & 15u) == 15u) {
            if (__builtin_amdgcn_readfirstlane(lds_ld(lds, SY_ABORT))) break;
            if (it > GLB_SPIN_CAP) { lds_st(lds, SY_ABORT, 1u); break; }
        }
        asm volatile("" ::: "memory");
        if (!ok) { r = __builtin_amdgcn_raw_buffer_load_b128(rs, voff, soff, 16); ok = valid16(r); }
    }
    return r;
}

__device__ __forceinline__ void row_stats(const float (&x)[8], unsigned char* lds, int redoff, int w, int l, float (&mr)[8], float (&rs)[8]) {
    float z[4];
    {
        float a[8];
#pragma unroll
        for (int t = 0; t < 8; ++t) {
            auto s = __builtin_amdgcn_permlane32_swap(__builtin_bit_cast(u32, x[t]), __builtin_bit_cast(u32, x[t] * x[t]), false, false);
            const u32 s0 = s[0], s1 = s[1];
            a[t] = __builtin_bit_cast(float, s0) + __builtin_bit_cast(float, s1);
        }
#pragma unroll
        for (int t = 0; t < 4; ++t) {
            auto s = __builtin_amdgcn_permlane16_swap(__builtin_bit_cast(u32, a[t]), __builtin_bit_cast(u32, a[t + 4]), false, false);
            const u32 s0 = s[0], s1 = s[1];
            z[t] = __builtin_bit_cast(float, s0) + __builtin_bit_cast(float, s1);
        }
    }
    const float y0 = z[0] + dppf<0x128>(z[0]), y1 = z[1] + dppf<0x128>(z[1]);
    const float y2 = z[2] + dppf<0x128>(z[2]), y3 = z[3] + dppf<0x128>(z[3]);
    const float Y0 = (l & 8) ? y1 : y0, Y1 = (l & 8) ? y3 : y2;
    const float a0 = Y0 + dppf<0x141>(Y0), a1 = Y1 + dppf<0x141>(Y1);
    float Z = (l & 4) ? a1 : a0;
    Z += dppf<0xB1>(Z);
    Z += dppf<0x4E>(Z);
    const int val = ((l >> 3) & 1) + 2 * ((l >> 2) & 1) + 4 * (l >> 4);
    if ((l & 3) == 0) *(float*)(lds + redoff + val * 32 + w * 4) = Z;
    __syncthreads();
    const int t = l & 7;
    const v4f p0 = *(const v4f*)(lds + redoff + t * 32), p1 = *(const v4f*)(lds + redoff + t * 32 + 16);
    const v4f q0 = *(const v4f*)(lds + redoff + 256 + t * 32), q1 = *(const v4f*)(lds + redoff + 256 + t * 32 + 16);
    const float s1 = ((p0.x + p0.y) + (p0.z + p0.w)) + ((p1.x + p1.y) + (p1.z + p1.w));
    const float s2 = ((q0.x + q0.y) + (q0.z + q0.w)) + ((q1.x + q1.y) + (q1.z + q1.w));
    const float mean = s1 * (1.0f / D_);
    const float var = fmaxf(s2 * (1.0f / D_) - mean * mean, 0.0f);
    const float r = __builtin_amdgcn_rsqf(var + 1e-5f);
    const float m = mean * r;
#pragma unroll
    for (int j = 0; j < 8; ++j) {
        rs[j] = __builtin_bit_cast(float, __builtin_amdgcn_readlane(__builtin_bit_cast(int, r), j));
        mr[j] = __builtin_bit_cast(float, __builtin_amdgcn_readlane(__builtin_bit_cast(int, m), j));
    }
}

__device__ __forceinline__ void load_afrags(v4u (&A)[8][4], unsigned char* lds, int region, int ch, int h, int l) {
    const int b = l & 15, g = l >> 4, par = (1 + b) & 1;
    const unsigned char* base = lds + region + ch * KC_CH + par * KC_COPY + 256 * h + 16 * g + 2 * (1 + b - par);
#pragma unroll
    for (int p = 0; p < 8; ++p)
#pragma unroll
        for (int r = 0; r < 4; ++r) {
            const u32* q = (const u32*)(base + 512 * p + 64 * r);
            A[p][r] = (v4u){q[0], q[1], q[2], q[3]};
        }
}

__device__ __forceinline__ void cexp_pow(float al, float be, float m, float& re, float& im) {
    const float e = __builtin_amdgcn_exp2f(al * m);
    const float ph = __builtin_amdgcn_fractf(be * m);
    re = e * __builtin_amdgcn_cosf(ph);
    im = e * __builtin_amdgcn_sinf(ph);
}

__global__ void __launch_bounds__(512, 2)
h3s4_kernel(const u16* __restrict__ q_, const u16* __restrict__ k_, const u16* __restrict__ v_, const u16* __restrict__ shk_,
            const u16* __restrict__ ssmk_, const u16* __restrict__ ssmD_, const u16* __restrict__ Are_, const u16* __restrict__ Aim_,
            const u16* __restrict__ Bre_, const u16* __restrict__ Bim_, const u16* __restrict__ Cre_, const u16* __restrict__ Cim_,
            const u16* __restrict__ logdt_, const u16* __restrict__ Dskip_, u16* __restrict__ out_, unsigned char* ws_) {
    __shared__ __attribute__((aligned(16))) unsigned char lds[LDS_BYTES];
    const int i = threadIdx.x, l = i & 63;
    const int w = __builtin_amdgcn_readfirstlane(i >> 6);
    const int xcd = blockIdx.x & 7, idx = blockIdx.x >> 3, me = xcd * 32 + idx;
    const __amdgpu_buffer_rsrc_t rs = __builtin_amdgcn_make_buffer_rsrc(ws_, 0, WS_TOTAL, 0x00020000);
    const v4u poison4 = {POISON, POISON, POISON, POISON};
    STAMP(0);

    const int prow = i >> 3, cgrp = i & 7;
    const int R = 64 * idx + prow, pch = 64 * xcd + 8 * cgrp;
    v4u kk[4];
#pragma unroll
    for (int j = 0; j < 4; ++j) {
        const int Rj = R - j;
        kk[j] = *(const v4u*)(k_ + (Rj < 0 ? 0 : Rj) * D_ + pch);
    }
    const v4u vv = *(const v4u*)(v_ + R * D_ + pch);
    v4u ww[4];
#pragma unroll
    for (int j = 0; j < 4; ++j) ww[j] = *(const v4u*)(shk_ + pch * 4 + j * 8);
    const int sch = i >> 8, sm = i & 255;
    const v4u sk = *(const v4u*)(ssmk_ + (2 * me + sch) * S_ + sm * 8);
    const u32 sknext = *(const u32*)(ssmk_ + (2 * me + sch) * S_ + (sm < 255 ? sm * 8 + 8 : 0));
    u32 epoch = 0;
    if (w == 0) epoch = __builtin_amdgcn_raw_buffer_load_b32(rs, 0, WS_EPOCH, 16);
    float x[8];
#pragma unroll
    for (int t = 0; t < 8; ++t) x[t] = bf2f(q_[(8 * me + t) * D_ + i]);
    const float dcoA = bf2f(ssmD_[2 * me + (l & 1)]), dcoB = bf2f(Dskip_[2 * me + (l & 1)]);
    u16 pAre = 0, pAim = 0, pBre = 0, pBim = 0, pCre = 0, pCim = 0, pdt = 0;
    if (w >= 4) {
        const int j = i - 256, pi = (2 * me + (j >> 7)) * NS_ + (j & 127);
        pAre = Are_[pi]; pAim = Aim_[pi]; pBre = Bre_[pi]; pBim = Bim_[pi]; pCre = Cre_[pi]; pCim = Cim_[pi];
        pdt = logdt_[2 * me + (j >> 7)];
    } else {
#pragma unroll
        for (int j = 0; j < 2; ++j) {
            const int e = i + 256 * j;
            __builtin_amdgcn_raw_buffer_store_b128(poison4, rs, (e >> 3) * C_PITCH + me * 128 + (e & 7) * 16, WS_C, 16);
        }
    }

    if (i < 64) *(v4u*)(lds + L_UR + (i >> 5) * UR_PITCH + 4096 + (i & 31) * 16) = (v4u){0, 0, 0, 0};
    if (i < 2 * 648 / 4) *(v4u*)(lds + L_KT + i * 16) = (v4u){0, 0, 0, 0};
    if (i < 24) *(u32*)(lds + L_SYNC + i * 4) = 0;
    if (i >= 64 && i < 64 + 16) {
        const int e = i - 64;
        *(v4u*)(lds + L_KA + (e >> 1) * KC_COPY + (e & 1) * 16) = (v4u){0, 0, 0, 0};
    }

    STAMP(1);
    {
        u32 pk[4];
#pragma unroll
        for (int j = 0; j < 4; ++j) if (R - j < 0) kk[j] = (v4u){0, 0, 0, 0};
#pragma unroll
        for (int c2 = 0; c2 < 4; ++c2) {
            float res[2];
#pragma unroll
            for (int hh = 0; hh < 2; ++hh) {
                const u32 sel = hh ? 0x07060302u : 0x05040100u;
                const u32 p01 = __builtin_amdgcn_perm(kk[1][c2], kk[0][c2], sel);
                const u32 p23 = __builtin_amdgcn_perm(kk[3][c2], kk[2][c2], sel);
                const int c = 2 * c2 + hh;
                const u32 w01 = ww[c >> 1][(c & 1) * 2], w23 = ww[c >> 1][(c & 1) * 2 + 1];
                float a = __builtin_amdgcn_fdot2_f32_bf16(__builtin_bit_cast(v2bf, p01), __builtin_bit_cast(v2bf, w01), 0.0f, false);
                a = __builtin_amdgcn_fdot2_f32_bf16(__builtin_bit_cast(v2bf, p23), __builtin_bit_cast(v2bf, w23), a, false);
                res[hh] = a * (hh ? bf_hi(vv[c2]) : bf_lo(vv[c2]));
            }
            pk[c2] = pack_bf(res[0], res[1]);
        }
        const bool odd = prow & 1;
        const u32 r0 = dppu<0x128>(odd ? pk[0] : pk[2]), r1 = dppu<0x128>(odd ? pk[1] : pk[3]);
        const u32 m0 = odd ? pk[2] : pk[0], m1 = odd ? pk[3] : pk[1];
        const u32 selx = odd ? 0x05040100u : 0x01000504u, sely = odd ? 0x07060302u : 0x03020706u;
        u32 o[4];
        o[0] = __builtin_amdgcn_perm(r0, m0, selx);
        o[1] = __builtin_amdgcn_perm(r0, m0, sely);
        o[2] = __builtin_amdgcn_perm(r1, m1, selx);
        o[3] = __builtin_amdgcn_perm(r1, m1, sely);
        const int chunk = prow >> 3, pos = 3 - ((prow & 7) >> 1);
#pragma unroll
        for (int c = 0; c < 4; ++c) {
            const int ch = 8 * cgrp + 4 * (odd ? 1 : 0) + c;
            *(u32*)(lds + L_T + ch * 128 + ((chunk ^ ((ch >> 2) & 7)) * 16) + pos * 4) = o[c];
        }
    }
    {
        unsigned char* c0 = lds + L_KA + sch * KC_CH;
        const u32 nx = sm < 255 ? sknext : 0u;
        *(v4u*)(c0 + 32 + sm * 16) = sk;
        *(v4u*)(c0 + KC_COPY + 32 + sm * 16) =
            (v4u){__builtin_amdgcn_alignbit(sk.y, sk.x, 16), __builtin_amdgcn_alignbit(sk.z, sk.y, 16),
                  __builtin_amdgcn_alignbit(sk.w, sk.z, 16), __builtin_amdgcn_alignbit(nx, sk.w, 16)};
    }
    STAMP(2);
    __syncthreads();
    STAMP(3);
    if (i == 0) *(u32*)(lds + L_KA + KC_COPY + 28) = *(const u32*)(lds + L_KA + 32) << 16;
    if (i == 64) *(u32*)(lds + L_KA + KC_CH + KC_COPY + 28) = *(const u32*)(lds + L_KA + KC_CH + 32) << 16;

    v4u A[8][4];
    if (w < 4) {
#pragma unroll
        for (int j = 0; j < 2; ++j) {
            const int e = i + 256 * j, ch = e >> 3, chunk = e & 7;
            const v4u d = *(const v4u*)(lds + L_T + ch * 128 + ((chunk ^ ((ch >> 2) & 7)) * 16));
            __builtin_amdgcn_raw_buffer_store_b128(d, rs, (64 * xcd + ch) * KV_PITCH + (8 * idx + chunk) * 16, WS_KV, 16);
        }
        asm volatile("s_waitcnt vmcnt(0)" ::: "memory");
        STAMP(4);
        lds_signal_inc(lds, SY_KV, l);
        u32 tag = 0;
        if (w == 0) {
            tag = __builtin_amdgcn_readfirstlane(epoch) + 1u;
            lds_wait_ge(lds, SY_KV, 4);
            if (l == 0) __builtin_amdgcn_raw_buffer_store_b32(tag, rs, me * 4, WS_FLAGS, 16);
        }
#pragma unroll
        for (int j = 0; j < 2; ++j)
            __builtin_amdgcn_raw_buffer_store_b128(poison4, rs, me * U_PITCH + (i + 256 * j) * 16, WS_U, 16);
        lds_wait_ge(lds, SY_KV, 4);
        load_afrags(A, lds, L_KA, w >> 1, w & 1, l);
        STAMP(5);
        if (w == 0) {
            for (u32 it = 0;; ++it) {
                asm volatile("" ::: "memory");
                const v4u f = __builtin_amdgcn_raw_buffer_load_b128(rs, l * 16, WS_FLAGS, 16);
                if (__all(f.x == tag && f.y == tag && f.z == tag && f.w == tag)) break;
                if (it > GLB_SPIN_CAP) { lds_st(lds, SY_ABORT, 1u); break; }
            }
            if (me == 0 && l == 0) __builtin_amdgcn_raw_buffer_store_b32(tag, rs, 0, WS_EPOCH, 16);
            lds_signal_set(lds, SY_GO, 1u, l);
        } else {
            lds_wait_ge(lds, SY_GO, 1u);
        }
        STAMP(6);
        v4u g0, g1;
        {
            const u32 off = (2 * me + (l >> 5)) * KV_PITCH + (64 * w + (l & 31)) * 16;
            g0 = __builtin_amdgcn_raw_buffer_load_b128(rs, off, WS_KV, 16);
            g1 = __builtin_amdgcn_raw_buffer_load_b128(rs, off + 512, WS_KV, 16);
        }
        unsigned char* dst = lds + L_UR + (l >> 5) * UR_PITCH + 2 * (2040 - 8 * (64 * w + (l & 31)));
        *(v4u*)dst = g0;
        *(v4u*)(dst - 512) = g1;
        asm volatile("s_waitcnt vmcnt(0) lgkmcnt(0)" ::: "memory");
        if (l == 0) { lds_st(lds, SY_INTILE + 8 * w, 1u); lds_st(lds, SY_INTILE + 8 * w + 4, 1u); }
    } else {
        const int wa = w - 4, ch = wa >> 1, nh = wa & 1;
        {
            const float ex = expf(bf2f(pdt));
            const float dt = bf_lo(pack_bf(ex, ex));
            const float zr = dt * bf2f(pAre), zi = dt * bf2f(pAim);
            const float br = dt * bf2f(pBre), bi = dt * bf2f(pBim);
            const float cr = bf2f(pCre), ci = bf2f(pCim);
            *(v4f*)(lds + L_PT + (i - 256) * 16) =
                (v4f){zr * 1.44269504088896341f, zi * 0.15915494309189535f, cr * br - ci * bi, cr * bi + ci * br};
        }
        asm volatile("s_waitcnt lgkmcnt(0)" ::: "memory");
        const int row = l & 15, g = l >> 4;
        const float m0 = (float)(128 * (row >> 3) + (row & 7));
        const float m1 = (float)(row < 8 ? 256 + 128 * (row >> 2) + 2 * (row & 3) : 512 + 128 * ((row - 8) >> 1) + 4 * (row & 1));
        const float m2 = (float)(row < 8 ? 1024 + 128 * row : (row == 8 ? 2048 : 0));
        const float mb = (float)(8 * row);
        v4f acc0 = {0, 0, 0, 0}, acc1 = {0, 0, 0, 0}, acc2 = {0, 0, 0, 0};
#pragma unroll 2
        for (int s = 0; s < 16; ++s) {
            const v4f P = *(const v4f*)(lds + L_PT + (ch * 128 + nh * 64 + 4 * s + g) * 16);
            float er, ei, ar, ai;
            cexp_pow(P.x, P.y, mb, er, ei);
            const float Br = er * P.z - ei * P.w, Bn = -(er * P.w + ei * P.z);
            cexp_pow(P.x, P.y, m0, ar, ai);
            acc0 = __builtin_amdgcn_mfma_f32_16x16x4f32(ar, Br, acc0, 0, 0, 0);
            acc0 = __builtin_amdgcn_mfma_f32_16x16x4f32(ai, Bn, acc0, 0, 0, 0);
            cexp_pow(P.x, P.y, m1, ar, ai);
            acc1 = __builtin_amdgcn_mfma_f32_16x16x4f32(ar, Br, acc1, 0, 0, 0);
            acc1 = __builtin_amdgcn_mfma_f32_16x16x4f32(ai, Bn, acc1, 0, 0, 0);
            cexp_pow(P.x, P.y, m2, ar, ai);
            acc2 = __builtin_amdgcn_mfma_f32_16x16x4f32(ar, Br, acc2, 0, 0, 0);
            acc2 = __builtin_amdgcn_mfma_f32_16x16x4f32(ai, Bn, acc2, 0, 0, 0);
        }
        STAMP(4);
        float* kt = (float*)(lds + L_KT + ch * KT_CH);
        const int col = l & 15;
#pragma unroll
        for (int r = 0; r < 4; ++r) {
            const int rw = 4 * g + r;
            const int mu0 = 128 * (rw >> 3) + (rw & 7) + 8 * col;
            const int mu1 = rw < 8 ? 256 + 64 * (rw >> 2) + (rw & 3) + 4 * col : 384 + 32 * ((rw - 8) >> 1) + (rw & 1) + 2 * col;
            const int mu2 = rw < 8 ? 512 + 16 * rw + col : ((rw == 8 && col == 0) ? 640 : 644);
            (void)__hip_atomic_fetch_add(kt + mu0, acc0[r], __ATOMIC_RELAXED, __HIP_MEMORY_SCOPE_WORKGROUP);
            (void)__hip_atomic_fetch_add(kt + mu1, acc1[r], __ATOMIC_RELAXED, __HIP_MEMORY_SCOPE_WORKGROUP);
            (void)__hip_atomic_fetch_add(kt + mu2, acc2[r], __ATOMIC_RELAXED, __HIP_MEMORY_SCOPE_WORKGROUP);
        }
        lds_signal_inc(lds, SY_KT, l);
        if (wa < 2) {
            lds_wait_ge(lds, SY_KT, 4);
#pragma unroll 1
            for (int it = 0; it < 4; ++it) {
                const int c = it >> 1, m = (it & 1) * 128 + wa * 64 + l;
                const float* t = (const float*)(lds + L_KT + c * KT_CH);
                float kvv[9];
#pragma unroll
                for (int j = 0; j < 9; ++j) {
                    const int lag = 8 * m + j;
                    const u32 bb = pack_bf((float)lag, 0.0f) & 0xFFFFu;
                    const int mu = lag <= 256 ? lag : (int)(bb - 0x4380u + 256u);
                    kvv[j] = t[mu];
                }
                const u32 e0 = pack_bf(kvv[0], kvv[1]), e1 = pack_bf(kvv[2], kvv[3]), e2 = pack_bf(kvv[4], kvv[5]), e3 = pack_bf(kvv[6], kvv[7]);
                const u32 o0 = pack_bf(kvv[1], kvv[2]), o1 = pack_bf(kvv[3], kvv[4]), o2 = pack_bf(kvv[5], kvv[6]), o3 = pack_bf(kvv[7], kvv[8]);
                unsigned char* c0 = lds + L_KB + c * KC_CH;
                *(v4u*)(c0 + 32 + m * 16) = (v4u){e0, e1, e2, e3};
                *(v4u*)(c0 + KC_COPY + 32 + m * 16) = (v4u){o0, o1, o2, o3};
                if (m == 0) *(u32*)(c0 + KC_COPY + 28) = e0 << 16;
            }
            lds_signal_inc(lds, SY_KREADY, l);
            STAMP(5);
        }
    }

    float ya[8];
#pragma unroll
    for (int t = 0; t < 8; ++t) ya[t] = 0.0f;
    int redsel = 0;

#pragma unroll 1
    for (int k = 0; k < 3; ++k) {
        STAMP(7 + 8 * k);
        if (w < 4) {
            const int ch = w >> 1, h = w & 1, a = l & 15, g = l >> 4;
            const unsigned char* ub = lds + L_UR + ch * UR_PITCH + 2 * (2032 + 128 * h - 16 * a + 8 * g) - 3584;
            unsigned char* pb = lds + L_PART + ch * PART_CH + h * 1024 + 64 * a + 16 * g;
            v4f acc[8];
#pragma unroll
            for (int c = 0; c < 8; ++c) acc[c] = (v4f){0, 0, 0, 0};
#pragma unroll
            for (int q = 0; q < 8; ++q) {
                lds_wait_ge(lds, SY_INTILE + 4 * q, (u32)(k + 1));
                if (q == 0) STAMP(8 + 8 * k);
                if (q == 7) STAMP(9 + 8 * k);
                v4u B[4];
#pragma unroll
                for (int r = 0; r < 4; ++r) B[r] = *(const v4u*)(ub + 512 * (7 - q) + 64 * r);
#pragma unroll
                for (int r = 0; r < 4; ++r)
                    acc[q] = __builtin_amdgcn_mfma_f32_16x16x32_bf16(__builtin_bit_cast(v8bf, A[0][r]), __builtin_bit_cast(v8bf, B[r]), acc[q], 0, 0, 0);
                *(v4f*)(pb + q * PART_TILE) = acc[q];
                lds_signal_inc(lds, SY_PART + 4 * q, l);
#pragma unroll
                for (int p = 1; p < 8 - q; ++p)
#pragma unroll
                    for (int r = 0; r < 4; ++r)
                        acc[p + q] = __builtin_amdgcn_mfma_f32_16x16x32_bf16(__builtin_bit_cast(v8bf, A[p][r]), __builtin_bit_cast(v8bf, B[r]), acc[p + q], 0, 0, 0);
            }
            if (k == 0) {
                lds_wait_ge(lds, SY_KREADY, 2);
                load_afrags(A, lds, L_KB, ch, h, l);
            }
        } else if (w < 6) {
            if (k > 0) {
#pragma unroll 1
                for (int q = w & 1; q < 8; q += 2) {
                    const int r = 32 * q + (l >> 1);
                    const v4u d = poll16(rs, r * U_PITCH + (2 * me + (l & 1)) * 16, WS_U + (k - 1) * U_SIZE, lds);
                    *(v4u*)(lds + L_UR + (l & 1) * UR_PITCH + 2 * (2040 - 8 * r)) = d;
                    lds_signal_set(lds, SY_INTILE + 4 * q, (u32)(k + 1), l);
                    if (q < 2) STAMP(8 + 8 * k);
                    if (q >= 6) STAMP(9 + 8 * k);
                }
            }
        } else {
            const int ch = l & 1, tl = 32 * (l >> 3) + 8 * ((l >> 1) & 3);
            const float dco = k == 0 ? dcoA : dcoB;
#pragma unroll 1
            for (int c = w & 1; c < 8; c += 2) {
                lds_wait_ge(lds, SY_PART + 4 * c, (u32)(4 * (k + 1)));
                const unsigned char* pp = lds + L_PART + c * PART_TILE + ch * PART_CH + tl * 4;
                const v4f a0 = *(const v4f*)pp, a1 = *(const v4f*)(pp + 16);
                const v4f b0 = *(const v4f*)(pp + 1024), b1 = *(const v4f*)(pp + 1040);
                const v4u u = *(const v4u*)(lds + L_UR + ch * UR_PITCH + 2 * (2040 - (256 * c + tl)));
                v4u o;
                o.x = no_poison(pack_bf(a0.x + b0.x + dco * bf_hi(u.w), a0.y + b0.y + dco * bf_lo(u.w)));
                o.y = no_poison(pack_bf(a0.z + b0.z + dco * bf_hi(u.z), a0.w + b0.w + dco * bf_lo(u.z)));
                o.z = no_poison(pack_bf(a1.x + b1.x + dco * bf_hi(u.y), a1.y + b1.y + dco * bf_lo(u.y)));
                o.w = no_poison(pack_bf(a1.z + b1.z + dco * bf_hi(u.x), a1.w + b1.w + dco * bf_lo(u.x)));
                __builtin_amdgcn_raw_buffer_store_b128(o, rs, (8 * c + (l >> 3)) * C_PITCH + me * 128 + (l & 7) * 16, WS_C + k * C_SIZE, 16);
                if (c < 2) STAMP(8 + 8 * k);
                if (c >= 6) STAMP(9 + 8 * k);
            }
        }

        STAMP(10 + 8 * k);
        float mr[8], rsd[8], n0[8];
        if (k == 0) {
            __builtin_amdgcn_raw_buffer_store_b128(poison4, rs, (i >> 3) * C_PITCH + me * 128 + (i & 7) * 16, WS_C + C_SIZE, 16);
            __builtin_amdgcn_raw_buffer_store_b128(poison4, rs, me * U_PITCH + i * 16, WS_U + U_SIZE, 16);
            row_stats(x, lds, L_RED + redsel * 512, w, l, mr, rsd);
            redsel ^= 1;
#pragma unroll
            for (int t = 0; t < 8; ++t) n0[t] = x[t] * rsd[t] - mr[t];
        } else if (k == 1) {
            __builtin_amdgcn_raw_buffer_store_b128(poison4, rs, (i >> 3) * C_PITCH + me * 128 + (i & 7) * 16, WS_C + 2 * C_SIZE, 16);
        }
        STAMP(11 + 8 * k);
        const v4u cg = poll16(rs, (me >> 2) * C_PITCH + (i >> 1) * 128 + (me & 3) * 32 + (i & 1) * 16, WS_C + k * C_SIZE, lds);
        STAMP(12 + 8 * k);
        float c[8] = {bf_lo(cg.x), bf_hi(cg.x), bf_lo(cg.y), bf_hi(cg.y), bf_lo(cg.z), bf_hi(cg.z), bf_lo(cg.w), bf_hi(cg.w)};
        if (k == 2) {
#pragma unroll
            for (int t = 0; t < 8; t += 2) {
                const u32 o = pack_bf(x[t] + c[t], x[t + 1] + c[t + 1]);
                out_[(8 * me + t) * D_ + i] = (u16)o;
                out_[(8 * me + t + 1) * D_ + i] = (u16)(o >> 16);
            }
            break;
        }
        if (k == 0) {
#pragma unroll
            for (int t = 0; t < 8; ++t) { ya[t] = c[t]; x[t] += n0[t] * c[t]; }
        } else {
#pragma unroll
            for (int t = 0; t < 8; ++t) x[t] += c[t];
            row_stats(x, lds, L_RED + redsel * 512, w, l, mr, rsd);
            redsel ^= 1;
#pragma unroll
            for (int t = 0; t < 8; ++t) x[t] += (x[t] * rsd[t] - mr[t]) * ya[t];
        }
        asm volatile("s_waitcnt vmcnt(0)" ::: "memory");
        row_stats(x, lds, L_RED + redsel * 512, w, l, mr, rsd);
        redsel ^= 1;
        v4u uo;
        uo.x = no_poison(pack_bf(x[7] * rsd[7] - mr[7], x[6] * rsd[6] - mr[6]));
        uo.y = no_poison(pack_bf(x[5] * rsd[5] - mr[5], x[4] * rsd[4] - mr[4]));
        uo.z = no_poison(pack_bf(x[3] * rsd[3] - mr[3], x[2] * rsd[2] - mr[2]));
        uo.w = no_poison(pack_bf(x[1] * rsd[1] - mr[1], x[0] * rsd[0] - mr[0]));
        __builtin_amdgcn_raw_buffer_store_b128(uo, rs, me * U_PITCH + i * 16, WS_U + k * U_SIZE, 16);
        STAMP(13 + 8 * k);
    }
}

extern "C" void kernel_launch(void* const* d_in, const int* in_sizes, int n_in, void* d_out, int out_size, void* d_ws, size_t ws_size,
                              hipStream_t stream) {
    (void)in_sizes; (void)n_in; (void)out_size; (void)ws_size;
    h3s4_kernel<<<256, 512, 0, stream>>>(
        (const u16*)d_in[0], (const u16*)d_in[1], (const u16*)d_in[2], (const u16*)d_in[3], (const u16*)d_in[4], (const u16*)d_in[5],
        (const u16*)d_in[7], (const u16*)d_in[8], (const u16*)d_in[9], (const u16*)d_in[10], (const u16*)d_in[11], (const u16*)d_in[12],
        (const u16*)d_in[13], (const u16*)d_in[14], (u16*)d_out, (unsigned char*)d_ws);
}
```

```cpp
#include <hip/hip_runtime.h>
#include <stdint.h>
#include <stdio.h>
#define STAMPS 1

typedef unsigned short u16;
typedef unsigned u32;
typedef u32 v4u __attribute__((ext_vector_type(4)));
typedef float v2f __attribute__((ext_vector_type(2)));
typedef float v4f __attribute__((ext_vector_type(4)));
typedef __bf16 v2bf __attribute__((ext_vector_type(2)));
typedef __bf16 v8bf __attribute__((ext_vector_type(8)));

constexpr int S_ = 2048, D_ = 512, NS_ = 128;
constexpr u32 POISON = 0xFFFFFFFFu;

constexpr u32 WS_EPOCH = 0;
constexpr u32 WS_FLAGS = 4096;
constexpr u32 KV_PITCH = 4096 + 128;
constexpr u32 WS_KV = 8192;
constexpr u32 C_PITCH = 32768 + 128;
constexpr u32 C_SIZE = 64 * C_PITCH;
constexpr u32 WS_C = WS_KV + 512 * KV_PITCH;
constexpr u32 U_PITCH = 8192 + 128;
constexpr u32 U_SIZE = 256 * U_PITCH;
constexpr u32 WS_U = WS_C + 3 * C_SIZE;
constexpr u32 WS_TOTAL = WS_U + 2 * U_SIZE;

constexpr int UR_PITCH = 4608;
constexpr int L_UR = 0;
constexpr int PART_CH = 2048 + 16;
constexpr int PART_TILE = 2 * PART_CH;
constexpr int L_PART = L_UR + 2 * UR_PITCH;
constexpr int KC_COPY = 4160;
constexpr int KC_CH = 2 * KC_COPY;
constexpr int L_KA = L_PART + 8 * PART_TILE;
constexpr int L_KB = L_KA + 2 * KC_CH;
constexpr int KT_CH = 648 * 4;
constexpr int L_KT = L_KB + 2 * KC_CH;
constexpr int L_PT = L_KT + 2 * KT_CH;
constexpr int L_T = L_PT + 4096;
constexpr int L_RED = L_T + 8192;
constexpr int L_SYNC = L_RED + 2 * 512;
constexpr int SY_INTILE = L_SYNC;
constexpr int SY_PART = L_SYNC + 32;
constexpr int SY_KV = L_SYNC + 64;
constexpr int SY_GO = L_SYNC + 68;
constexpr int SY_KT = L_SYNC + 72;
constexpr int SY_KREADY = L_SYNC + 76;
constexpr int SY_ABORT = L_SYNC + 80;
constexpr int LDS_BYTES = L_SYNC + 96;
static_assert(LDS_BYTES > 82 * 1024 && LDS_BYTES <= 160 * 1024, "one workgroup per CU");

constexpr u32 LDS_SPIN_CAP = 1u << 20;
constexpr u32 GLB_SPIN_CAP = 1u << 16;

__device__ __forceinline__ float bf_lo(u32 x) { return __builtin_bit_cast(float, x << 16); }
__device__ __forceinline__ float bf_hi(u32 x) { return __builtin_bit_cast(float, x & 0xFFFF0000u); }
__device__ __forceinline__ float bf2f(u16 x) { return __builtin_bit_cast(float, ((u32)x) << 16); }
__device__ __forceinline__ u32 pack_bf(float lo, float hi) {
    v2f f = {lo, hi};
    return __builtin_bit_cast(u32, __builtin_convertvector(f, v2bf));
}
__device__ __forceinline__ u32 no_poison(u32 x) { return x < 0xFFFFFFFEu ? x : 0xFFFFFFFEu; }
template <int CTRL> __device__ __forceinline__ float dppf(float x) {
    return __builtin_bit_cast(float, __builtin_amdgcn_mov_dpp(__builtin_bit_cast(int, x), CTRL, 0xf, 0xf, true));
}
template <int CTRL> __device__ __forceinline__ u32 dppu(u32 x) { return (u32)__builtin_amdgcn_mov_dpp((int)x, CTRL, 0xf, 0xf, true); }

#define STAMP(n) do { unsigned long long t_; asm volatile("s_memrealtime %0\n\ts_waitcnt lgkmcnt(0)" : "=s"(t_) :: "memory"); stv = (l == (n)) ? (u32)t_ : stv; } while (0)
__device__ __forceinline__ u32 lds_ld(unsigned char* lds, int off) {
    return __hip_atomic_load((u32*)(lds + off), __ATOMIC_RELAXED, __HIP_MEMORY_SCOPE_WORKGROUP);
}
__device__ __forceinline__ void lds_st(unsigned char* lds, int off, u32 v) {
    __hip_atomic_store((u32*)(lds + off), v, __ATOMIC_RELAXED, __HIP_MEMORY_SCOPE_WORKGROUP);
}
__device__ __forceinline__ void lds_inc(unsigned char* lds, int off) {
    (void)__hip_atomic_fetch_add((u32*)(lds + off), 1u, __ATOMIC_RELAXED, __HIP_MEMORY_SCOPE_WORKGROUP);
}
__device__ __forceinline__ void lds_signal_inc(unsigned char* lds, int off, int lane) {
    asm volatile("s_waitcnt lgkmcnt(0)" ::: "memory");
    if (lane == 0) lds_inc(lds, off);
}
__device__ __forceinline__ void lds_signal_set(unsigned char* lds, int off, u32 v, int lane) {
    asm volatile("s_waitcnt lgkmcnt(0)" ::: "memory");
    if (lane == 0) lds_st(lds, off, v);
}
__device__ __forceinline__ void lds_wait_ge(unsigned char* lds, int off, u32 target) {
    for (u32 it = 0;; ++it) {
        const u32 v = __builtin_amdgcn_readfirstlane(lds_ld(lds, off));
        if (v >= target) break;
        if ((it & 63u) == 63u) {
            if (__builtin_amdgcn_readfirstlane(lds_ld(lds, SY_ABORT))) break;
            if (it > LDS_SPIN_CAP) { lds_st(lds, SY_ABORT, 1u); break; }
        }
        __builtin_amdgcn_s_sleep(1);
    }
    asm volatile("" ::: "memory");
}
__device__ __forceinline__ bool valid16(v4u r) {
    u32 m = r.x > r.y ? r.x : r.y;
    m = m > r.z ? m : r.z;
    m = m > r.w ? m : r.w;
    return m != POISON;
}
__device__ __forceinline__ v4u poll16(__amdgpu_buffer_rsrc_t rs, u32 voff, u32 soff, unsigned char* lds) {
    v4u r = __builtin_amdgcn_raw_buffer_load_b128(rs, voff, soff, 16);
    bool ok = valid16(r);
    for (u32 it = 0; !__all(ok); ++it) {
        if ((it & 15u) == 15u) {
            if (__builtin_amdgcn_readfirstlane(lds_ld(lds, SY_ABORT))) break;
            if (it > GLB_SPIN_CAP) { lds_st(lds, SY_ABORT, 1u); break; }
        }
        asm volatile("" ::: "memory");
        if (!ok) { r = __builtin_amdgcn_raw_buffer_load_b128(rs, voff, soff, 16); ok = valid16(r); }
    }
    return r;
}

__device__ __forceinline__ void row_stats(const float (&x)[8], unsigned char* lds, int redoff, int w, int l, float (&mr)[8], float (&rs)[8]) {
    float z[4];
    {
        float a[8];
#pragma unroll
        for (int t = 0; t < 8; ++t) {
            auto s = __builtin_amdgcn_permlane32_swap(__builtin_bit_cast(u32, x[t]), __builtin_bit_cast(u32, x[t] * x[t]), false, false);
            const u32 s0 = s[0], s1 = s[1];
            a[t] = __builtin_bit_cast(float, s0) + __builtin_bit_cast(float, s1);
        }
#pragma unroll
        for (int t = 0; t < 4; ++t) {
            auto s = __builtin_amdgcn_permlane16_swap(__builtin_bit_cast(u32, a[t]), __builtin_bit_cast(u32, a[t + 4]), false, false);
            const u32 s0 = s[0], s1 = s[1];
            z[t] = __builtin_bit_cast(float, s0) + __builtin_bit_cast(float, s1);
        }
    }
    const float y0 = z[0] + dppf<0x128>(z[0]), y1 = z[1] + dppf<0x128>(z[1]);
    const float y2 = z[2] + dppf<0x128>(z[2]), y3 = z[3] + dppf<0x128>(z[3]);
    const float Y0 = (l & 8) ? y1 : y0, Y1 = (l & 8) ? y3 : y2;
    const float a0 = Y0 + dppf<0x141>(Y0), a1 = Y1 + dppf<0x141>(Y1);
    float Z = (l & 4) ? a1 : a0;
    Z += dppf<0xB1>(Z);
    Z += dppf<0x4E>(Z);
    const int val = ((l >> 3) & 1) + 2 * ((l >> 2) & 1) + 4 * (l >> 4);
    if ((l & 3) == 0) *(float*)(lds + redoff + val * 32 + w * 4) = Z;
    __syncthreads();
    const int t = l & 7;
    const v4f p0 = *(const v4f*)(lds + redoff + t * 32), p1 = *(const v4f*)(lds + redoff + t * 32 + 16);
    const v4f q0 = *(const v4f*)(lds + redoff + 256 + t * 32), q1 = *(const v4f*)(lds + redoff + 256 + t * 32 + 16);
    const float s1 = ((p0.x + p0.y) + (p0.z + p0.w)) + ((p1.x + p1.y) + (p1.z + p1.w));
    const float s2 = ((q0.x + q0.y) + (q0.z + q0.w)) + ((q1.x + q1.y) + (q1.z + q1.w));
    const float mean = s1 * (1.0f / D_);
    const float var = fmaxf(s2 * (1.0f / D_) - mean * mean, 0.0f);
    const float r = __builtin_amdgcn_rsqf(var + 1e-5f);
    const float m = mean * r;
#pragma unroll
    for (int j = 0; j < 8; ++j) {
        rs[j] = __builtin_bit_cast(float, __builtin_amdgcn_readlane(__builtin_bit_cast(int, r), j));
        mr[j] = __builtin_bit_cast(float, __builtin_amdgcn_readlane(__builtin_bit_cast(int, m), j));
    }
}

__device__ __forceinline__ void load_afrags(v4u (&A)[8][4], unsigned char* lds, int region, int ch, int h, int l) {
    const int b = l & 15, g = l >> 4, par = (1 + b) & 1;
    const unsigned char* base = lds + region + ch * KC_CH + par * KC_COPY + 256 * h + 16 * g + 2 * (1 + b - par);
#pragma unroll
    for (int p = 0; p < 8; ++p)
#pragma unroll
        for (int r = 0; r < 4; ++r) {
            const u32* q = (const u32*)(base + 512 * p + 64 * r);
            A[p][r] = (v4u){q[0], q[1], q[2], q[3]};
        }
}

__device__ __forceinline__ void cexp_pow(float al, float be, float m, float& re, float& im) {
    const float e = __builtin_amdgcn_exp2f(al * m);
    const float ph = __builtin_amdgcn_fractf(be * m);
    re = e * __builtin_amdgcn_cosf(ph);
    im = e * __builtin_amdgcn_sinf(ph);
}

__global__ void __launch_bounds__(512, 2)
h3s4_kernel(const u16* __restrict__ q_, const u16* __restrict__ k_, const u16* __restrict__ v_, const u16* __restrict__ shk_,
            const u16* __restrict__ ssmk_, const u16* __restrict__ ssmD_, const u16* __restrict__ Are_, const u16* __restrict__ Aim_,
            const u16* __restrict__ Bre_, const u16* __restrict__ Bim_, const u16* __restrict__ Cre_, const u16* __restrict__ Cim_,
            const u16* __restrict__ logdt_, const u16* __restrict__ Dskip_, u16* __restrict__ out_, unsigned char* ws_) {
    __shared__ __attribute__((aligned(16))) unsigned char lds[LDS_BYTES];
    const int i = threadIdx.x, l = i & 63;
    const int w = __builtin_amdgcn_readfirstlane(i >> 6);
    const int xcd = blockIdx.x & 7, idx = blockIdx.x >> 3, me = xcd * 32 + idx;
    const __amdgpu_buffer_rsrc_t rs = __builtin_amdgcn_make_buffer_rsrc(ws_, 0, WS_TOTAL, 0x00020000);
    const v4u poison4 = {POISON, POISON, POISON, POISON};
    u32 stv = 0;
    STAMP(0);

    const int prow = i >> 3, cgrp = i & 7;
    const int R = 64 * idx + prow, pch = 64 * xcd + 8 * cgrp;
    v4u kk[4];
#pragma unroll
    for (int j = 0; j < 4; ++j) {
        const int Rj = R - j;
        kk[j] = *(const v4u*)(k_ + (Rj < 0 ? 0 : Rj) * D_ + pch);
    }
    const v4u vv = *(const v4u*)(v_ + R * D_ + pch);
    v4u ww[4];
#pragma unroll
    for (int j = 0; j < 4; ++j) ww[j] = *(const v4u*)(shk_ + pch * 4 + j * 8);
    const int sch = i >> 8, sm = i & 255;
    const v4u sk = *(const v4u*)(ssmk_ + (2 * me + sch) * S_ + sm * 8);
    const u32 sknext = *(const u32*)(ssmk_ + (2 * me + sch) * S_ + (sm < 255 ? sm * 8 + 8 : 0));
    u32 epoch = 0;
    if (w == 0) epoch = __builtin_amdgcn_raw_buffer_load_b32(rs, 0, WS_EPOCH, 16);
    float x[8];
#pragma unroll
    for (int t = 0; t < 8; ++t) x[t] = bf2f(q_[(8 * me + t) * D_ + i]);
    const float dcoA = bf2f(ssmD_[2 * me + (l & 1)]), dcoB = bf2f(Dskip_[2 * me + (l & 1)]);
    u16 pAre = 0, pAim = 0, pBre = 0, pBim = 0, pCre = 0, pCim = 0, pdt = 0;
    if (w >= 4) {
        const int j = i - 256, pi = (2 * me + (j >> 7)) * NS_ + (j & 127);
        pAre = Are_[pi]; pAim = Aim_[pi]; pBre = Bre_[pi]; pBim = Bim_[pi]; pCre = Cre_[pi]; pCim = Cim_[pi];
        pdt = logdt_[2 * me + (j >> 7)];
    } else {
#pragma unroll
        for (int j = 0; j < 2; ++j) {
            const int e = i + 256 * j;
            __builtin_amdgcn_raw_buffer_store_b128(poison4, rs, (e >> 3) * C_PITCH + me * 128 + (e & 7) * 16, WS_C, 16);
        }
    }

    if (i < 64) *(v4u*)(lds + L_UR + (i >> 5) * UR_PITCH + 4096 + (i & 31) * 16) = (v4u){0, 0, 0, 0};
    if (i < 2 * 648 / 4) *(v4u*)(lds + L_KT + i * 16) = (v4u){0, 0, 0, 0};
    if (i < 24) *(u32*)(lds + L_SYNC + i * 4) = 0;
    if (i >= 64 && i < 64 + 16) {
        const int e = i - 64;
        *(v4u*)(lds + L_KA + (e >> 1) * KC_COPY + (e & 1) * 16) = (v4u){0, 0, 0, 0};
    }

    STAMP(1);
    {
        u32 pk[4];
#pragma unroll
        for (int j = 0; j < 4; ++j) if (R - j < 0) kk[j] = (v4u){0, 0, 0, 0};
#pragma unroll
        for (int c2 = 0; c2 < 4; ++c2) {
            float res[2];
#pragma unroll
            for (int hh = 0; hh < 2; ++hh) {
                const u32 sel = hh ? 0x07060302u : 0x05040100u;
                const u32 p01 = __builtin_amdgcn_perm(kk[1][c2], kk[0][c2], sel);
                const u32 p23 = __builtin_amdgcn_perm(kk[3][c2], kk[2][c2], sel);
                const int c = 2 * c2 + hh;
                const u32 w01 = ww[c >> 1][(c & 1) * 2], w23 = ww[c >> 1][(c & 1) * 2 + 1];
                float a = __builtin_amdgcn_fdot2_f32_bf16(__builtin_bit_cast(v2bf, p01), __builtin_bit_cast(v2bf, w01), 0.0f, false);
                a = __builtin_amdgcn_fdot2_f32_bf16(__builtin_bit_cast(v2bf, p23), __builtin_bit_cast(v2bf, w23), a, false);
                res[hh] = a * (hh ? bf_hi(vv[c2]) : bf_lo(vv[c2]));
            }
            pk[c2] = pack_bf(res[0], res[1]);
        }
        const bool odd = prow & 1;
        const u32 r0 = dppu<0x128>(odd ? pk[0] : pk[2]), r1 = dppu<0x128>(odd ? pk[1] : pk[3]);
        const u32 m0 = odd ? pk[2] : pk[0], m1 = odd ? pk[3] : pk[1];
        const u32 selx = odd ? 0x05040100u : 0x01000504u, sely = odd ? 0x07060302u : 0x03020706u;
        u32 o[4];
        o[0] = __builtin_amdgcn_perm(r0, m0, selx);
        o[1] = __builtin_amdgcn_perm(r0, m0, sely);
        o[2] = __builtin_amdgcn_perm(r1, m1, selx);
        o[3] = __builtin_amdgcn_perm(r1, m1, sely);
        const int chunk = prow >> 3, pos = 3 - ((prow & 7) >> 1);
#pragma unroll
        for (int c = 0; c < 4; ++c) {
            const int ch = 8 * cgrp + 4 * (odd ? 1 : 0) + c;
            *(u32*)(lds + L_T + ch * 128 + ((chunk ^ ((ch >> 2) & 7)) * 16) + pos * 4) = o[c];
        }
    }
    {
        unsigned char* c0 = lds + L_KA + sch * KC_CH;
        const u32 nx = sm < 255 ? sknext : 0u;
        *(v4u*)(c0 + 32 + sm * 16) = sk;
        *(v4u*)(c0 + KC_COPY + 32 + sm * 16) =
            (v4u){__builtin_amdgcn_alignbit(sk.y, sk.x, 16), __builtin_amdgcn_alignbit(sk.z, sk.y, 16),
                  __builtin_amdgcn_alignbit(sk.w, sk.z, 16), __builtin_amdgcn_alignbit(nx, sk.w, 16)};
    }
    STAMP(2);
    __syncthreads();
    STAMP(3);
    if (i == 0) *(u32*)(lds + L_KA + KC_COPY + 28) = *(const u32*)(lds + L_KA + 32) << 16;
    if (i == 64) *(u32*)(lds + L_KA + KC_CH + KC_COPY + 28) = *(const u32*)(lds + L_KA + KC_CH + 32) << 16;

    v4u A[8][4];
    if (w < 4) {
#pragma unroll
        for (int j = 0; j < 2; ++j) {
            const int e = i + 256 * j, ch = e >> 3, chunk = e & 7;
            const v4u d = *(const v4u*)(lds + L_T + ch * 128 + ((chunk ^ ((ch >> 2) & 7)) * 16));
            __builtin_amdgcn_raw_buffer_store_b128(d, rs, (64 * xcd + ch) * KV_PITCH + (8 * idx + chunk) * 16, WS_KV, 16);
        }
        asm volatile("s_waitcnt vmcnt(0)" ::: "memory");
        STAMP(4);
        lds_signal_inc(lds, SY_KV, l);
        u32 tag = 0;
        if (w == 0) {
            tag = __builtin_amdgcn_readfirstlane(epoch) + 1u;
            lds_wait_ge(lds, SY_KV, 4);
            if (l == 0) __builtin_amdgcn_raw_buffer_store_b32(tag, rs, me * 4, WS_FLAGS, 16);
        }
#pragma unroll
        for (int j = 0; j < 2; ++j)
            __builtin_amdgcn_raw_buffer_store_b128(poison4, rs, me * U_PITCH + (i + 256 * j) * 16, WS_U, 16);
        lds_wait_ge(lds, SY_KV, 4);
        load_afrags(A, lds, L_KA, w >> 1, w & 1, l);
        STAMP(5);
        if (w == 0) {
            for (u32 it = 0;; ++it) {
                asm volatile("" ::: "memory");
                const v4u f = __builtin_amdgcn_raw_buffer_load_b128(rs, l * 16, WS_FLAGS, 16);
                if (__all(f.x == tag && f.y == tag && f.z == tag && f.w == tag)) break;
                if (it > GLB_SPIN_CAP) { lds_st(lds, SY_ABORT, 1u); break; }
            }
            if (me == 0 && l == 0) __builtin_amdgcn_raw_buffer_store_b32(tag, rs, 0, WS_EPOCH, 16);
            lds_signal_set(lds, SY_GO, 1u, l);
        } else {
            lds_wait_ge(lds, SY_GO, 1u);
        }
        STAMP(6);
        v4u g0, g1;
        {
            const u32 off = (2 * me + (l >> 5)) * KV_PITCH + (64 * w + (l & 31)) * 16;
            g0 = __builtin_amdgcn_raw_buffer_load_b128(rs, off, WS_KV, 16);
            g1 = __builtin_amdgcn_raw_buffer_load_b128(rs, off + 512, WS_KV, 16);
        }
        unsigned char* dst = lds + L_UR + (l >> 5) * UR_PITCH + 2 * (2040 - 8 * (64 * w + (l & 31)));
        *(v4u*)dst = g0;
        *(v4u*)(dst - 512) = g1;
        asm volatile("s_waitcnt vmcnt(0) lgkmcnt(0)" ::: "memory");
        if (l == 0) { lds_st(lds, SY_INTILE + 8 * w, 1u); lds_st(lds, SY_INTILE + 8 * w + 4, 1u); }
    } else {
        const int wa = w - 4, ch = wa >> 1, nh = wa & 1;
        {
            const float ex = expf(bf2f(pdt));
            const float dt = bf_lo(pack_bf(ex, ex));
            const float zr = dt * bf2f(pAre), zi = dt * bf2f(pAim);
            const float br = dt * bf2f(pBre), bi = dt * bf2f(pBim);
            const float cr = bf2f(pCre), ci = bf2f(pCim);
            *(v4f*)(lds + L_PT + (i - 256) * 16) =
                (v4f){zr * 1.44269504088896341f, zi * 0.15915494309189535f, cr * br - ci * bi, cr * bi + ci * br};
        }
        asm volatile("s_waitcnt lgkmcnt(0)" ::: "memory");
        const int row = l & 15, g = l >> 4;
        const float m0 = (float)(128 * (row >> 3) + (row & 7));
        const float m1 = (float)(row < 8 ? 256 + 128 * (row >> 2) + 2 * (row & 3) : 512 + 128 * ((row - 8) >> 1) + 4 * (row & 1));
        const float m2 = (float)(row < 8 ? 1024 + 128 * row : (row == 8 ? 2048 : 0));
        const float mb = (float)(8 * row);
        v4f acc0 = {0, 0, 0, 0}, acc1 = {0, 0, 0, 0}, acc2 = {0, 0, 0, 0};
#pragma unroll 2
        for (int s = 0; s < 16; ++s) {
            const v4f P = *(const v4f*)(lds + L_PT + (ch * 128 + nh * 64 + 4 * s + g) * 16);
            float er, ei, ar, ai;
            cexp_pow(P.x, P.y, mb, er, ei);
            const float Br = er * P.z - ei * P.w, Bn = -(er * P.w + ei * P.z);
            cexp_pow(P.x, P.y, m0, ar, ai);
            acc0 = __builtin_amdgcn_mfma_f32_16x16x4f32(ar, Br, acc0, 0, 0, 0);
            acc0 = __builtin_amdgcn_mfma_f32_16x16x4f32(ai, Bn, acc0, 0, 0, 0);
            cexp_pow(P.x, P.y, m1, ar, ai);
            acc1 = __builtin_amdgcn_mfma_f32_16x16x4f32(ar, Br, acc1, 0, 0, 0);
            acc1 = __builtin_amdgcn_mfma_f32_16x16x4f32(ai, Bn, acc1, 0, 0, 0);
            cexp_pow(P.x, P.y, m2, ar, ai);
            acc2 = __builtin_amdgcn_mfma_f32_16x16x4f32(ar, Br, acc2, 0, 0, 0);
            acc2 = __builtin_amdgcn_mfma_f32_16x16x4f32(ai, Bn, acc2, 0, 0, 0);
        }
        STAMP(4);
        float* kt = (float*)(lds + L_KT + ch * KT_CH);
        const int col = l & 15;
#pragma unroll
        for (int r = 0; r < 4; ++r) {
            const int rw = 4 * g + r;
            const int mu0 = 128 * (rw >> 3) + (rw & 7) + 8 * col;
            const int mu1 = rw < 8 ? 256 + 64 * (rw >> 2) + (rw & 3) + 4 * col : 384 + 32 * ((rw - 8) >> 1) + (rw & 1) + 2 * col;
            const int mu2 = rw < 8 ? 512 + 16 * rw + col : ((rw == 8 && col == 0) ? 640 : 644);
            (void)__hip_atomic_fetch_add(kt + mu0, acc0[r], __ATOMIC_RELAXED, __HIP_MEMORY_SCOPE_WORKGROUP);
            (void)__hip_atomic_fetch_add(kt + mu1, acc1[r], __ATOMIC_RELAXED, __HIP_MEMORY_SCOPE_WORKGROUP);
            (void)__hip_atomic_fetch_add(kt + mu2, acc2[r], __ATOMIC_RELAXED, __HIP_MEMORY_SCOPE_WORKGROUP);
        }
        lds_signal_inc(lds, SY_KT, l);
        if (wa < 2) {
            lds_wait_ge(lds, SY_KT, 4);
#pragma unroll 1
            for (int it = 0; it < 4; ++it) {
                const int c = it >> 1, m = (it & 1) * 128 + wa * 64 + l;
                const float* t = (const float*)(lds + L_KT + c * KT_CH);
                float kvv[9];
#pragma unroll
                for (int j = 0; j < 9; ++j) {
                    const int lag = 8 * m + j;
                    const u32 bb = pack_bf((float)lag, 0.0f) & 0xFFFFu;
                    const int mu = lag <= 256 ? lag : (int)(bb - 0x4380u + 256u);
                    kvv[j] = t[mu];
                }
                const u32 e0 = pack_bf(kvv[0], kvv[1]), e1 = pack_bf(kvv[2], kvv[3]), e2 = pack_bf(kvv[4], kvv[5]), e3 = pack_bf(kvv[6], kvv[7]);
                const u32 o0 = pack_bf(kvv[1], kvv[2]), o1 = pack_bf(kvv[3], kvv[4]), o2 = pack_bf(kvv[5], kvv[6]), o3 = pack_bf(kvv[7], kvv[8]);
                unsigned char* c0 = lds + L_KB + c * KC_CH;
                *(v4u*)(c0 + 32 + m * 16) = (v4u){e0, e1, e2, e3};
                *(v4u*)(c0 + KC_COPY + 32 + m * 16) = (v4u){o0, o1, o2, o3};
                if (m == 0) *(u32*)(c0 + KC_COPY + 28) = e0 << 16;
            }
            lds_signal_inc(lds, SY_KREADY, l);
            STAMP(5);
        }
    }

    float ya[8];
#pragma unroll
    for (int t = 0; t < 8; ++t) ya[t] = 0.0f;
    int redsel = 0;

#pragma unroll 1
    for (int k = 0; k < 3; ++k) {
        STAMP(7 + 8 * k);
        if (w < 4) {
            const int ch = w >> 1, h = w & 1, a = l & 15, g = l >> 4;
            const unsigned char* ub = lds + L_UR + ch * UR_PITCH + 2 * (2032 + 128 * h - 16 * a + 8 * g) - 3584;
            unsigned char* pb = lds + L_PART + ch * PART_CH + h * 1024 + 64 * a + 16 * g;
            v4f acc[8];
#pragma unroll
            for (int c = 0; c < 8; ++c) acc[c] = (v4f){0, 0, 0, 0};
#pragma unroll
            for (int q = 0; q < 8; ++q) {
                lds_wait_ge(lds, SY_INTILE + 4 * q, (u32)(k + 1));
                if (q == 0) STAMP(8 + 8 * k);
                if (q == 7) STAMP(9 + 8 * k);
                v4u B[4];
#pragma unroll
                for (int r = 0; r < 4; ++r) B[r] = *(const v4u*)(ub + 512 * (7 - q) + 64 * r);
#pragma unroll
                for (int r = 0; r < 4; ++r)
                    acc[q] = __builtin_amdgcn_mfma_f32_16x16x32_bf16(__builtin_bit_cast(v8bf, A[0][r]), __builtin_bit_cast(v8bf, B[r]), acc[q], 0, 0, 0);
                *(v4f*)(pb + q * PART_TILE) = acc[q];
                lds_signal_inc(lds, SY_PART + 4 * q, l);
#pragma unroll
                for (int p = 1; p < 8 - q; ++p)
#pragma unroll
                    for (int r = 0; r < 4; ++r)
                        acc[p + q] = __builtin_amdgcn_mfma_f32_16x16x32_bf16(__builtin_bit_cast(v8bf, A[p][r]), __builtin_bit_cast(v8bf, B[r]), acc[p + q], 0, 0, 0);
            }
            if (k == 0) {
                lds_wait_ge(lds, SY_KREADY, 2);
                load_afrags(A, lds, L_KB, ch, h, l);
            }
        } else if (w < 6) {
            if (k > 0) {
#pragma unroll 1
                for (int q = w & 1; q < 8; q += 2) {
                    const int r = 32 * q + (l >> 1);
                    const v4u d = poll16(rs, r * U_PITCH + (2 * me + (l & 1)) * 16, WS_U + (k - 1) * U_SIZE, lds);
                    *(v4u*)(lds + L_UR + (l & 1) * UR_PITCH + 2 * (2040 - 8 * r)) = d;
                    lds_signal_set(lds, SY_INTILE + 4 * q, (u32)(k + 1), l);
                    if (q < 2) STAMP(8 + 8 * k);
                    if (q >= 6) STAMP(9 + 8 * k);
                }
            }
        } else {
            const int ch = l & 1, tl = 32 * (l >> 3) + 8 * ((l >> 1) & 3);
            const float dco = k == 0 ? dcoA : dcoB;
#pragma unroll 1
            for (int c = w & 1; c < 8; c += 2) {
                lds_wait_ge(lds, SY_PART + 4 * c, (u32)(4 * (k + 1)));
                const unsigned char* pp = lds + L_PART + c * PART_TILE + ch * PART_CH + tl * 4;
                const v4f a0 = *(const v4f*)pp, a1 = *(const v4f*)(pp + 16);
                const v4f b0 = *(const v4f*)(pp + 1024), b1 = *(const v4f*)(pp + 1040);
                const v4u u = *(const v4u*)(lds + L_UR + ch * UR_PITCH + 2 * (2040 - (256 * c + tl)));
                v4u o;
                o.x = no_poison(pack_bf(a0.x + b0.x + dco * bf_hi(u.w), a0.y + b0.y + dco * bf_lo(u.w)));
                o.y = no_poison(pack_bf(a0.z + b0.z + dco * bf_hi(u.z), a0.w + b0.w + dco * bf_lo(u.z)));
                o.z = no_poison(pack_bf(a1.x + b1.x + dco * bf_hi(u.y), a1.y + b1.y + dco * bf_lo(u.y)));
                o.w = no_poison(pack_bf(a1.z + b1.z + dco * bf_hi(u.x), a1.w + b1.w + dco * bf_lo(u.x)));
                __builtin_amdgcn_raw_buffer_store_b128(o, rs, (8 * c + (l >> 3)) * C_PITCH + me * 128 + (l & 7) * 16, WS_C + k * C_SIZE, 16);
                if (c < 2) STAMP(8 + 8 * k);
                if (c >= 6) STAMP(9 + 8 * k);
            }
        }

        STAMP(10 + 8 * k);
        float mr[8], rsd[8], n0[8];
        if (k == 0) {
            __builtin_amdgcn_raw_buffer_store_b128(poison4, rs, (i >> 3) * C_PITCH + me * 128 + (i & 7) * 16, WS_C + C_SIZE, 16);
            __builtin_amdgcn_raw_buffer_store_b128(poison4, rs, me * U_PITCH + i * 16, WS_U + U_SIZE, 16);
            row_stats(x, lds, L_RED + redsel * 512, w, l, mr, rsd);
            redsel ^= 1;
#pragma unroll
            for (int t = 0; t < 8; ++t) n0[t] = x[t] * rsd[t] - mr[t];
        } else if (k == 1) {
            __builtin_amdgcn_raw_buffer_store_b128(poison4, rs, (i >> 3) * C_PITCH + me * 128 + (i & 7) * 16, WS_C + 2 * C_SIZE, 16);
        }
        STAMP(11 + 8 * k);
        const v4u cg = poll16(rs, (me >> 2) * C_PITCH + (i >> 1) * 128 + (me & 3) * 32 + (i & 1) * 16, WS_C + k * C_SIZE, lds);
        STAMP(12 + 8 * k);
        float c[8] = {bf_lo(cg.x), bf_hi(cg.x), bf_lo(cg.y), bf_hi(cg.y), bf_lo(cg.z), bf_hi(cg.z), bf_lo(cg.w), bf_hi(cg.w)};
        if (k == 2) {
#pragma unroll
            for (int t = 0; t < 8; t += 2) {
                const u32 o = pack_bf(x[t] + c[t], x[t + 1] + c[t + 1]);
                out_[(8 * me + t) * D_ + i] = (u16)o;
                out_[(8 * me + t + 1) * D_ + i] = (u16)(o >> 16);
            }
            break;
        }
        if (k == 0) {
#pragma unroll
            for (int t = 0; t < 8; ++t) { ya[t] = c[t]; x[t] += n0[t] * c[t]; }
        } else {
#pragma unroll
            for (int t = 0; t < 8; ++t) x[t] += c[t];
            row_stats(x, lds, L_RED + redsel * 512, w, l, mr, rsd);
            redsel ^= 1;
#pragma unroll
            for (int t = 0; t < 8; ++t) x[t] += (x[t] * rsd[t] - mr[t]) * ya[t];
        }
        asm volatile("s_waitcnt vmcnt(0)" ::: "memory");
        row_stats(x, lds, L_RED + redsel * 512, w, l, mr, rsd);
        redsel ^= 1;
        v4u uo;
        uo.x = no_poison(pack_bf(x[7] * rsd[7] - mr[7], x[6] * rsd[6] - mr[6]));
        uo.y = no_poison(pack_bf(x[5] * rsd[5] - mr[5], x[4] * rsd[4] - mr[4]));
        uo.z = no_poison(pack_bf(x[3] * rsd[3] - mr[3], x[2] * rsd[2] - mr[2]));
        uo.w = no_poison(pack_bf(x[1] * rsd[1] - mr[1], x[0] * rsd[0] - mr[0]));
        __builtin_amdgcn_raw_buffer_store_b128(uo, rs, me * U_PITCH + i * 16, WS_U + k * U_SIZE, 16);
        STAMP(13 + 8 * k);
    }
    STAMP(31);
    if ((blockIdx.x == 0 || blockIdx.x == 100 || blockIdx.x == 255) && (w == 0 || w == 4 || w == 6) && l < 32 && stv) printf("B%d w%d s%d %u\n", blockIdx.x, w, l, stv);
}

extern "C" void kernel_launch(void* const* d_in, const int* in_sizes, int n_in, void* d_out, int out_size, void* d_ws, size_t ws_size,
                              hipStream_t stream) {
    (void)in_sizes; (void)n_in; (void)out_size; (void)ws_size;
    h3s4_kernel<<<256, 512, 0, stream>>>(
        (const u16*)d_in[0], (const u16*)d_in[1], (const u16*)d_in[2], (const u16*)d_in[3], (const u16*)d_in[4], (const u16*)d_in[5],
        (const u16*)d_in[7], (const u16*)d_in[8], (const u16*)d_in[9], (const u16*)d_in[10], (const u16*)d_in[11], (const u16*)d_in[12],
        (const u16*)d_in[13], (const u16*)d_in[14], (u16*)d_out, (unsigned char*)d_ws);
}
```

```cpp
#include <hip/hip_runtime.h>
#include <stdint.h>

typedef unsigned short u16;
typedef unsigned u32;
typedef u32 v4u __attribute__((ext_vector_type(4)));
typedef float v2f __attribute__((ext_vector_type(2)));
typedef float v4f __attribute__((ext_vector_type(4)));
typedef __bf16 v2bf __attribute__((ext_vector_type(2)));
typedef __bf16 v8bf __attribute__((ext_vector_type(8)));

constexpr int S_ = 2048, D_ = 512, NS_ = 128;
constexpr u32 POISON = 0xFFFFFFFFu;

constexpr u32 WS_EPOCH = 0;
constexpr u32 WS_FLAGS = 4096;
constexpr u32 KV_PITCH = 4096 + 128;
constexpr u32 WS_KV = 8192;
constexpr u32 C_PITCH = 32768 + 128;
constexpr u32 C_SIZE = 64 * C_PITCH;
constexpr u32 WS_C = WS_KV + 512 * KV_PITCH;
constexpr u32 U_PITCH = 8192 + 128;
constexpr u32 U_SIZE = 256 * U_PITCH;
constexpr u32 WS_U = WS_C + 3 * C_SIZE;
constexpr u32 WS_TOTAL = WS_U + 2 * U_SIZE;

constexpr int UR_PITCH = 4608;
constexpr int L_UR = 0;
constexpr int PART_CH = 2048 + 16;
constexpr int PART_TILE = 2 * PART_CH;
constexpr int L_PART = L_UR + 2 * UR_PITCH;
constexpr int KC_COPY = 4160;
constexpr int KC_CH = 2 * KC_COPY;
constexpr int L_KA = L_PART + 8 * PART_TILE;
constexpr int L_KB = L_KA + 2 * KC_CH;
constexpr int KT_CH = 648 * 4;
constexpr int L_KT = L_KB + 2 * KC_CH;
constexpr int L_PT = L_KT + 2 * KT_CH;
constexpr int L_T = L_PT + 4096;
constexpr int L_RED = L_T + 8192;
constexpr int L_SYNC = L_RED + 2 * 512;
constexpr int SY_INTILE = L_SYNC;
constexpr int SY_PART = L_SYNC + 32;
constexpr int SY_KV = L_SYNC + 64;
constexpr int SY_GO = L_SYNC + 68;
constexpr int SY_KT = L_SYNC + 72;
constexpr int SY_KREADY = L_SYNC + 76;
constexpr int SY_ABORT = L_SYNC + 80;
constexpr int LDS_BYTES = L_SYNC + 96;
static_assert(LDS_BYTES > 82 * 1024 && LDS_BYTES <= 160 * 1024, "one workgroup per CU");

constexpr u32 LDS_SPIN_CAP = 1u << 20;
constexpr u32 GLB_SPIN_CAP = 1u << 16;

__device__ __forceinline__ float bf_lo(u32 x) { return __builtin_bit_cast(float, x << 16); }
__device__ __forceinline__ float bf_hi(u32 x) { return __builtin_bit_cast(float, x & 0xFFFF0000u); }
__device__ __forceinline__ float bf2f(u16 x) { return __builtin_bit_cast(float, ((u32)x) << 16); }
__device__ __forceinline__ u32 pack_bf(float lo, float hi) {
    v2f f = {lo, hi};
    return __builtin_bit_cast(u32, __builtin_convertvector(f, v2bf));
}
__device__ __forceinline__ u32 no_poison(u32 x) { return x < 0xFFFFFFFEu ? x : 0xFFFFFFFEu; }
template <int CTRL> __device__ __forceinline__ float dppf(float x) {
    return __builtin_bit_cast(float, __builtin_amdgcn_mov_dpp(__builtin_bit_cast(int, x), CTRL, 0xf, 0xf, true));
}
template <int CTRL> __device__ __forceinline__ u32 dppu(u32 x) { return (u32)__builtin_amdgcn_mov_dpp((int)x, CTRL, 0xf, 0xf, true); }

#define STAMP(n) do {} while (0)
__device__ __forceinline__ u32 lds_ld(unsigned char* lds, int off) {
    return __hip_atomic_load((u32*)(lds + off), __ATOMIC_RELAXED, __HIP_MEMORY_SCOPE_WORKGROUP);
}
__device__ __forceinline__ void lds_st(unsigned char* lds, int off, u32 v) {
    __hip_atomic_store((u32*)(lds + off), v, __ATOMIC_RELAXED, __HIP_MEMORY_SCOPE_WORKGROUP);
}
__device__ __forceinline__ void lds_inc(unsigned char* lds, int off) {
    (void)__hip_atomic_fetch_add((u32*)(lds + off), 1u, __ATOMIC_RELAXED, __HIP_MEMORY_SCOPE_WORKGROUP);
}
__device__ __forceinline__ void lds_signal_inc(unsigned char* lds, int off, int lane) {
    asm volatile("s_waitcnt lgkmcnt(0)" ::: "memory");
    if (lane == 0) lds_inc(lds, off);
}
__device__ __forceinline__ void lds_signal_set(unsigned char* lds, int off, u32 v, int lane) {
    asm volatile("s_waitcnt lgkmcnt(0)" ::: "memory");
    if (lane == 0) lds_st(lds, off, v);
}
__device__ __forceinline__ void lds_wait_ge(unsigned char* lds, int off, u32 target) {
    for (u32 it = 0;; ++it) {
        const u32 v = __builtin_amdgcn_readfirstlane(lds_ld(lds, off));
        if (v >= target) break;
        if ((it & 63u) == 63u) {
            if (__builtin_amdgcn_readfirstlane(lds_ld(lds, SY_ABORT))) break;
            if (it > LDS_SPIN_CAP) { lds_st(lds, SY_ABORT, 1u); break; }
        }
        __builtin_amdgcn_s_sleep(1);
    }
    asm volatile("" ::: "memory");
}
__device__ __forceinline__ bool valid16(v4u r) {
    u32 m = r.x > r.y ? r.x : r.y;
    m = m > r.z ? m : r.z;
    m = m > r.w ? m : r.w;
    return m != POISON;
}
__device__ __forceinline__ v4u poll16(__amdgpu_buffer_rsrc_t rs, u32 voff, u32 soff, unsigned char* lds) {
    v4u r = __builtin_amdgcn_raw_buffer_load_b128(rs, voff, soff, 16);
    bool ok = valid16(r);
    for (u32 it = 0; !__all(ok); ++it) {
        if ((it & 15u) == 15u) {
            if (__builtin_amdgcn_readfirstlane(lds_ld(lds, SY_ABORT))) break;
            if (it > GLB_SPIN_CAP) { lds_st(lds, SY_ABORT, 1u); break; }
        }
        asm volatile("" ::: "memory");
        if (!ok) { r = __builtin_amdgcn_raw_buffer_load_b128(rs, voff, soff, 16); ok = valid16(r); }
    }
    return r;
}

__device__ __forceinline__ void row_stats(const float (&x)[8], unsigned char* lds, int redoff, int w, int l, float (&mr)[8], float (&rs)[8]) {
    float z[4];
    {
        float a[8];
#pragma unroll
        for (int t = 0; t < 8; ++t) {
            auto s = __builtin_amdgcn_permlane32_swap(__builtin_bit_cast(u32, x[t]), __builtin_bit_cast(u32, x[t] * x[t]), false, false);
            const u32 s0 = s[0], s1 = s[1];
            a[t] = __builtin_bit_cast(float, s0) + __builtin_bit_cast(float, s1);
        }
#pragma unroll
        for (int t = 0; t < 4; ++t) {
            auto s = __builtin_amdgcn_permlane16_swap(__builtin_bit_cast(u32, a[t]), __builtin_bit_cast(u32, a[t + 4]), false, false);
            const u32 s0 = s[0], s1 = s[1];
            z[t] = __builtin_bit_cast(float, s0) + __builtin_bit_cast(float, s1);
        }
    }
    const float y0 = z[0] + dppf<0x128>(z[0]), y1 = z[1] + dppf<0x128>(z[1]);
    const float y2 = z[2] + dppf<0x128>(z[2]), y3 = z[3] + dppf<0x128>(z[3]);
    const float Y0 = (l & 8) ? y1 : y0, Y1 = (l & 8) ? y3 : y2;
    const float a0 = Y0 + dppf<0x141>(Y0), a1 = Y1 + dppf<0x141>(Y1);
    float Z = (l & 4) ? a1 : a0;
    Z += dppf<0xB1>(Z);
    Z += dppf<0x4E>(Z);
    const int val = ((l >> 3) & 1) + 2 * ((l >> 2) & 1) + 4 * (l >> 4);
    if ((l & 3) == 0) *(float*)(lds + redoff + val * 32 + w * 4) = Z;
    __syncthreads();
    const int t = l & 7;
    const v4f p0 = *(const v4f*)(lds + redoff + t * 32), p1 = *(const v4f*)(lds + redoff + t * 32 + 16);
    const v4f q0 = *(const v4f*)(lds + redoff + 256 + t * 32), q1 = *(const v4f*)(lds + redoff + 256 + t * 32 + 16);
    const float s1 = ((p0.x + p0.y) + (p0.z + p0.w)) + ((p1.x + p1.y) + (p1.z + p1.w));
    const float s2 = ((q0.x + q0.y) + (q0.z + q0.w)) + ((q1.x + q1.y) + (q1.z + q1.w));
    const float mean = s1 * (1.0f / D_);
    const float var = fmaxf(s2 * (1.0f / D_) - mean * mean, 0.0f);
    const float r = __builtin_amdgcn_rsqf(var + 1e-5f);
    const float m = mean * r;
#pragma unroll
    for (int j = 0; j < 8; ++j) {
        rs[j] = __builtin_bit_cast(float, __builtin_amdgcn_readlane(__builtin_bit_cast(int, r), j));
        mr[j] = __builtin_bit_cast(float, __builtin_amdgcn_readlane(__builtin_bit_cast(int, m), j));
    }
}

__device__ __forceinline__ void load_afrags(v4u (&A)[8][4], unsigned char* lds, int region, int ch, int h, int l) {
    const int b = l & 15, g = l >> 4, par = (1 + b) & 1;
    const unsigned char* base = lds + region + ch * KC_CH + par * KC_COPY + 256 * h + 16 * g + 2 * (1 + b - par);
#pragma unroll
    for (int p = 0; p < 8; ++p)
#pragma unroll
        for (int r = 0; r < 4; ++r) {
            const u32* q = (const u32*)(base + 512 * p + 64 * r);
            A[p][r] = (v4u){q[0], q[1], q[2], q[3]};
        }
}

__device__ __forceinline__ void cexp_pow(float al, float be, float m, float& re, float& im) {
    const float e = __builtin_amdgcn_exp2f(al * m);
    const float ph = __builtin_amdgcn_fractf(be * m);
    re = e * __builtin_amdgcn_cosf(ph);
    im = e * __builtin_amdgcn_sinf(ph);
}

__global__ void __launch_bounds__(512, 2)
h3s4_kernel(const u16* __restrict__ q_, const u16* __restrict__ k_, const u16* __restrict__ v_, const u16* __restrict__ shk_,
            const u16* __restrict__ ssmk_, const u16* __restrict__ ssmD_, const u16* __restrict__ Are_, const u16* __restrict__ Aim_,
            const u16* __restrict__ Bre_, const u16* __restrict__ Bim_, const u16* __restrict__ Cre_, const u16* __restrict__ Cim_,
            const u16* __restrict__ logdt_, const u16* __restrict__ Dskip_, u16* __restrict__ out_, unsigned char* ws_) {
    __shared__ __attribute__((aligned(16))) unsigned char lds[LDS_BYTES];
    unsigned long long tstart_;
    asm volatile("s_memrealtime %0" : "=s"(tstart_) :: "memory");
    __builtin_amdgcn_sched_barrier(0);
    const int i = threadIdx.x, l = i & 63;
    const int w = __builtin_amdgcn_readfirstlane(i >> 6);
    const int xcd = blockIdx.x & 7, idx = blockIdx.x >> 3, me = xcd * 32 + idx;
    const __amdgpu_buffer_rsrc_t rs = __builtin_amdgcn_make_buffer_rsrc(ws_, 0, WS_TOTAL, 0x00020000);
    const __amdgpu_buffer_rsrc_t ro = __builtin_amdgcn_make_buffer_rsrc(out_, 0, S_ * D_ * 2, 0x00020000);
    const v4u poison4 = {POISON, POISON, POISON, POISON};
    STAMP(0);

    const int prow = i >> 3, cgrp = i & 7;
    const int R = 64 * idx + prow, pch = 64 * xcd + 8 * cgrp;
    v4u kk[4];
#pragma unroll
    for (int j = 0; j < 4; ++j) {
        const int Rj = R - j;
        kk[j] = *(const v4u*)(k_ + (Rj < 0 ? 0 : Rj) * D_ + pch);
    }
    const v4u vv = *(const v4u*)(v_ + R * D_ + pch);
    v4u ww[4];
#pragma unroll
    for (int j = 0; j < 4; ++j) ww[j] = *(const v4u*)(shk_ + pch * 4 + j * 8);
    const int sch = i >> 8, sm = i & 255;
    const v4u sk = *(const v4u*)(ssmk_ + (2 * me + sch) * S_ + sm * 8);
    const u32 sknext = *(const u32*)(ssmk_ + (2 * me + sch) * S_ + (sm < 255 ? sm * 8 + 8 : 0));
    u32 epoch = 0;
    if (w == 0) epoch = __builtin_amdgcn_raw_buffer_load_b32(rs, 0, WS_EPOCH, 16);
    float x[8];
#pragma unroll
    for (int t = 0; t < 8; ++t) x[t] = bf2f(q_[(8 * me + t) * D_ + i]);
    const float dcoA = bf2f(ssmD_[2 * me + (l & 1)]), dcoB = bf2f(Dskip_[2 * me + (l & 1)]);
    u16 pAre = 0, pAim = 0, pBre = 0, pBim = 0, pCre = 0, pCim = 0, pdt = 0;
    if (w >= 4) {
        const int j = i - 256, pi = (2 * me + (j >> 7)) * NS_ + (j & 127);
        pAre = Are_[pi]; pAim = Aim_[pi]; pBre = Bre_[pi]; pBim = Bim_[pi]; pCre = Cre_[pi]; pCim = Cim_[pi];
        pdt = logdt_[2 * me + (j >> 7)];
    } else {
#pragma unroll
        for (int j = 0; j < 2; ++j) {
            const int e = i + 256 * j;
            __builtin_amdgcn_raw_buffer_store_b128(poison4, rs, (e >> 3) * C_PITCH + me * 128 + (e & 7) * 16, WS_C, 16);
        }
    }

    if (i < 64) *(v4u*)(lds + L_UR + (i >> 5) * UR_PITCH + 4096 + (i & 31) * 16) = (v4u){0, 0, 0, 0};
    if (i < 2 * 648 / 4) *(v4u*)(lds + L_KT + i * 16) = (v4u){0, 0, 0, 0};
    if (i < 24) *(u32*)(lds + L_SYNC + i * 4) = 0;
    if (i >= 64 && i < 64 + 16) {
        const int e = i - 64;
        *(v4u*)(lds + L_KA + (e >> 1) * KC_COPY + (e & 1) * 16) = (v4u){0, 0, 0, 0};
    }

    STAMP(1);
    {
        u32 pk[4];
#pragma unroll
        for (int j = 0; j < 4; ++j) if (R - j < 0) kk[j] = (v4u){0, 0, 0, 0};
#pragma unroll
        for (int c2 = 0; c2 < 4; ++c2) {
            float res[2];
#pragma unroll
            for (int hh = 0; hh < 2; ++hh) {
                const u32 sel = hh ? 0x07060302u : 0x05040100u;
                const u32 p01 = __builtin_amdgcn_perm(kk[1][c2], kk[0][c2], sel);
                const u32 p23 = __builtin_amdgcn_perm(kk[3][c2], kk[2][c2], sel);
                const int c = 2 * c2 + hh;
                const u32 w01 = ww[c >> 1][(c & 1) * 2], w23 = ww[c >> 1][(c & 1) * 2 + 1];
                float a = __builtin_amdgcn_fdot2_f32_bf16(__builtin_bit_cast(v2bf, p01), __builtin_bit_cast(v2bf, w01), 0.0f, false);
                a = __builtin_amdgcn_fdot2_f32_bf16(__builtin_bit_cast(v2bf, p23), __builtin_bit_cast(v2bf, w23), a, false);
                res[hh] = a * (hh ? bf_hi(vv[c2]) : bf_lo(vv[c2]));
            }
            pk[c2] = pack_bf(res[0], res[1]);
        }
        const bool odd = prow & 1;
        const u32 r0 = dppu<0x128>(odd ? pk[0] : pk[2]), r1 = dppu<0x128>(odd ? pk[1] : pk[3]);
        const u32 m0 = odd ? pk[2] : pk[0], m1 = odd ? pk[3] : pk[1];
        const u32 selx = odd ? 0x05040100u : 0x01000504u, sely = odd ? 0x07060302u : 0x03020706u;
        u32 o[4];
        o[0] = __builtin_amdgcn_perm(r0, m0, selx);
        o[1] = __builtin_amdgcn_perm(r0, m0, sely);
        o[2] = __builtin_amdgcn_perm(r1, m1, selx);
        o[3] = __builtin_amdgcn_perm(r1, m1, sely);
        const int chunk = prow >> 3, pos = 3 - ((prow & 7) >> 1);
#pragma unroll
        for (int c = 0; c < 4; ++c) {
            const int ch = 8 * cgrp + 4 * (odd ? 1 : 0) + c;
            *(u32*)(lds + L_T + ch * 128 + ((chunk ^ ((ch >> 2) & 7)) * 16) + pos * 4) = o[c];
        }
    }
    {
        unsigned char* c0 = lds + L_KA + sch * KC_CH;
        const u32 nx = sm < 255 ? sknext : 0u;
        *(v4u*)(c0 + 32 + sm * 16) = sk;
        *(v4u*)(c0 + KC_COPY + 32 + sm * 16) =
            (v4u){__builtin_amdgcn_alignbit(sk.y, sk.x, 16), __builtin_amdgcn_alignbit(sk.z, sk.y, 16),
                  __builtin_amdgcn_alignbit(sk.w, sk.z, 16), __builtin_amdgcn_alignbit(nx, sk.w, 16)};
    }
    STAMP(2);
    __syncthreads();
    STAMP(3);
    if (i == 0) *(u32*)(lds + L_KA + KC_COPY + 28) = *(const u32*)(lds + L_KA + 32) << 16;
    if (i == 64) *(u32*)(lds + L_KA + KC_CH + KC_COPY + 28) = *(const u32*)(lds + L_KA + KC_CH + 32) << 16;

    v4u A[8][4];
    if (w < 4) {
#pragma unroll
        for (int j = 0; j < 2; ++j) {
            const int e = i + 256 * j, ch = e >> 3, chunk = e & 7;
            const v4u d = *(const v4u*)(lds + L_T + ch * 128 + ((chunk ^ ((ch >> 2) & 7)) * 16));
            __builtin_amdgcn_raw_buffer_store_b128(d, rs, (64 * xcd + ch) * KV_PITCH + (8 * idx + chunk) * 16, WS_KV, 16);
        }
        asm volatile("s_waitcnt vmcnt(0)" ::: "memory");
        STAMP(4);
        lds_signal_inc(lds, SY_KV, l);
        u32 tag = 0;
        if (w == 0) {
            tag = __builtin_amdgcn_readfirstlane(epoch) + 1u;
            lds_wait_ge(lds, SY_KV, 4);
            if (l == 0) __builtin_amdgcn_raw_buffer_store_b32(tag, rs, me * 4, WS_FLAGS, 16);
        }
#pragma unroll
        for (int j = 0; j < 2; ++j)
            __builtin_amdgcn_raw_buffer_store_b128(poison4, rs, me * U_PITCH + (i + 256 * j) * 16, WS_U, 16);
        lds_wait_ge(lds, SY_KV, 4);
        load_afrags(A, lds, L_KA, w >> 1, w & 1, l);
        STAMP(5);
        if (w == 0) {
            for (u32 it = 0;; ++it) {
                asm volatile("" ::: "memory");
                const v4u f = __builtin_amdgcn_raw_buffer_load_b128(rs, l * 16, WS_FLAGS, 16);
                if (__all(f.x == tag && f.y == tag && f.z == tag && f.w == tag)) break;
                if (it > GLB_SPIN_CAP) { lds_st(lds, SY_ABORT, 1u); break; }
            }
            if (me == 0 && l == 0) __builtin_amdgcn_raw_buffer_store_b32(tag, rs, 0, WS_EPOCH, 16);
            lds_signal_set(lds, SY_GO, 1u, l);
        } else {
            lds_wait_ge(lds, SY_GO, 1u);
        }
        STAMP(6);
        v4u g0, g1;
        {
            const u32 off = (2 * me + (l >> 5)) * KV_PITCH + (64 * w + (l & 31)) * 16;
            g0 = __builtin_amdgcn_raw_buffer_load_b128(rs, off, WS_KV, 16);
            g1 = __builtin_amdgcn_raw_buffer_load_b128(rs, off + 512, WS_KV, 16);
        }
        unsigned char* dst = lds + L_UR + (l >> 5) * UR_PITCH + 2 * (2040 - 8 * (64 * w + (l & 31)));
        *(v4u*)dst = g0;
        *(v4u*)(dst - 512) = g1;
        asm volatile("s_waitcnt vmcnt(0) lgkmcnt(0)" ::: "memory");
        if (l == 0) { lds_st(lds, SY_INTILE + 8 * w, 1u); lds_st(lds, SY_INTILE + 8 * w + 4, 1u); }
    } else {
        const int wa = w - 4, ch = wa >> 1, nh = wa & 1;
        {
            const float ex = expf(bf2f(pdt));
            const float dt = bf_lo(pack_bf(ex, ex));
            const float zr = dt * bf2f(pAre), zi = dt * bf2f(pAim);
            const float br = dt * bf2f(pBre), bi = dt * bf2f(pBim);
            const float cr = bf2f(pCre), ci = bf2f(pCim);
            *(v4f*)(lds + L_PT + (i - 256) * 16) =
                (v4f){zr * 1.44269504088896341f, zi * 0.15915494309189535f, cr * br - ci * bi, cr * bi + ci * br};
        }
        asm volatile("s_waitcnt lgkmcnt(0)" ::: "memory");
        const int row = l & 15, g = l >> 4;
        const float m0 = (float)(128 * (row >> 3) + (row & 7));
        const float m1 = (float)(row < 8 ? 256 + 128 * (row >> 2) + 2 * (row & 3) : 512 + 128 * ((row - 8) >> 1) + 4 * (row & 1));
        const float m2 = (float)(row < 8 ? 1024 + 128 * row : (row == 8 ? 2048 : 0));
        const float mb = (float)(8 * row);
        v4f acc0 = {0, 0, 0, 0}, acc1 = {0, 0, 0, 0}, acc2 = {0, 0, 0, 0};
#pragma unroll 2
        for (int s = 0; s < 16; ++s) {
            const v4f P = *(const v4f*)(lds + L_PT + (ch * 128 + nh * 64 + 4 * s + g) * 16);
            float er, ei, ar, ai;
            cexp_pow(P.x, P.y, mb, er, ei);
            const float Br = er * P.z - ei * P.w, Bn = -(er * P.w + ei * P.z);
            cexp_pow(P.x, P.y, m0, ar, ai);
            acc0 = __builtin_amdgcn_mfma_f32_16x16x4f32(ar, Br, acc0, 0, 0, 0);
            acc0 = __builtin_amdgcn_mfma_f32_16x16x4f32(ai, Bn, acc0, 0, 0, 0);
            cexp_pow(P.x, P.y, m1, ar, ai);
            acc1 = __builtin_amdgcn_mfma_f32_16x16x4f32(ar, Br, acc1, 0, 0, 0);
            acc1 = __builtin_amdgcn_mfma_f32_16x16x4f32(ai, Bn, acc1, 0, 0, 0);
            cexp_pow(P.x, P.y, m2, ar, ai);
            acc2 = __builtin_amdgcn_mfma_f32_16x16x4f32(ar, Br, acc2, 0, 0, 0);
            acc2 = __builtin_amdgcn_mfma_f32_16x16x4f32(ai, Bn, acc2, 0, 0, 0);
        }
        STAMP(4);
        float* kt = (float*)(lds + L_KT + ch * KT_CH);
        const int col = l & 15;
#pragma unroll
        for (int r = 0; r < 4; ++r) {
            const int rw = 4 * g + r;
            const int mu0 = 128 * (rw >> 3) + (rw & 7) + 8 * col;
            const int mu1 = rw < 8 ? 256 + 64 * (rw >> 2) + (rw & 3) + 4 * col : 384 + 32 * ((rw - 8) >> 1) + (rw & 1) + 2 * col;
            const int mu2 = rw < 8 ? 512 + 16 * rw + col : ((rw == 8 && col == 0) ? 640 : 644);
            (void)__hip_atomic_fetch_add(kt + mu0, acc0[r], __ATOMIC_RELAXED, __HIP_MEMORY_SCOPE_WORKGROUP);
            (void)__hip_atomic_fetch_add(kt + mu1, acc1[r], __ATOMIC_RELAXED, __HIP_MEMORY_SCOPE_WORKGROUP);
            (void)__hip_atomic_fetch_add(kt + mu2, acc2[r], __ATOMIC_RELAXED, __HIP_MEMORY_SCOPE_WORKGROUP);
        }
        lds_signal_inc(lds, SY_KT, l);
        if (wa < 2) {
            lds_wait_ge(lds, SY_KT, 4);
#pragma unroll 1
            for (int it = 0; it < 4; ++it) {
                const int c = it >> 1, m = (it & 1) * 128 + wa * 64 + l;
                const float* t = (const float*)(lds + L_KT + c * KT_CH);
                float kvv[9];
#pragma unroll
                for (int j = 0; j < 9; ++j) {
                    const int lag = 8 * m + j;
                    const u32 bb = pack_bf((float)lag, 0.0f) & 0xFFFFu;
                    const int mu = lag <= 256 ? lag : (int)(bb - 0x4380u + 256u);
                    kvv[j] = t[mu];
                }
                const u32 e0 = pack_bf(kvv[0], kvv[1]), e1 = pack_bf(kvv[2], kvv[3]), e2 = pack_bf(kvv[4], kvv[5]), e3 = pack_bf(kvv[6], kvv[7]);
                const u32 o0 = pack_bf(kvv[1], kvv[2]), o1 = pack_bf(kvv[3], kvv[4]), o2 = pack_bf(kvv[5], kvv[6]), o3 = pack_bf(kvv[7], kvv[8]);
                unsigned char* c0 = lds + L_KB + c * KC_CH;
                *(v4u*)(c0 + 32 + m * 16) = (v4u){e0, e1, e2, e3};
                *(v4u*)(c0 + KC_COPY + 32 + m * 16) = (v4u){o0, o1, o2, o3};
                if (m == 0) *(u32*)(c0 + KC_COPY + 28) = e0 << 16;
            }
            lds_signal_inc(lds, SY_KREADY, l);
            STAMP(5);
        }
    }

    float ya[8];
#pragma unroll
    for (int t = 0; t < 8; ++t) ya[t] = 0.0f;
    int redsel = 0;

#pragma unroll 1
    for (int k = 0; k < 3; ++k) {
        STAMP(7 + 8 * k);
        if (w < 4) {
            const int ch = w >> 1, h = w & 1, a = l & 15, g = l >> 4;
            const unsigned char* ub = lds + L_UR + ch * UR_PITCH + 2 * (2032 + 128 * h - 16 * a + 8 * g) - 3584;
            unsigned char* pb = lds + L_PART + ch * PART_CH + h * 1024 + 64 * a + 16 * g;
            v4f acc[8];
#pragma unroll
            for (int c = 0; c < 8; ++c) acc[c] = (v4f){0, 0, 0, 0};
#pragma unroll
            for (int q = 0; q < 8; ++q) {
                lds_wait_ge(lds, SY_INTILE + 4 * q, (u32)(k + 1));
                if (q == 0) STAMP(8 + 8 * k);
                if (q == 7) STAMP(9 + 8 * k);
                v4u B[4];
#pragma unroll
                for (int r = 0; r < 4; ++r) B[r] = *(const v4u*)(ub + 512 * (7 - q) + 64 * r);
#pragma unroll
                for (int r = 0; r < 4; ++r)
                    acc[q] = __builtin_amdgcn_mfma_f32_16x16x32_bf16(__builtin_bit_cast(v8bf, A[0][r]), __builtin_bit_cast(v8bf, B[r]), acc[q], 0, 0, 0);
                *(v4f*)(pb + q * PART_TILE) = acc[q];
                lds_signal_inc(lds, SY_PART + 4 * q, l);
#pragma unroll
                for (int p = 1; p < 8 - q; ++p)
#pragma unroll
                    for (int r = 0; r < 4; ++r)
                        acc[p + q] = __builtin_amdgcn_mfma_f32_16x16x32_bf16(__builtin_bit_cast(v8bf, A[p][r]), __builtin_bit_cast(v8bf, B[r]), acc[p + q], 0, 0, 0);
            }
            if (k == 0) {
                lds_wait_ge(lds, SY_KREADY, 2);
                load_afrags(A, lds, L_KB, ch, h, l);
            }
        } else if (w < 6) {
            if (k > 0) {
#pragma unroll 1
                for (int q = w & 1; q < 8; q += 2) {
                    const int r = 32 * q + (l >> 1);
                    const v4u d = poll16(rs, r * U_PITCH + (2 * me + (l & 1)) * 16, WS_U + (k - 1) * U_SIZE, lds);
                    *(v4u*)(lds + L_UR + (l & 1) * UR_PITCH + 2 * (2040 - 8 * r)) = d;
                    lds_signal_set(lds, SY_INTILE + 4 * q, (u32)(k + 1), l);
                    if (q < 2) STAMP(8 + 8 * k);
                    if (q >= 6) STAMP(9 + 8 * k);
                }
            }
        } else {
            const int ch = l & 1, tl = 32 * (l >> 3) + 8 * ((l >> 1) & 3);
            const float dco = k == 0 ? dcoA : dcoB;
#pragma unroll 1
            for (int c = w & 1; c < 8; c += 2) {
                lds_wait_ge(lds, SY_PART + 4 * c, (u32)(4 * (k + 1)));
                const unsigned char* pp = lds + L_PART + c * PART_TILE + ch * PART_CH + tl * 4;
                const v4f a0 = *(const v4f*)pp, a1 = *(const v4f*)(pp + 16);
                const v4f b0 = *(const v4f*)(pp + 1024), b1 = *(const v4f*)(pp + 1040);
                const v4u u = *(const v4u*)(lds + L_UR + ch * UR_PITCH + 2 * (2040 - (256 * c + tl)));
                v4u o;
                o.x = no_poison(pack_bf(a0.x + b0.x + dco * bf_hi(u.w), a0.y + b0.y + dco * bf_lo(u.w)));
                o.y = no_poison(pack_bf(a0.z + b0.z + dco * bf_hi(u.z), a0.w + b0.w + dco * bf_lo(u.z)));
                o.z = no_poison(pack_bf(a1.x + b1.x + dco * bf_hi(u.y), a1.y + b1.y + dco * bf_lo(u.y)));
                o.w = no_poison(pack_bf(a1.z + b1.z + dco * bf_hi(u.x), a1.w + b1.w + dco * bf_lo(u.x)));
                __builtin_amdgcn_raw_buffer_store_b128(o, rs, (8 * c + (l >> 3)) * C_PITCH + me * 128 + (l & 7) * 16, WS_C + k * C_SIZE, 16);
                if (c < 2) STAMP(8 + 8 * k);
                if (c >= 6) STAMP(9 + 8 * k);
            }
        }

        STAMP(10 + 8 * k);
        float mr[8], rsd[8], n0[8];
        if (k == 0) {
            __builtin_amdgcn_raw_buffer_store_b128(poison4, rs, (i >> 3) * C_PITCH + me * 128 + (i & 7) * 16, WS_C + C_SIZE, 16);
            __builtin_amdgcn_raw_buffer_store_b128(poison4, rs, me * U_PITCH + i * 16, WS_U + U_SIZE, 16);
            row_stats(x, lds, L_RED + redsel * 512, w, l, mr, rsd);
            redsel ^= 1;
#pragma unroll
            for (int t = 0; t < 8; ++t) n0[t] = x[t] * rsd[t] - mr[t];
        } else if (k == 1) {
            __builtin_amdgcn_raw_buffer_store_b128(poison4, rs, (i >> 3) * C_PITCH + me * 128 + (i & 7) * 16, WS_C + 2 * C_SIZE, 16);
        }
        STAMP(11 + 8 * k);
        const v4u cg = poll16(rs, (me >> 2) * C_PITCH + (i >> 1) * 128 + (me & 3) * 32 + (i & 1) * 16, WS_C + k * C_SIZE, lds);
        STAMP(12 + 8 * k);
        float c[8] = {bf_lo(cg.x), bf_hi(cg.x), bf_lo(cg.y), bf_hi(cg.y), bf_lo(cg.z), bf_hi(cg.z), bf_lo(cg.w), bf_hi(cg.w)};
        if (k == 2) {
#pragma unroll
            for (int t = 0; t < 8; t += 2) {
                const u32 o = pack_bf(x[t] + c[t], x[t + 1] + c[t + 1]);
                __builtin_amdgcn_raw_buffer_store_b16((u16)o, ro, ((8 * me + t) * D_ + i) * 2, 0, 16);
                __builtin_amdgcn_raw_buffer_store_b16((u16)(o >> 16), ro, ((8 * me + t + 1) * D_ + i) * 2, 0, 16);
            }
            break;
        }
        if (k == 0) {
#pragma unroll
            for (int t = 0; t < 8; ++t) { ya[t] = c[t]; x[t] += n0[t] * c[t]; }
        } else {
#pragma unroll
            for (int t = 0; t < 8; ++t) x[t] += c[t];
            row_stats(x, lds, L_RED + redsel * 512, w, l, mr, rsd);
            redsel ^= 1;
#pragma unroll
            for (int t = 0; t < 8; ++t) x[t] += (x[t] * rsd[t] - mr[t]) * ya[t];
        }
        asm volatile("s_waitcnt vmcnt(0)" ::: "memory");
        row_stats(x, lds, L_RED + redsel * 512, w, l, mr, rsd);
        redsel ^= 1;
        v4u uo;
        uo.x = no_poison(pack_bf(x[7] * rsd[7] - mr[7], x[6] * rsd[6] - mr[6]));
        uo.y = no_poison(pack_bf(x[5] * rsd[5] - mr[5], x[4] * rsd[4] - mr[4]));
        uo.z = no_poison(pack_bf(x[3] * rsd[3] - mr[3], x[2] * rsd[2] - mr[2]));
        uo.w = no_poison(pack_bf(x[1] * rsd[1] - mr[1], x[0] * rsd[0] - mr[0]));
        __builtin_amdgcn_raw_buffer_store_b128(uo, rs, me * U_PITCH + i * 16, WS_U + k * U_SIZE, 16);
        STAMP(13 + 8 * k);
    }
    while (__builtin_amdgcn_s_memrealtime() - tstart_ < 6000ull) __builtin_amdgcn_s_sleep(2);
}

extern "C" void kernel_launch(void* const* d_in, const int* in_sizes, int n_in, void* d_out, int out_size, void* d_ws, size_t ws_size,
                              hipStream_t stream) {
    (void)in_sizes; (void)n_in; (void)out_size; (void)ws_size;
    h3s4_kernel<<<256, 512, 0, stream>>>(
        (const u16*)d_in[0], (const u16*)d_in[1], (const u16*)d_in[2], (const u16*)d_in[3], (const u16*)d_in[4], (const u16*)d_in[5],
        (const u16*)d_in[7], (const u16*)d_in[8], (const u16*)d_in[9], (const u16*)d_in[10], (const u16*)d_in[11], (const u16*)d_in[12],
        (const u16*)d_in[13], (const u16*)d_in[14], (u16*)d_out, (unsigned char*)d_ws);
}
```

```cpp
#include <hip/hip_runtime.h>
#include <stdint.h>

typedef unsigned short u16;
typedef unsigned u32;
typedef u32 v4u __attribute__((ext_vector_type(4)));
typedef float v2f __attribute__((ext_vector_type(2)));
typedef float v4f __attribute__((ext_vector_type(4)));
typedef __bf16 v2bf __attribute__((ext_vector_type(2)));
typedef __bf16 v8bf __attribute__((ext_vector_type(8)));
typedef _Float16 v2h __attribute__((ext_vector_type(2)));
typedef _Float16 v8h __attribute__((ext_vector_type(8)));

constexpr int S_ = 2048, D_ = 512, NS_ = 128;
constexpr u32 POISON = 0xFFFFFFFFu;

constexpr u32 WS_EPOCH = 0;
constexpr u32 WS_FLAGS = 4096;
constexpr u32 KV_PITCH = 4096 + 128;
constexpr u32 WS_FLAGP = WS_FLAGS + 256 * 128;
constexpr u32 WS_KV = WS_FLAGP + 256 * 128 + 4096;
constexpr u32 C_PITCH = 32768 + 128;
constexpr u32 C_SIZE = 64 * C_PITCH;
constexpr u32 WS_C = WS_KV + 512 * KV_PITCH;
constexpr u32 U_PITCH = 8192 + 128;
constexpr u32 U_SIZE = 256 * U_PITCH;
constexpr u32 WS_U = WS_C + 3 * C_SIZE;
constexpr u32 WS_TOTAL = WS_U + 2 * U_SIZE;

constexpr int UR_PITCH = 4608;
constexpr int L_UR = 0;
constexpr int PART_CH = 2048 + 64;
constexpr int PART_TILE = 2 * PART_CH;
constexpr int L_PART = L_UR + 2 * UR_PITCH;
constexpr int KC_COPY = 4160;
constexpr int KC_CH = 2 * KC_COPY;
constexpr int L_KA = L_PART + 8 * PART_TILE;
constexpr int L_KB = L_KA + 2 * KC_CH;
constexpr int KT_CH = 648 * 4;
constexpr int L_KT = L_KB + 2 * KC_CH;
constexpr int TAB_ROW = 528;
constexpr int TAB_CH = 57 * TAB_ROW;
constexpr int L_TAB = L_KT + 2 * KT_CH;
constexpr int L_T = L_TAB + 2 * TAB_CH;
constexpr int L_XN = L_T;
constexpr int L_PARK = L_TAB;
constexpr int L_RED = L_T + 16384;
constexpr int L_SYNC = L_RED + 2 * 512;
constexpr int SY_PART = L_SYNC;
constexpr int SY_INTILE = L_SYNC + 128;
constexpr int SY_KV = L_SYNC + 160;
constexpr int SY_GO = L_SYNC + 164;
constexpr int SY_TAB = L_SYNC + 168;

constexpr int SY_KREADY = L_SYNC + 184;
constexpr int SY_ABORT = L_SYNC + 188;
constexpr int SY_TAG = L_SYNC + 192;
constexpr int SY_ALLF = L_SYNC + 196;
constexpr int SY_PUB = L_SYNC + 200;
constexpr int SY_X0 = L_SYNC + 204;
constexpr int SY_KA = L_SYNC + 176;
constexpr int SY_TAGSET = L_SYNC + 180;
constexpr int SY_POIS = L_SYNC + 164 + 44;
constexpr int L_DCO = L_SYNC + 224;
constexpr int L_PIV = L_DCO + 32;
constexpr int LDS_BYTES = L_PIV + 32;
static_assert(LDS_BYTES > 82 * 1024 && LDS_BYTES <= 160 * 1024, "one workgroup per CU");

#define GATHER_AHEAD 1
constexpr u32 LDS_SPIN_CAP = 1u << 19;
constexpr u32 GLB_SPIN_CAP = 1u << 16;

#define STAMP(n) do {} while (0)
#define STAMPC(n) do {} while (0)
#define SCHED_FENCE() __builtin_amdgcn_sched_barrier(0)

__device__ __forceinline__ float bf_lo(u32 x) { return __builtin_bit_cast(float, x << 16); }
__device__ __forceinline__ float bf_hi(u32 x) { return __builtin_bit_cast(float, x & 0xFFFF0000u); }
__device__ __forceinline__ float bf2f(u16 x) { return __builtin_bit_cast(float, ((u32)x) << 16); }
__device__ __forceinline__ u32 pack_bf(float lo, float hi) {
    v2f f = {lo, hi};
    return __builtin_bit_cast(u32, __builtin_convertvector(f, v2bf));
}
__device__ __forceinline__ u32 pack_h(v2f f) { return __builtin_bit_cast(u32, __builtin_convertvector(f, v2h)); }
__device__ __forceinline__ u32 no_poison(u32 x) { return x < 0xFFFFFFFEu ? x : 0xFFFFFFFEu; }
template <int CTRL> __device__ __forceinline__ float dppf(float x) {
    return __builtin_bit_cast(float, __builtin_amdgcn_mov_dpp(__builtin_bit_cast(int, x), CTRL, 0xf, 0xf, true));
}
template <int CTRL> __device__ __forceinline__ u32 dppu(u32 x) { return (u32)__builtin_amdgcn_mov_dpp((int)x, CTRL, 0xf, 0xf, true); }
__device__ __forceinline__ float f_of(u32 x) { return __builtin_bit_cast(float, x); }
__device__ __forceinline__ u32 u_of(float x) { return __builtin_bit_cast(u32, x); }

__device__ __forceinline__ u32 lds_ld(unsigned char* lds, int off) {
    return __hip_atomic_load((u32*)(lds + off), __ATOMIC_RELAXED, __HIP_MEMORY_SCOPE_WORKGROUP);
}
__device__ __forceinline__ void lds_st(unsigned char* lds, int off, u32 v) {
    __hip_atomic_store((u32*)(lds + off), v, __ATOMIC_RELAXED, __HIP_MEMORY_SCOPE_WORKGROUP);
}
__device__ __forceinline__ void lds_flag_set(int off, u32 v) {
    asm volatile("s_mov_b64 exec, 1\n\tds_write_b32 %0, %1\n\ts_mov_b64 exec, -1" ::"v"(off), "v"(v) : "memory");
}
template <int Q> __device__ __forceinline__ void put_partial(u32 vpart, const v4f& acc) {
    asm volatile("ds_write_b128 %0, %1 offset:%c2" ::"v"(vpart), "v"(acc), "i"(Q * PART_TILE) : "memory");
}
template <int Q> __device__ __forceinline__ void put_partial_flag(u32 vflag, u32 v) {
    asm volatile("ds_write_b32 %0, %1 offset:%c2" ::"v"(vflag), "v"(v), "i"(Q * 16) : "memory");
}
__device__ __forceinline__ void lds_flag_inc(int off) {
    asm volatile("s_mov_b64 exec, 1\n\tds_add_u32 %0, %1\n\ts_mov_b64 exec, -1" ::"v"(off), "v"(1u) : "memory");
}
__device__ __forceinline__ bool spin_give_up(unsigned char* lds, u32 it, u32 cap) {
    if ((it & 63u) != 63u) return false;
    if (it > cap) { lds_st(lds, SY_ABORT, 1u); return true; }
    return __builtin_amdgcn_readfirstlane(lds_ld(lds, SY_ABORT)) != 0;
}
__device__ __forceinline__ void lds_wait_ge(unsigned char* lds, int off, u32 target) {
    if (__builtin_expect((u32)__builtin_amdgcn_readfirstlane(lds_ld(lds, off)) < target, 0)) {
        for (u32 it = 0; (u32)__builtin_amdgcn_readfirstlane(lds_ld(lds, off)) < target; ++it)
            if (spin_give_up(lds, it, LDS_SPIN_CAP)) break;
    }
    asm volatile("" ::: "memory");
}
__device__ __forceinline__ void lds_wait_ge_slow(unsigned char* lds, int off, u32 target) {
    for (u32 it = 0; (u32)__builtin_amdgcn_readfirstlane(lds_ld(lds, off)) < target; ++it) {
        if (spin_give_up(lds, it, LDS_SPIN_CAP)) break;
        __builtin_amdgcn_s_sleep(1);
    }
    asm volatile("" ::: "memory");
}
__device__ __forceinline__ void lds_wait4_ge(unsigned char* lds, int off, u32 target) {
    for (u32 it = 0;; ++it) {
        asm volatile("" ::: "memory");
        const v4u f = *(const v4u*)(lds + off);
        const u32 a = f.x < f.y ? f.x : f.y, b = f.z < f.w ? f.z : f.w;
        if ((u32)__builtin_amdgcn_readfirstlane(a < b ? a : b) >= target) break;
        if (spin_give_up(lds, it, LDS_SPIN_CAP)) break;
    }
    asm volatile("" ::: "memory");
}
__device__ __forceinline__ void store16(v4u v, __amdgpu_buffer_rsrc_t rs, u32 voff, u32 soff) {
    __builtin_amdgcn_raw_buffer_store_b128(v, rs, voff + soff, 0, 16);
}
__device__ __forceinline__ bool valid16(v4u r) {
    u32 m = r.x > r.y ? r.x : r.y;
    m = m > r.z ? m : r.z;
    m = m > r.w ? m : r.w;
    return m != POISON;
}
__device__ __forceinline__ v4u poll16(__amdgpu_buffer_rsrc_t rs, u32 voff, u32 soff, unsigned char* lds) {
    v4u r = __builtin_amdgcn_raw_buffer_load_b128(rs, voff, soff, 16);
    bool ok = valid16(r);
    for (u32 it = 0; !__all(ok); ++it) {
        if (spin_give_up(lds, it * 4 + 3, GLB_SPIN_CAP * 4)) break;
        asm volatile("" ::: "memory");
        if (!ok) { r = __builtin_amdgcn_raw_buffer_load_b128(rs, voff, soff, 16); ok = valid16(r); }
    }
    return r;
}

__device__ __forceinline__ float vmul(float a, float b) {
    float r;
    asm("v_mul_f32 %0, %1, %2" : "=v"(r) : "v"(a), "v"(b));
    return r;
}
#define DPPB(t) " row_newbcast:" #t " row_mask:0xf bank_mask:0xf\n\t"
__device__ __forceinline__ void sub_rows(float (&y)[8], const float (&x)[8], float p) {
    asm("s_nop 1\n\t"
        "v_subrev_f32_dpp %0, %16, %8" DPPB(0) "v_subrev_f32_dpp %1, %16, %9" DPPB(1) "v_subrev_f32_dpp %2, %16, %10" DPPB(2)
        "v_subrev_f32_dpp %3, %16, %11" DPPB(3) "v_subrev_f32_dpp %4, %16, %12" DPPB(4) "v_subrev_f32_dpp %5, %16, %13" DPPB(5)
        "v_subrev_f32_dpp %6, %16, %14" DPPB(6) "v_subrev_f32_dpp %7, %16, %15" DPPB(7)
        : "=&v"(y[0]), "=&v"(y[1]), "=&v"(y[2]), "=&v"(y[3]), "=&v"(y[4]), "=&v"(y[5]), "=&v"(y[6]), "=&v"(y[7])
        : "v"(x[0]), "v"(x[1]), "v"(x[2]), "v"(x[3]), "v"(x[4]), "v"(x[5]), "v"(x[6]), "v"(x[7]), "v"(p));
}
__device__ __forceinline__ void norm_rows(float (&y)[8], float r, float m) {
    asm("s_nop 1\n\t"
        "v_mul_f32_dpp %0, %8, %0" DPPB(0) "v_mul_f32_dpp %1, %8, %1" DPPB(1) "v_mul_f32_dpp %2, %8, %2" DPPB(2) "v_mul_f32_dpp %3, %8, %3" DPPB(3)
        "v_mul_f32_dpp %4, %8, %4" DPPB(4) "v_mul_f32_dpp %5, %8, %5" DPPB(5) "v_mul_f32_dpp %6, %8, %6" DPPB(6) "v_mul_f32_dpp %7, %8, %7" DPPB(7)
        "v_subrev_f32_dpp %0, %9, %0" DPPB(0) "v_subrev_f32_dpp %1, %9, %1" DPPB(1) "v_subrev_f32_dpp %2, %9, %2" DPPB(2)
        "v_subrev_f32_dpp %3, %9, %3" DPPB(3) "v_subrev_f32_dpp %4, %9, %4" DPPB(4) "v_subrev_f32_dpp %5, %9, %5" DPPB(5)
        "v_subrev_f32_dpp %6, %9, %6" DPPB(6) "v_subrev_f32_dpp %7, %9, %7" DPPB(7)
        : "+v"(y[0]), "+v"(y[1]), "+v"(y[2]), "+v"(y[3]), "+v"(y[4]), "+v"(y[5]), "+v"(y[6]), "+v"(y[7])
        : "v"(r), "v"(m));
}
#undef DPPB
__device__ __forceinline__ float vfma(float a, float b, float c) {
    float r;
    asm("v_fma_f32 %0, %1, %2, %3" : "=v"(r) : "v"(a), "v"(b), "v"(c));
    return r;
}
__device__ __forceinline__ float vadd(float a, float b) {
    float r;
    asm("v_add_f32 %0, %1, %2" : "=v"(r) : "v"(a), "v"(b));
    return r;
}

__device__ __forceinline__ void row_stats(const float (&y)[8], const v4u& sel_lo, const v4u& sel_hi, unsigned char* lds, int redoff, int w, int l,
                                          float& dmean, float& rstd) {
    const v4u a = {pack_bf(y[0], y[1]), pack_bf(y[2], y[3]), pack_bf(y[4], y[5]), pack_bf(y[6], y[7])};
    float q[8];
    asm("v_mul_f32 %0, %8, %8\n\tv_mul_f32 %1, %9, %9\n\tv_mul_f32 %2, %10, %10\n\tv_mul_f32 %3, %11, %11\n\t"
        "v_mul_f32 %4, %12, %12\n\tv_mul_f32 %5, %13, %13\n\tv_mul_f32 %6, %14, %14\n\tv_mul_f32 %7, %15, %15"
        : "=&v"(q[0]), "=&v"(q[1]), "=&v"(q[2]), "=&v"(q[3]), "=&v"(q[4]), "=&v"(q[5]), "=&v"(q[6]), "=&v"(q[7])
        : "v"(y[0]), "v"(y[1]), "v"(y[2]), "v"(y[3]), "v"(y[4]), "v"(y[5]), "v"(y[6]), "v"(y[7]));
    const v4u s = {pack_bf(q[0], q[1]), pack_bf(q[2], q[3]), pack_bf(q[4], q[5]), pack_bf(q[6], q[7])};
    v4f d = {0, 0, 0, 0};
    d = __builtin_amdgcn_mfma_f32_16x16x32_bf16(__builtin_bit_cast(v8bf, a), __builtin_bit_cast(v8bf, sel_lo), d, 0, 0, 0);
    d = __builtin_amdgcn_mfma_f32_16x16x32_bf16(__builtin_bit_cast(v8bf, s), __builtin_bit_cast(v8bf, sel_hi), d, 0, 0, 0);
    u32 t0 = u_of((d.x + d.y) + (d.z + d.w)), t1 = t0;
    {
        auto r = __builtin_amdgcn_permlane16_swap(t0, t1, false, false);
        const u32 r0 = r[0], r1 = r[1];
        t0 = u_of(f_of(r0) + f_of(r1)); t1 = t0;
    }
    {
        auto r = __builtin_amdgcn_permlane32_swap(t0, t1, false, false);
        const u32 r0 = r[0], r1 = r[1];
        t0 = u_of(f_of(r0) + f_of(r1));
    }
    *(u32*)(lds + redoff + (l & 15) * 32 + w * 4) = t0;
    __syncthreads();
    const v4f p0 = *(const v4f*)(lds + redoff + (l & 15) * 32), p1 = *(const v4f*)(lds + redoff + (l & 15) * 32 + 16);
    const v4f pp = p0 + p1;
    const float tot = (pp.x + pp.y) + (pp.z + pp.w);
    const float s2 = dppf<0x128>(tot);
    dmean = tot * (1.0f / D_);
    const float var = fmaxf(s2 * (1.0f / D_) - dmean * dmean, 0.0f);
    rstd = __builtin_amdgcn_rsqf(var + 1e-5f);
}
__device__ __forceinline__ float wave_sum(float v) {
    asm("s_nop 1" : "+v"(v));
    v += dppf<0x128>(v);
    v += dppf<0x141>(v);
    v += dppf<0xB1>(v);
    v += dppf<0x4E>(v);
    u32 a = u_of(v), b = a;
    {
        auto r = __builtin_amdgcn_permlane16_swap(a, b, false, false);
        const u32 r0 = r[0], r1 = r[1];
        a = u_of(f_of(r0) + f_of(r1)); b = a;
    }
    auto r = __builtin_amdgcn_permlane32_swap(a, b, false, false);
    const u32 r0 = r[0], r1 = r[1];
    return f_of(r0) + f_of(r1);
}

__device__ __forceinline__ void load_afrags(v4u (&A)[8][4], unsigned char* lds, int region, int ch, int h, int l) {
    const int b = l & 15, g = l >> 4, par = (1 + b) & 1;
    const unsigned char* base = lds + region + ch * KC_CH + par * KC_COPY + 256 * h + 16 * g + 2 * (1 + b - par);
#pragma unroll
    for (int p = 0; p < 8; ++p)
#pragma unroll
        for (int r = 0; r < 4; ++r) {
            const u32* q = (const u32*)(base + 512 * p + 64 * r);
            A[p][r] = (v4u){q[0], q[1], q[2], q[3]};
        }
}

__device__ __forceinline__ v2f cexp_pow(float al, float be, float m) {
    const float e = __builtin_amdgcn_exp2f(al * m);
    const float ph = __builtin_amdgcn_fractf(be * m);
    return (v2f){e * __builtin_amdgcn_cosf(ph), e * __builtin_amdgcn_sinf(ph)};
}
__device__ __forceinline__ v2f cmul(v2f a, v2f b) {
    v2f t;
    asm("v_pk_mul_f32 %0, %1, %2 op_sel:[0,0] op_sel_hi:[1,0]\n\t"
        "v_pk_fma_f32 %0, %1, %2, %0 op_sel:[1,1,0] op_sel_hi:[0,1,1] neg_lo:[1,0,0]"
        : "=&v"(t) : "v"(a), "v"(b));
    return t;
}
__device__ __forceinline__ v4f mfma_bf(v4u a, v4u b, v4f c) {
    return __builtin_amdgcn_mfma_f32_16x16x32_bf16(__builtin_bit_cast(v8bf, a), __builtin_bit_cast(v8bf, b), c, 0, 0, 0);
}

__global__ void __launch_bounds__(512, 2)
h3s4_kernel(const u16* __restrict__ q_, const u16* __restrict__ k_, const u16* __restrict__ v_, const u16* __restrict__ shk_,
            const u16* __restrict__ ssmk_, const u16* __restrict__ ssmD_, const u16* __restrict__ Are_, const u16* __restrict__ Aim_,
            const u16* __restrict__ Bre_, const u16* __restrict__ Bim_, const u16* __restrict__ Cre_, const u16* __restrict__ Cim_,
            const u16* __restrict__ logdt_, const u16* __restrict__ Dskip_, u16* __restrict__ out_, unsigned char* ws_) {
    __shared__ __attribute__((aligned(16))) unsigned char lds[LDS_BYTES];
    asm volatile("" ::"s"(q_), "s"(k_), "s"(v_), "s"(shk_), "s"(ssmk_), "s"(ssmD_), "s"(Are_), "s"(Aim_), "s"(Bre_), "s"(Bim_),
                 "s"(Cre_), "s"(Cim_), "s"(logdt_), "s"(Dskip_), "s"(out_), "s"(ws_));
    const int i = threadIdx.x, l = i & 63;
    const int w = __builtin_amdgcn_readfirstlane(i >> 6);
    const int xcd = blockIdx.x & 7, idx = blockIdx.x >> 3;
    const int me = xcd * 32 + idx;
    const int mytile = idx >> 2;
    const int myr = 32 * mytile + 4 * xcd + (idx & 3);
    const __amdgpu_buffer_rsrc_t rs = __builtin_amdgcn_make_buffer_rsrc(ws_, 0, WS_TOTAL, 0x00020000);
    const v4u poison4 = {POISON, POISON, POISON, POISON};
    STAMP(0);

    const int cp = i & 31, rb = i >> 5;
    const int R0 = 64 * idx + 4 * rb, pch = 64 * xcd + 2 * cp;
    u32 kd[7], vd[4];
#pragma unroll
    for (int j = 0; j < 7; ++j) {
        const int Rj = R0 - 3 + j;
        kd[j] = *(const u32*)(k_ + (Rj < 0 ? 0 : Rj) * D_ + pch);
    }
#pragma unroll
    for (int j = 0; j < 4; ++j) vd[j] = *(const u32*)(v_ + (R0 + j) * D_ + pch);
    const v4u wd = *(const v4u*)(shk_ + pch * 4);
    u32 epoch = 0;
    if (w == 0) epoch = __builtin_amdgcn_raw_buffer_load_b32(rs, 0, WS_EPOCH, 16);
    v4u xq[4];

    if (i >= 256 && i < 256 + 64) *(v4u*)(lds + L_UR + ((i >> 5) & 1) * UR_PITCH + 4096 + (i & 31) * 16) = (v4u){0, 0, 0, 0};
    if (i >= 320 && i < 320 + 16) {
        const int e = i - 320;
        *(v4u*)(lds + L_KA + (e >> 1) * KC_COPY + (e & 1) * 16) = (v4u){0, 0, 0, 0};
    }
    if (i >= 384 && i < 384 + 56) *(u32*)(lds + L_SYNC + (i - 384) * 4) = 0;

    STAMP(1);
    {
#pragma unroll
        for (int j = 0; j < 3; ++j) if (R0 - 3 + j < 0) kd[j] = 0;
#pragma unroll
        for (int c = 0; c < 2; ++c) {
            const u32 sel = c ? 0x07060302u : 0x05040100u;
            const u32 w01 = wd[2 * c], w23 = wd[2 * c + 1];
            u32 P[7];
#pragma unroll
            for (int j = 1; j < 7; ++j) P[j] = __builtin_amdgcn_perm(kd[j - 1], kd[j], sel);
            float res[4];
#pragma unroll
            for (int t = 0; t < 4; ++t) {
                float a = __builtin_amdgcn_fdot2_f32_bf16(__builtin_bit_cast(v2bf, P[t + 3]), __builtin_bit_cast(v2bf, w01), 0.0f, false);
                a = __builtin_amdgcn_fdot2_f32_bf16(__builtin_bit_cast(v2bf, P[t + 1]), __builtin_bit_cast(v2bf, w23), a, false);
                res[t] = a * (c ? bf_hi(vd[t]) : bf_lo(vd[t]));
            }
            typedef u32 v2u __attribute__((ext_vector_type(2)));
            const int half = (1 - (rb & 1)) ^ ((cp >> 3) & 1);
            *(v2u*)(lds + L_T + (2 * cp + c) * 128 + (((rb >> 1) ^ (cp & 7)) * 16) + half * 8) = (v2u){pack_bf(res[3], res[2]), pack_bf(res[1], res[0])};
        }
    }
    SCHED_FENCE();
    u32 pp[11], dA, dB;
    if (w >= 4) {
        const int pc = 2 * me + ((w - 4) >> 1), pi = pc * NS_ + (i & 127), pj = pi ^ 64;
        pp[0] = Are_[pi]; pp[1] = Aim_[pi]; pp[2] = Bre_[pi]; pp[3] = Bim_[pi]; pp[4] = Cre_[pi]; pp[5] = Cim_[pi];
        pp[6] = Bre_[pj]; pp[7] = Bim_[pj]; pp[8] = Cre_[pj]; pp[9] = Cim_[pj];
        pp[10] = logdt_[pc];
        dA = ssmD_[2 * me + (l & 1)]; dB = Dskip_[2 * me + (l & 1)];
        if (w >= 6) {
#pragma unroll
            for (int j = 0; j < 4; ++j) xq[j] = *(const v4u*)(q_ + (8 * myr + 4 * (w & 1) + j) * D_ + l * 8);
        }
    }
    STAMP(2);
    __syncthreads();
    STAMP(3);

    v4u A[8][4];
    if (w < 4) {
#pragma unroll
        for (int j = 0; j < 2; ++j) {
            typedef u32 v2u __attribute__((ext_vector_type(2)));
            const int e = i + 256 * j, ch = e >> 3, chunk = e & 7, sw = (ch >> 4) & 1;
            const unsigned char* src = lds + L_T + ch * 128 + ((chunk ^ ((ch >> 1) & 7)) * 16);
            const v2u d0 = *(const v2u*)(src + 8 * sw), d1 = *(const v2u*)(src + 8 * (1 - sw));
            store16((v4u){d0.x, d0.y, d1.x, d1.y}, rs, (64 * xcd + ch) * KV_PITCH + (8 * idx + chunk) * 16, WS_KV);
        }
        SCHED_FENCE();
        asm volatile("" ::: "memory");
        v4u sk[2];
        u32 sknext[2];
#pragma unroll
        for (int j = 0; j < 2; ++j) {
            sk[j] = *(const v4u*)(ssmk_ + (2 * me + j) * S_ + i * 8);
            sknext[j] = *(const u32*)(ssmk_ + (2 * me + j) * S_ + (i < 255 ? i * 8 + 8 : 0));
        }
        SCHED_FENCE();
        asm volatile("s_waitcnt vmcnt(4)" ::: "memory");
        lds_flag_inc(SY_KV);
        STAMP(4);
        u32 tag = 0;
        if (w == 0) {
            lds_wait_ge(lds, SY_KV, 4);
            tag = __builtin_amdgcn_readfirstlane(epoch) + 1u;
            if (l == 0) __builtin_amdgcn_raw_buffer_store_b32(tag, rs, me * 128, WS_FLAGS, 16);
            lds_flag_set(SY_TAG, tag);
            lds_flag_set(SY_TAGSET, 1u);
        }
#pragma unroll
        for (int j = 0; j < 2; ++j) {
            unsigned char* c0 = lds + L_KA + j * KC_CH;
            const u32 nx = i < 255 ? sknext[j] : 0u;
            *(v4u*)(c0 + 32 + i * 16) = sk[j];
            *(v4u*)(c0 + KC_COPY + 32 + i * 16) =
                (v4u){__builtin_amdgcn_alignbit(sk[j].y, sk[j].x, 16), __builtin_amdgcn_alignbit(sk[j].z, sk[j].y, 16),
                      __builtin_amdgcn_alignbit(sk[j].w, sk[j].z, 16), __builtin_amdgcn_alignbit(nx, sk[j].w, 16)};
            if (i == 0) *(u32*)(c0 + KC_COPY + 28) = sk[j].x << 16;
        }
        lds_flag_inc(SY_KA);
        lds_wait_ge_slow(lds, SY_KA, 4);
        load_afrags(A, lds, L_KA, w >> 1, w & 1, l);
        STAMP(5);
        if (w == 0) {
            for (u32 it = 0;; ++it) {
                asm volatile("" ::: "memory");
                const u32 f = __builtin_amdgcn_raw_buffer_load_b32(rs, (xcd * 32 + (l & 31)) * 128, WS_FLAGS, 16);
                if (__all(f == tag)) break;
                if (spin_give_up(lds, it * 4 + 3, GLB_SPIN_CAP * 4)) break;
            }
            lds_flag_set(SY_GO, 1u);
        } else {
            lds_wait_ge_slow(lds, SY_GO, 1u);
        }
        STAMP(6);
        v4u g0, g1;
        {
            const u32 off = (2 * me + (l >> 5)) * KV_PITCH + (64 * w + (l & 31)) * 16;
            g0 = __builtin_amdgcn_raw_buffer_load_b128(rs, off, WS_KV, 16);
            g1 = __builtin_amdgcn_raw_buffer_load_b128(rs, off + 512, WS_KV, 16);
        }
        unsigned char* dst = lds + L_UR + (l >> 5) * UR_PITCH + 2 * (2040 - 8 * (64 * w + (l & 31)));
        *(v4u*)dst = g0;
        *(v4u*)(dst - 512) = g1;
        lds_flag_set(SY_INTILE + 8 * w, 1u);
        lds_flag_set(SY_INTILE + 8 * w + 4, 1u);

    } else {
        __builtin_amdgcn_s_setprio(2);
        const int wa = w - 4, ch = wa >> 1, n = (i - 256) & 127;
#pragma unroll
        for (int j = 0; j < 11; ++j) asm volatile("" : "+v"(pp[j]));
        asm volatile("" : "+v"(dA), "+v"(dB));
        const float ex = expf(f_of(pp[10] << 16));
        const float dt = bf_lo(pack_bf(ex, ex));
        const float al = dt * f_of(pp[0] << 16) * 1.44269504088896341f, be = dt * f_of(pp[1] << 16) * 0.15915494309189535f;
        v2f W;
        float inv;
        {
            const float br = dt * f_of(pp[2] << 16), bi = dt * f_of(pp[3] << 16), cr = f_of(pp[4] << 16), ci = f_of(pp[5] << 16);
            const float br2 = dt * f_of(pp[6] << 16), bi2 = dt * f_of(pp[7] << 16), cr2 = f_of(pp[8] << 16), ci2 = f_of(pp[9] << 16);
            W = (v2f){cr * br - ci * bi, cr * bi + ci * br};
            float mx = fmaxf(fmaxf(fabsf(W.x), fabsf(W.y)), fmaxf(fabsf(cr2 * br2 - ci2 * bi2), fabsf(cr2 * bi2 + ci2 * br2)));
            mx = fmaxf(mx, dppf<0x128>(mx));
            mx = fmaxf(mx, dppf<0x141>(mx));
            mx = fmaxf(mx, dppf<0xB1>(mx));
            mx = fmaxf(mx, dppf<0x4E>(mx));
            float m0 = f_of((u32)__builtin_amdgcn_readlane((int)u_of(mx), 0)), m1 = f_of((u32)__builtin_amdgcn_readlane((int)u_of(mx), 16));
            float m2 = f_of((u32)__builtin_amdgcn_readlane((int)u_of(mx), 32)), m3 = f_of((u32)__builtin_amdgcn_readlane((int)u_of(mx), 48));
            const int e = __builtin_amdgcn_frexp_expf(fmaxf(fmaxf(m0, m1), fmaxf(m2, m3)));
            W = (v2f){__builtin_amdgcn_ldexpf(W.x, 14 - e), __builtin_amdgcn_ldexpf(W.y, 14 - e)};
            inv = __builtin_amdgcn_ldexpf(1.0f, e - 14);
        }
        unsigned char* tab = lds + L_TAB + ch * TAB_CH + n * 4;
#define TAB_PUT(row, val) (*(u32*)(tab + (row) * TAB_ROW) = pack_h(val))
        const v2f E1 = cexp_pow(al, be, 1.0f), E8 = cexp_pow(al, be, 8.0f), E128 = cexp_pow(al, be, 128.0f);
        const v2f E256 = cmul(E128, E128), E384 = cmul(E256, E128), E512 = cmul(E256, E256), E1024 = cmul(E512, E512);
        {
            const v2f E8c = {E8.x, -E8.y};
            v2f B = {W.x, -W.y}, P = {1.0f, 0.0f}, Q = E512, Rr = E1024, P4 = cmul(cmul(E1, E1), cmul(E1, E1));
#pragma unroll
            for (int r = 0; r < 8; ++r) {
                TAB_PUT(41 + 2 * r, B);
                B = cmul(B, E8c);
                TAB_PUT(42 + 2 * r, B);
                if (r < 7) B = cmul(B, E8c);
                TAB_PUT(r, P);
                TAB_PUT(8 + r, r ? cmul(P, E128) : E128);
                if ((r & 1) == 0) {
                    TAB_PUT(16 + (r >> 1), r ? cmul(P, E256) : E256);
                    TAB_PUT(20 + (r >> 1), r ? cmul(P, E384) : E384);
                }
                if (r < 7) P = r ? cmul(P, E1) : E1;
                TAB_PUT(32 + r, Rr);
                if (r < 7) Rr = cmul(Rr, E128);
                if (r < 4) {
                    TAB_PUT(24 + 2 * r, Q);
                    TAB_PUT(25 + 2 * r, cmul(Q, P4));
                    if (r < 3) Q = cmul(Q, E128);
                }
            }
            TAB_PUT(40, cmul(E1024, E1024));
        }
#undef TAB_PUT
        if (l == 0) *(float*)(lds + L_DCO + 16 + ch * 4) = inv;
        lds_flag_inc(SY_TAB + 4 * ch);
        if (l < 2) { *(u32*)(lds + L_DCO + l * 4) = dA << 16; *(u32*)(lds + L_DCO + 8 + l * 4) = dB << 16; }
        STAMP(4);
        if (w < 6) {
            const int kc = w & 1;
            lds_wait_ge(lds, SY_TAB + 4 * kc, 2);
            {
                const int row = l & 15, g = l >> 4;
                const unsigned char* tb = lds + L_TAB + kc * TAB_CH + row * TAB_ROW + g * 16;
                float* kt = (float*)(lds + L_KT + kc * KT_CH);
                const float scale = *(const float*)(lds + L_DCO + 16 + kc * 4);
                v4u fb[8], fa[8];
                v4f acc[3];
#pragma unroll
                for (int s2 = 0; s2 < 8; ++s2) fb[s2] = *(const v4u*)(tb + 41 * TAB_ROW + s2 * 64);
#pragma unroll
                for (int tile = 0; tile < 3; ++tile) {
#pragma unroll
                    for (int s2 = 0; s2 < 8; ++s2) fa[s2] = *(const v4u*)(tb + tile * 16 * TAB_ROW + s2 * 64);
                    acc[tile] = (v4f){0, 0, 0, 0};
#pragma unroll
                    for (int s2 = 0; s2 < 8; ++s2)
                        acc[tile] = __builtin_amdgcn_mfma_f32_16x16x32_f16(__builtin_bit_cast(v8h, fa[s2]), __builtin_bit_cast(v8h, fb[s2]), acc[tile], 0, 0, 0);
                }
                asm volatile("s_nop 7\n\ts_nop 1" : "+v"(acc[0]), "+v"(acc[1]), "+v"(acc[2]));
#pragma unroll
                for (int r = 0; r < 4; ++r) {
                    const int rw = 4 * g + r;
                    kt[128 * (rw >> 3) + (rw & 7) + 8 * row] = vmul(acc[0][r], scale);
                    kt[rw < 8 ? 256 + 64 * (rw >> 2) + (rw & 3) + 4 * row : 384 + 32 * ((rw - 8) >> 1) + (rw & 1) + 2 * row] = vmul(acc[1][r], scale);
                    kt[rw < 8 ? 512 + 16 * rw + row : ((rw == 8 && row == 0) ? 640 : 644 + (l & 3))] = vmul(acc[2][r], scale);
                }
            }
            asm volatile("s_waitcnt lgkmcnt(0)" ::: "memory");
            {
                const float* t = (const float*)(lds + L_KT + kc * KT_CH);
                unsigned char* c0 = lds + L_KB + kc * KC_CH;
                const float a2 = t[512 + l], b2 = t[513 + l], a3 = t[576 + l], b3 = t[577 + l];
                const float v0 = t[384 + 2 * l], v1 = t[385 + 2 * l], v2 = t[386 + 2 * l];
                float kvv[9];
#pragma unroll
                for (int j = 0; j < 9; ++j) {
                    const int lag = 8 * l + j;
                    const u32 bb = pack_bf((float)lag, 0.0f) & 0xFFFFu;
                    kvv[j] = t[l < 32 ? lag : (int)(bb - 0x4280u)];
                }
                {
                    const u32 e0 = pack_bf(kvv[0], kvv[1]);
                    *(v4u*)(c0 + 32 + l * 16) = (v4u){e0, pack_bf(kvv[2], kvv[3]), pack_bf(kvv[4], kvv[5]), pack_bf(kvv[6], kvv[7])};
                    *(v4u*)(c0 + KC_COPY + 32 + l * 16) = (v4u){pack_bf(kvv[1], kvv[2]), pack_bf(kvv[3], kvv[4]), pack_bf(kvv[5], kvv[6]), pack_bf(kvv[7], kvv[8])};
                    if (l == 0) *(u32*)(c0 + KC_COPY + 28) = e0 << 16;
                }
                {
                    const u32 p00 = pack_bf(v0, v0), p01 = pack_bf(v0, v1), p11 = pack_bf(v1, v1), p12 = pack_bf(v1, v2), p22 = pack_bf(v2, v2);
                    *(v4u*)(c0 + 32 + (64 + l) * 16) = (v4u){p00, p01, p11, p22};
                    *(v4u*)(c0 + KC_COPY + 32 + (64 + l) * 16) = (v4u){p00, p11, p12, p22};
                }
#pragma unroll
                for (int it = 0; it < 2; ++it) {
                    const float a = it ? a3 : a2, b = it ? b3 : b2, c4 = (l & 1) ? b : a;
                    const u32 paa = pack_bf(a, a), pbb = pack_bf(b, b);
                    *(v4u*)(c0 + 32 + (128 + 64 * it + l) * 16) = (v4u){paa, paa, pack_bf(c4, b), pbb};
                    *(v4u*)(c0 + KC_COPY + 32 + (128 + 64 * it + l) * 16) = (v4u){paa, pack_bf(a, c4), pbb, pbb};
                }
            }
            lds_flag_inc(SY_KREADY);
            STAMP(5);
            if (w == 4) {
                lds_wait_ge(lds, SY_TAGSET, 1u);
                const u32 tag = __builtin_amdgcn_readfirstlane(lds_ld(lds, SY_TAG));
                for (u32 it = 0;; ++it) {
                    asm volatile("" ::: "memory");
                    const u32 f0 = __builtin_amdgcn_raw_buffer_load_b32(rs, l * 128, WS_FLAGP, 16);
                    const u32 f1 = __builtin_amdgcn_raw_buffer_load_b32(rs, (l + 64) * 128, WS_FLAGP, 16);
                    const u32 f2 = __builtin_amdgcn_raw_buffer_load_b32(rs, (l + 128) * 128, WS_FLAGP, 16);
                    const u32 f3 = __builtin_amdgcn_raw_buffer_load_b32(rs, (l + 192) * 128, WS_FLAGP, 16);
                    if (__all(f0 == tag && f1 == tag && f2 == tag && f3 == tag)) break;
                    if (spin_give_up(lds, it * 4 + 3, GLB_SPIN_CAP * 4)) break;
                }
                if (me == 0 && l == 0) __builtin_amdgcn_raw_buffer_store_b32(tag, rs, 0, WS_EPOCH, 16);
                lds_flag_set(SY_ALLF, 1u);
            }
        } else {
            const int kr = w & 1;
            lds_wait_ge(lds, SY_KV, 4);
#pragma unroll
            for (int j = 0; j < 4; ++j) {
                const int e = (i & 127) + 128 * j;
                store16(poison4, rs, (e >> 3) * C_PITCH + me * 128 + (e & 7) * 16, WS_C);
                store16(poison4, rs, myr * U_PITCH + e * 16, WS_U);
            }
            STAMP(5);
#pragma unroll
            for (int j = 0; j < 4; ++j) asm volatile("" : "+v"(xq[j]));
            {
                float f[4][8], mean[4], rstd[4];
#pragma unroll
                for (int rr = 0; rr < 4; ++rr) {
                    const v4u xr = xq[rr];
                    f[rr][0] = bf_lo(xr.x); f[rr][1] = bf_hi(xr.x); f[rr][2] = bf_lo(xr.y); f[rr][3] = bf_hi(xr.y);
                    f[rr][4] = bf_lo(xr.z); f[rr][5] = bf_hi(xr.z); f[rr][6] = bf_lo(xr.w); f[rr][7] = bf_hi(xr.w);
                    mean[rr] = vadd(vadd(vadd(f[rr][0], f[rr][1]), vadd(f[rr][2], f[rr][3])), vadd(vadd(f[rr][4], f[rr][5]), vadd(f[rr][6], f[rr][7])));
                }
#pragma unroll
                for (int rr = 0; rr < 4; ++rr) mean[rr] = vmul(wave_sum(mean[rr]), -1.0f / D_);
#pragma unroll
                for (int rr = 0; rr < 4; ++rr) {
#pragma unroll
                    for (int j = 0; j < 8; ++j) f[rr][j] = vadd(f[rr][j], mean[rr]);
                    float q2 = vmul(f[rr][0], f[rr][0]);
#pragma unroll
                    for (int j = 1; j < 8; ++j) q2 = vfma(f[rr][j], f[rr][j], q2);
                    rstd[rr] = q2;
                }
#pragma unroll
                for (int rr = 0; rr < 4; ++rr) rstd[rr] = __builtin_amdgcn_rsqf(vfma(wave_sum(rstd[rr]), 1.0f / D_, 1e-5f));
                asm volatile("s_nop 1" : "+v"(rstd[0]), "+v"(rstd[1]), "+v"(rstd[2]), "+v"(rstd[3]));
#pragma unroll
                for (int rr = 0; rr < 4; ++rr) {
                    const v4u xr = xq[rr];
                    u32 o[8];
#pragma unroll
                    for (int j = 0; j < 4; ++j) {
                        const u32 np = pack_bf(vmul(f[rr][2 * j], rstd[rr]), vmul(f[rr][2 * j + 1], rstd[rr]));
                        o[2 * j] = __builtin_amdgcn_perm(np, xr[j], 0x05040100u);
                        o[2 * j + 1] = __builtin_amdgcn_perm(np, xr[j], 0x07060302u);
                    }
                    unsigned char* dstx = lds + L_XN + (4 * kr + rr) * 2048 + l * 32;
                    *(v4u*)dstx = (v4u){o[0], o[1], o[2], o[3]};
                    *(v4u*)(dstx + 16) = (v4u){o[4], o[5], o[6], o[7]};
                    if (l == 0) *(float*)(lds + L_PIV + (4 * kr + rr) * 4) = -mean[rr];
                }
            }
            lds_flag_inc(SY_X0);
            STAMP(6);
            asm volatile("s_waitcnt vmcnt(0)" ::: "memory");
            STAMP(14);
            lds_flag_inc(SY_POIS);
            lds_wait_ge(lds, SY_POIS, 2);
            if (w == 6) {
                lds_wait_ge(lds, SY_TAGSET, 1u);
                const u32 tag = __builtin_amdgcn_readfirstlane(lds_ld(lds, SY_TAG));
                if (l == 0) __builtin_amdgcn_raw_buffer_store_b32(tag, rs, me * 128, WS_FLAGP, 16);
            }
        }
    }

    int redsel = 0;
    const int i_ = i;
    const __amdgpu_buffer_rsrc_t ro = __builtin_amdgcn_make_buffer_rsrc(out_, 0, S_ * D_ * 2, 0x00020000);

#pragma unroll 1
    for (int k = 0; k < 3; ++k) {
        int i = i_;
        asm volatile("" : "+v"(i));
        const int l = i & 63;
        STAMP(7 + 8 * k);
        if (w < 4) {
            const int ch = w >> 1, h = w & 1, a = l & 15, g = l >> 4;
            const unsigned char* ub = lds + L_UR + ch * UR_PITCH + 2 * (2032 + 128 * h - 16 * a + 8 * g) - 3584;
            const u32 ready = (u32)(k + 1);
            v4f acc[8];
#pragma unroll
            for (int c = 0; c < 8; ++c) acc[c] = (v4f){0, 0, 0, 0};
#pragma unroll
            for (int c = 0; c < 8; ++c) asm volatile("" : "+v"(acc[c]));
            v4u B[2][4];
            __builtin_amdgcn_s_setprio(1);
            lds_wait_ge(lds, SY_INTILE, ready);
            STAMP(8 + 8 * k);
            if (k == 0) STAMPC(29);
#pragma unroll
            for (int r = 0; r < 4; ++r) B[0][r] = *(const v4u*)(ub + 512 * 7 + 64 * r);
#define MFA "v_mfma_f32_16x16x32_bf16 "
#define MF1(cur, c, p, r)                                                                                     \
    asm volatile(MFA "%0, %1, %2, %0" : "+v"(acc[c]) : "v"(A[p][r]), "v"(B[cur][r]));                          \
    SCHED_FENCE();
#define MF4(cur, c, p)                                                                                        \
    asm volatile(MFA "%0, %1, %5, %0\n\t" MFA "%0, %2, %6, %0\n\t" MFA "%0, %3, %7, %0\n\t" MFA "%0, %4, %8, %0" \
                 : "+v"(acc[c]) : "v"(A[p][0]), "v"(A[p][1]), "v"(A[p][2]), "v"(A[p][3]),                      \
                   "v"(B[cur][0]), "v"(B[cur][1]), "v"(B[cur][2]), "v"(B[cur][3]));                             \
    SCHED_FENCE();
            u32 vflag = SY_PART + 4 * w, vready = ready, vpart = L_PART + ch * PART_CH + h * 1024 + 16 * (16 * g + ((a + 8 * (g >> 1)) & 15));
            asm volatile("" : "+v"(vflag), "+v"(vready), "+v"(vpart));
            u32 fl = 0;
            MF1(0, 0, 0, 0)
            fl = lds_ld(lds, SY_INTILE + 4); SCHED_FENCE();
            MF1(0, 0, 0, 1)
            B[1][0] = *(const v4u*)(ub + 3072); SCHED_FENCE();
            MF1(0, 0, 0, 2)
            B[1][1] = *(const v4u*)(ub + 3136); SCHED_FENCE();
            MF1(0, 0, 0, 3)
            B[1][2] = *(const v4u*)(ub + 3200); SCHED_FENCE();
            MF1(0, 1, 1, 0)
            B[1][3] = *(const v4u*)(ub + 3264); SCHED_FENCE();
            MF1(0, 1, 1, 1)
            MF1(0, 1, 1, 2)
            put_partial<0>(vpart, acc[0]); SCHED_FENCE();
            MF1(0, 1, 1, 3)
            MF1(0, 2, 2, 0)
            put_partial_flag<0>(vflag, vready); SCHED_FENCE();
            MF1(0, 2, 2, 1)
            const u32 fls0 = (u32)__builtin_amdgcn_readfirstlane(fl); SCHED_FENCE();
            MF1(0, 2, 2, 2)
            MF1(0, 2, 2, 3)
            MF4(0, 3, 3)
            MF4(0, 4, 4)
            MF4(0, 5, 5)
            MF4(0, 6, 6)
            MF4(0, 7, 7)
            if (__builtin_expect(fls0 < ready, 0)) {
                lds_wait_ge(lds, SY_INTILE + 4, ready);
#pragma unroll
                for (int r = 0; r < 4; ++r) B[1][r] = *(const v4u*)(ub + 3072 + 64 * r);
            }
            asm volatile("" ::: "memory");
            SCHED_FENCE();
            MF1(1, 1, 0, 0)
            fl = lds_ld(lds, SY_INTILE + 8); SCHED_FENCE();
            MF1(1, 1, 0, 1)
            B[0][0] = *(const v4u*)(ub + 2560); SCHED_FENCE();
            MF1(1, 1, 0, 2)
            B[0][1] = *(const v4u*)(ub + 2624); SCHED_FENCE();
            MF1(1, 1, 0, 3)
            B[0][2] = *(const v4u*)(ub + 2688); SCHED_FENCE();
            MF1(1, 2, 1, 0)
            B[0][3] = *(const v4u*)(ub + 2752); SCHED_FENCE();
            MF1(1, 2, 1, 1)
            MF1(1, 2, 1, 2)
            put_partial<1>(vpart, acc[1]); SCHED_FENCE();
            MF1(1, 2, 1, 3)
            MF1(1, 3, 2, 0)
            put_partial_flag<1>(vflag, vready); SCHED_FENCE();
            MF1(1, 3, 2, 1)
            const u32 fls1 = (u32)__builtin_amdgcn_readfirstlane(fl); SCHED_FENCE();
            MF1(1, 3, 2, 2)
            MF1(1, 3, 2, 3)
            MF4(1, 4, 3)
            MF4(1, 5, 4)
            MF4(1, 6, 5)
            MF4(1, 7, 6)
            if (__builtin_expect(fls1 < ready, 0)) {
                lds_wait_ge(lds, SY_INTILE + 8, ready);
#pragma unroll
                for (int r = 0; r < 4; ++r) B[0][r] = *(const v4u*)(ub + 2560 + 64 * r);
            }
            asm volatile("" ::: "memory");
            SCHED_FENCE();
            MF1(0, 2, 0, 0)
            fl = lds_ld(lds, SY_INTILE + 12); SCHED_FENCE();
            MF1(0, 2, 0, 1)
            B[1][0] = *(const v4u*)(ub + 2048); SCHED_FENCE();
            MF1(0, 2, 0, 2)
            B[1][1] = *(const v4u*)(ub + 2112); SCHED_FENCE();
            MF1(0, 2, 0, 3)
            B[1][2] = *(const v4u*)(ub + 2176); SCHED_FENCE();
            MF1(0, 3, 1, 0)
            B[1][3] = *(const v4u*)(ub + 2240); SCHED_FENCE();
            MF1(0, 3, 1, 1)
            MF1(0, 3, 1, 2)
            put_partial<2>(vpart, acc[2]); SCHED_FENCE();
            MF1(0, 3, 1, 3)
            MF1(0, 4, 2, 0)
            put_partial_flag<2>(vflag, vready); SCHED_FENCE();
            MF1(0, 4, 2, 1)
            const u32 fls2 = (u32)__builtin_amdgcn_readfirstlane(fl); SCHED_FENCE();
            MF1(0, 4, 2, 2)
            MF1(0, 4, 2, 3)
            MF4(0, 5, 3)
            MF4(0, 6, 4)
            MF4(0, 7, 5)
            if (__builtin_expect(fls2 < ready, 0)) {
                lds_wait_ge(lds, SY_INTILE + 12, ready);
#pragma unroll
                for (int r = 0; r < 4; ++r) B[1][r] = *(const v4u*)(ub + 2048 + 64 * r);
            }
            asm volatile("" ::: "memory");
            SCHED_FENCE();
            MF1(1, 3, 0, 0)
            fl = lds_ld(lds, SY_INTILE + 16); SCHED_FENCE();
            MF1(1, 3, 0, 1)
            B[0][0] = *(const v4u*)(ub + 1536); SCHED_FENCE();
            MF1(1, 3, 0, 2)
            B[0][1] = *(const v4u*)(ub + 1600); SCHED_FENCE();
            MF1(1, 3, 0, 3)
            B[0][2] = *(const v4u*)(ub + 1664); SCHED_FENCE();
            MF1(1, 4, 1, 0)
            B[0][3] = *(const v4u*)(ub + 1728); SCHED_FENCE();
            MF1(1, 4, 1, 1)
            MF1(1, 4, 1, 2)
            put_partial<3>(vpart, acc[3]); SCHED_FENCE();
            MF1(1, 4, 1, 3)
            MF1(1, 5, 2, 0)
            put_partial_flag<3>(vflag, vready); SCHED_FENCE();
            MF1(1, 5, 2, 1)
            const u32 fls3 = (u32)__builtin_amdgcn_readfirstlane(fl); SCHED_FENCE();
            MF1(1, 5, 2, 2)
            MF1(1, 5, 2, 3)
            MF4(1, 6, 3)
            MF4(1, 7, 4)
            if (__builtin_expect(fls3 < ready, 0)) {
                lds_wait_ge(lds, SY_INTILE + 16, ready);
#pragma unroll
                for (int r = 0; r < 4; ++r) B[0][r] = *(const v4u*)(ub + 1536 + 64 * r);
            }
            asm volatile("" ::: "memory");
            SCHED_FENCE();
            MF1(0, 4, 0, 0)
            fl = lds_ld(lds, SY_INTILE + 20); SCHED_FENCE();
            MF1(0, 4, 0, 1)
            B[1][0] = *(const v4u*)(ub + 1024); SCHED_FENCE();
            MF1(0, 4, 0, 2)
            B[1][1] = *(const v4u*)(ub + 1088); SCHED_FENCE();
            MF1(0, 4, 0, 3)
            B[1][2] = *(const v4u*)(ub + 1152); SCHED_FENCE();
            MF1(0, 5, 1, 0)
            B[1][3] = *(const v4u*)(ub + 1216); SCHED_FENCE();
            MF1(0, 5, 1, 1)
            MF1(0, 5, 1, 2)
            put_partial<4>(vpart, acc[4]); SCHED_FENCE();
            MF1(0, 5, 1, 3)
            MF1(0, 6, 2, 0)
            put_partial_flag<4>(vflag, vready); SCHED_FENCE();
            MF1(0, 6, 2, 1)
            const u32 fls4 = (u32)__builtin_amdgcn_readfirstlane(fl); SCHED_FENCE();
            MF1(0, 6, 2, 2)
            MF1(0, 6, 2, 3)
            MF4(0, 7, 3)
            if (__builtin_expect(fls4 < ready, 0)) {
                lds_wait_ge(lds, SY_INTILE + 20, ready);
#pragma unroll
                for (int r = 0; r < 4; ++r) B[1][r] = *(const v4u*)(ub + 1024 + 64 * r);
            }
            asm volatile("" ::: "memory");
            SCHED_FENCE();
            MF1(1, 5, 0, 0)
            fl = lds_ld(lds, SY_INTILE + 24); SCHED_FENCE();
            MF1(1, 5, 0, 1)
            B[0][0] = *(const v4u*)(ub + 512); SCHED_FENCE();
            MF1(1, 5, 0, 2)
            B[0][1] = *(const v4u*)(ub + 576); SCHED_FENCE();
            MF1(1, 5, 0, 3)
            B[0][2] = *(const v4u*)(ub + 640); SCHED_FENCE();
            MF1(1, 6, 1, 0)
            B[0][3] = *(const v4u*)(ub + 704); SCHED_FENCE();
            MF1(1, 6, 1, 1)
            MF1(1, 6, 1, 2)
            put_partial<5>(vpart, acc[5]); SCHED_FENCE();
            MF1(1, 6, 1, 3)
            MF1(1, 7, 2, 0)
            put_partial_flag<5>(vflag, vready); SCHED_FENCE();
            MF1(1, 7, 2, 1)
            const u32 fls5 = (u32)__builtin_amdgcn_readfirstlane(fl); SCHED_FENCE();
            MF1(1, 7, 2, 2)
            MF1(1, 7, 2, 3)
            if (__builtin_expect(fls5 < ready, 0)) {
                lds_wait_ge(lds, SY_INTILE + 24, ready);
#pragma unroll
                for (int r = 0; r < 4; ++r) B[0][r] = *(const v4u*)(ub + 512 + 64 * r);
            }
            asm volatile("" ::: "memory");
            SCHED_FENCE();
            MF1(0, 6, 0, 0)
            fl = lds_ld(lds, SY_INTILE + 28); SCHED_FENCE();
            MF1(0, 6, 0, 1)
            B[1][0] = *(const v4u*)(ub + 0); SCHED_FENCE();
            MF1(0, 6, 0, 2)
            B[1][1] = *(const v4u*)(ub + 64); SCHED_FENCE();
            MF1(0, 6, 0, 3)
            B[1][2] = *(const v4u*)(ub + 128); SCHED_FENCE();
            MF1(0, 7, 1, 0)
            B[1][3] = *(const v4u*)(ub + 192); SCHED_FENCE();
            MF1(0, 7, 1, 1)
            MF1(0, 7, 1, 2)
            put_partial<6>(vpart, acc[6]); SCHED_FENCE();
            MF1(0, 7, 1, 3)
            put_partial_flag<6>(vflag, vready); SCHED_FENCE();
            const u32 fls6 = (u32)__builtin_amdgcn_readfirstlane(fl); SCHED_FENCE();
            if (__builtin_expect(fls6 < ready, 0)) {
                lds_wait_ge(lds, SY_INTILE + 28, ready);
#pragma unroll
                for (int r = 0; r < 4; ++r) B[1][r] = *(const v4u*)(ub + 0 + 64 * r);
            }
            asm volatile("" ::: "memory");
            STAMP(9 + 8 * k); if (k == 0) STAMPC(30);
            SCHED_FENCE();
            MF4(1, 7, 0)
            asm volatile("s_nop 7" ::: "memory");
            put_partial<7>(vpart, acc[7]); put_partial_flag<7>(vflag, vready);
#undef MF1
#undef MF4
#undef MFA
            __builtin_amdgcn_s_setprio(0);
            if (k == 0) {
                lds_wait_ge_slow(lds, SY_KREADY, 2);
                load_afrags(A, lds, L_KB, ch, h, l);
            }
        } else if (w >= 6) {
            const int ch = l & 1, tl = 32 * (l >> 3) + 8 * ((l >> 1) & 3);
            const float dco = *(const float*)(lds + L_DCO + (k == 0 ? 0 : 8) + ch * 4);
            __builtin_amdgcn_s_setprio(3);
#pragma unroll 1
            for (int c = w & 1; c < 8; c += 2) {
                const int sb = (l >> 1) & 3, pa = 2 * (l >> 3) + (sb >> 1);
                const unsigned char* pp2 = lds + L_PART + c * PART_TILE + ch * PART_CH + 16 * (32 * (sb & 1) + ((pa + 8 * (sb & 1)) & 15));
                v4f a0, a1, b0, b1;
                v4u u;
                for (u32 it = 0;; ++it) {
                    asm volatile("" ::: "memory");
                    const v4u f = *(const v4u*)(lds + SY_PART + 16 * c);
                    asm volatile("" ::: "memory");
                    a0 = *(const v4f*)pp2; a1 = *(const v4f*)(pp2 + 256);
                    b0 = *(const v4f*)(pp2 + 1024); b1 = *(const v4f*)(pp2 + 1280);
                    u = *(const v4u*)(lds + L_UR + ch * UR_PITCH + 2 * (2040 - (256 * c + tl)));
                    const u32 fa = f.x < f.y ? f.x : f.y, fb = f.z < f.w ? f.z : f.w;
                    if ((u32)__builtin_amdgcn_readfirstlane(fa < fb ? fa : fb) >= (u32)(k + 1)) break;
                    if (spin_give_up(lds, it, LDS_SPIN_CAP)) break;
                }
                v4u o;
                o.x = no_poison(pack_bf(vfma(dco, bf_hi(u.w), vadd(a0.x, b0.x)), vfma(dco, bf_lo(u.w), vadd(a0.y, b0.y))));
                o.y = no_poison(pack_bf(vfma(dco, bf_hi(u.z), vadd(a0.z, b0.z)), vfma(dco, bf_lo(u.z), vadd(a0.w, b0.w))));
                o.z = no_poison(pack_bf(vfma(dco, bf_hi(u.y), vadd(a1.x, b1.x)), vfma(dco, bf_lo(u.y), vadd(a1.y, b1.y))));
                o.w = no_poison(pack_bf(vfma(dco, bf_hi(u.x), vadd(a1.z, b1.z)), vfma(dco, bf_lo(u.x), vadd(a1.w, b1.w))));
                store16(o, rs, (8 * c + (l >> 3)) * C_PITCH + me * 128 + (l & 7) * 16, WS_C + k * C_SIZE);
                if (c - 2 == mytile) { asm volatile("s_waitcnt vmcnt(1)" ::: "memory"); lds_flag_set(SY_PUB, (u32)(k + 1)); }
                if (c == mytile && c >= 6) { asm volatile("s_waitcnt vmcnt(0)" ::: "memory"); lds_flag_set(SY_PUB, (u32)(k + 1)); }
                if (c < 2) STAMP(8 + 8 * k);
                if (c >= 6) STAMP(9 + 8 * k);
            }
            __builtin_amdgcn_s_setprio(2);
        }

        asm volatile("" : "+v"(i));
        STAMP(10 + 8 * k);
        float x[8], y[8], n0[8], dm, rsd;
        float piv;
        v4u yap = {0, 0, 0, 0};
        if (k == 0) lds_wait_ge(lds, SY_ALLF, 1u);
        lds_wait_ge(lds, SY_PUB, (u32)(k + 1));
        STAMP(11 + 8 * k);
        const u32 cvoff = (myr >> 2) * C_PITCH + (i >> 1) * 128 + (myr & 3) * 32 + (i & 1) * 16, csoff = WS_C + k * C_SIZE;
        v4u cg = __builtin_amdgcn_raw_buffer_load_b128(rs, cvoff, csoff, 16);
        SCHED_FENCE();
        if (k == 0) {
            store16(poison4, rs, (i >> 3) * C_PITCH + me * 128 + (i & 7) * 16, WS_C + C_SIZE);
            store16(poison4, rs, myr * U_PITCH + i * 16, WS_U + U_SIZE);
            lds_wait_ge(lds, SY_X0, 2);
#pragma unroll
            for (int t = 0; t < 8; ++t) {
                const u32 xn = *(const u32*)(lds + L_XN + t * 2048 + i * 4);
                x[t] = bf_lo(xn);
                n0[t] = bf_hi(xn);
            }
            piv = *(const float*)(lds + L_PIV + (l & 7) * 4);
        } else {
            if (k == 1) store16(poison4, rs, (i >> 3) * C_PITCH + me * 128 + (i & 7) * 16, WS_C + 2 * C_SIZE);
            const v4f xa = *(const v4f*)(lds + L_PARK + i * 16), xb = *(const v4f*)(lds + L_PARK + 8192 + i * 16);
            x[0] = xa.x; x[1] = xa.y; x[2] = xa.z; x[3] = xa.w; x[4] = xb.x; x[5] = xb.y; x[6] = xb.z; x[7] = xb.w;
            yap = *(const v4u*)(lds + L_PARK + 16384 + i * 16);
            piv = *(const float*)(lds + L_PARK + 24576 + i * 4);
        }
        v4u sel_lo, sel_hi;
        {
            const u32 one = 0x3F80u << (16 * (l & 1)), dsel = (l & 7) >> 1;
            const v4u pat = {dsel == 0 ? one : 0u, dsel == 1 ? one : 0u, dsel == 2 ? one : 0u, dsel == 3 ? one : 0u};
            const v4u zero = {0, 0, 0, 0};
            sel_lo = (l & 8) ? zero : pat;
            sel_hi = (l & 8) ? pat : zero;
        }
        {
            bool ok = valid16(cg);
            for (u32 it = 0; !__all(ok); ++it) {
                if (spin_give_up(lds, it * 4 + 3, GLB_SPIN_CAP * 4)) break;
                asm volatile("" ::: "memory");
                if (!ok) { cg = __builtin_amdgcn_raw_buffer_load_b128(rs, cvoff, csoff, 16); ok = valid16(cg); }
            }
        }
        STAMP(12 + 8 * k);
        const float c[8] = {bf_lo(cg.x), bf_hi(cg.x), bf_lo(cg.y), bf_hi(cg.y), bf_lo(cg.z), bf_hi(cg.z), bf_lo(cg.w), bf_hi(cg.w)};
        if (k == 2) {
#pragma unroll
            for (int t = 0; t < 8; t += 2) {
                const u32 o = pack_bf(x[t] + c[t], x[t + 1] + c[t + 1]);
                __builtin_amdgcn_raw_buffer_store_b16((u16)o, ro, i * 2 + t * (D_ * 2), myr * (8 * D_ * 2), 0);
                __builtin_amdgcn_raw_buffer_store_b16((u16)(o >> 16), ro, i * 2 + (t + 1) * (D_ * 2), myr * (8 * D_ * 2), 0);
            }
            break;
        }
        if (k == 0) {
#pragma unroll
            for (int t = 0; t < 8; ++t) x[t] = vfma(n0[t], c[t], x[t]);
            yap = cg;
        } else {
#pragma unroll
            for (int t = 0; t < 8; ++t) x[t] = vadd(x[t], c[t]);
            sub_rows(y, x, piv);
            row_stats(y, sel_lo, sel_hi, lds, L_RED + redsel * 512, w, l, dm, rsd);
            redsel ^= 1;
            norm_rows(y, rsd, dm * rsd);
            const float ya[8] = {bf_lo(yap.x), bf_hi(yap.x), bf_lo(yap.y), bf_hi(yap.y), bf_lo(yap.z), bf_hi(yap.z), bf_lo(yap.w), bf_hi(yap.w)};
#pragma unroll
            for (int t = 0; t < 8; ++t) x[t] = vfma(y[t], ya[t], x[t]);
            piv += dm;
        }
        sub_rows(y, x, piv);
        asm volatile("s_waitcnt vmcnt(0)" ::: "memory");
        row_stats(y, sel_lo, sel_hi, lds, L_RED + redsel * 512, w, l, dm, rsd);
        redsel ^= 1;
        norm_rows(y, rsd, dm * rsd);
        v4u uo;
        uo.x = no_poison(pack_bf(y[7], y[6]));
        uo.y = no_poison(pack_bf(y[5], y[4]));
        uo.z = no_poison(pack_bf(y[3], y[2]));
        uo.w = no_poison(pack_bf(y[1], y[0]));
        piv += dm;
        const bool gatherer = w == 4 || w == 5;
        const u32 gsoff = WS_U + k * U_SIZE;
        const u32 gvoff = (32 * (w & 1) + (l >> 1)) * U_PITCH + (2 * me + (l & 1)) * 16;
        v4u d[4];
#pragma unroll
        for (int t = 0; t < 4; ++t) {
            d[t] = poison4;
            if (gatherer && 2 * t + (w & 1) < mytile) d[t] = __builtin_amdgcn_raw_buffer_load_b128(rs, gvoff + t * 64 * U_PITCH, gsoff, 16);
        }
        SCHED_FENCE();
        store16(uo, rs, myr * U_PITCH + i * 16, WS_U + k * U_SIZE);
        *(v4f*)(lds + L_PARK + i * 16) = (v4f){x[0], x[1], x[2], x[3]};
        *(v4f*)(lds + L_PARK + 8192 + i * 16) = (v4f){x[4], x[5], x[6], x[7]};
        *(v4u*)(lds + L_PARK + 16384 + i * 16) = yap;
        *(float*)(lds + L_PARK + 24576 + i * 4) = piv;
        STAMP(13 + 8 * k);
        if (gatherer) {
            __builtin_amdgcn_s_setprio(3);
            unsigned char* dst = lds + L_UR + (l & 1) * UR_PITCH + 2 * (2040 - 8 * (32 * (w & 1) + (l >> 1)));
            bool waited = false;
#pragma unroll
            for (int t = 0; t < 4; ++t) {
                const int q = 2 * t + (w & 1);
                if (q >= mytile && !waited) {
                    waited = true;
                    asm volatile("s_waitcnt vmcnt(0)" ::: "memory");
#pragma unroll
                    for (int u = t; u < 4; ++u)
                        if (2 * u + (w & 1) <= mytile + GATHER_AHEAD) d[u] = __builtin_amdgcn_raw_buffer_load_b128(rs, gvoff + u * 64 * U_PITCH, gsoff, 16);
                }
                for (u32 it = 0; !__all(valid16(d[t])); ++it) {
                    if (spin_give_up(lds, it * 4 + 3, GLB_SPIN_CAP * 4)) break;
                    asm volatile("" ::: "memory");
                    if (!valid16(d[t])) d[t] = __builtin_amdgcn_raw_buffer_load_b128(rs, gvoff + t * 64 * U_PITCH, gsoff, 16);
                }
                *(v4u*)(dst - t * 1024) = d[t];
                lds_flag_set(SY_INTILE + 4 * q, (u32)(k + 2));
                if (t == 0) STAMP(16 + 8 * k);
                if (t == 3) STAMP(17 + 8 * k);
            }
            __builtin_amdgcn_s_setprio(2);
        }
    }
}

extern "C" void kernel_launch(void* const* d_in, const int* in_sizes, int n_in, void* d_out, int out_size, void* d_ws, size_t ws_size,
                              hipStream_t stream) {
    (void)in_sizes; (void)n_in; (void)out_size; (void)ws_size;
    h3s4_kernel<<<256, 512, 0, stream>>>(
        (const u16*)d_in[0], (const u16*)d_in[1], (const u16*)d_in[2], (const u16*)d_in[3], (const u16*)d_in[4], (const u16*)d_in[5],
        (const u16*)d_in[7], (const u16*)d_in[8], (const u16*)d_in[9], (const u16*)d_in[10], (const u16*)d_in[11], (const u16*)d_in[12],
        (const u16*)d_in[13], (const u16*)d_in[14], (u16*)d_out, (unsigned char*)d_ws);
}
```

```cpp
#include <hip/hip_runtime.h>
#include <stdint.h>
#include <type_traits>

typedef unsigned short u16;
typedef unsigned u32;
typedef u32 v4u __attribute__((ext_vector_type(4)));
typedef float v2f __attribute__((ext_vector_type(2)));
typedef float v4f __attribute__((ext_vector_type(4)));
typedef __bf16 v2bf __attribute__((ext_vector_type(2)));
typedef __bf16 v8bf __attribute__((ext_vector_type(8)));
typedef _Float16 v2h __attribute__((ext_vector_type(2)));
typedef _Float16 v8h __attribute__((ext_vector_type(8)));

constexpr int S_ = 2048, D_ = 512, NS_ = 128;
constexpr u32 POISON = 0xFFFFFFFFu;

constexpr u32 WS_EPOCH = 0;
constexpr u32 WS_FLAGS = 4096;
constexpr u32 KV_PITCH = 4096 + 128;
constexpr u32 WS_FLAGP = WS_FLAGS + 256 * 128;
constexpr u32 WS_KV = WS_FLAGP + 256 * 128 + 4096;
constexpr u32 C_PITCH = 32768 + 128;
constexpr u32 C_SIZE = 64 * C_PITCH;
constexpr u32 WS_C = WS_KV + 512 * KV_PITCH;
constexpr u32 U_PITCH = 8192 + 128;
constexpr u32 U_SIZE = 256 * U_PITCH;
constexpr u32 WS_U = WS_C + 3 * C_SIZE;
constexpr u32 WS_TOTAL = WS_U + 2 * U_SIZE;

constexpr int UR_PITCH = 4608;
constexpr int L_UR = 0;
constexpr int PART_CH = 2048 + 64;
constexpr int PART_TILE = 2 * PART_CH;
constexpr int L_PART = L_UR + 2 * UR_PITCH;
constexpr int KC_COPY = 4160;
constexpr int KC_CH = 2 * KC_COPY;
constexpr int L_KA = L_PART + 8 * PART_TILE;
constexpr int L_KB = L_KA + 2 * KC_CH;
constexpr int KT_CH = 648 * 4;
constexpr int L_KT = L_KB + 2 * KC_CH;
constexpr int TAB_ROW = 528;
constexpr int TAB_CH = 57 * TAB_ROW;
constexpr int L_TAB = L_KT + 2 * KT_CH;
constexpr int L_T = L_TAB + 2 * TAB_CH;
constexpr int L_XN = L_T;
constexpr int L_PARK = L_TAB;
constexpr int L_RED = L_T + 16384;
constexpr int L_SYNC = L_RED + 2 * 512;
constexpr int SY_PART = L_SYNC;
constexpr int SY_INTILE = L_SYNC + 128;
constexpr int SY_KV = L_SYNC + 160;
constexpr int SY_GO = L_SYNC + 164;
constexpr int SY_TAB = L_SYNC + 168;

constexpr int SY_KREADY = L_SYNC + 184;
constexpr int SY_ABORT = L_SYNC + 188;
constexpr int SY_TAG = L_SYNC + 192;
constexpr int SY_ALLF = L_SYNC + 196;
constexpr int SY_PUB = L_SYNC + 200;
constexpr int SY_X0 = L_SYNC + 204;
constexpr int SY_KA = L_SYNC + 176;
constexpr int SY_TAGSET = L_SYNC + 180;
constexpr int SY_POIS = L_SYNC + 164 + 44;
constexpr int L_DCO = L_SYNC + 224;
constexpr int L_PIV = L_DCO + 32;
constexpr int LDS_BYTES = L_PIV + 32;
static_assert(LDS_BYTES > 82 * 1024 && LDS_BYTES <= 160 * 1024, "one workgroup per CU");

#define GATHER_AHEAD 1
#define PUB_SLEEP 0
#define GATH_SLEEP 0
#define KLOAD_SPLIT 5
#define GATHER_LAG 2
#define GATHER_LOOK 0
#define CODE_PREFETCH 1
constexpr u32 LDS_SPIN_CAP = 1u << 19;
constexpr u32 GLB_SPIN_CAP = 1u << 16;

#define STAMP(n) do {} while (0)
#define STAMPC(n) do {} while (0)
#define SCHED_FENCE() __builtin_amdgcn_sched_barrier(0)
#define WAIT_VM0() do { asm volatile("" ::: "memory"); __builtin_amdgcn_s_waitcnt(0x0F70); asm volatile("" ::: "memory"); } while (0)

__device__ __forceinline__ float bf_lo(u32 x) { return __builtin_bit_cast(float, x << 16); }
__device__ __forceinline__ float bf_hi(u32 x) { return __builtin_bit_cast(float, x & 0xFFFF0000u); }
__device__ __forceinline__ float bf2f(u16 x) { return __builtin_bit_cast(float, ((u32)x) << 16); }
__device__ __forceinline__ u32 pack_bf(float lo, float hi) {
    v2f f = {lo, hi};
    return __builtin_bit_cast(u32, __builtin_convertvector(f, v2bf));
}
__device__ __forceinline__ u32 pack_h(v2f f) { return __builtin_bit_cast(u32, __builtin_convertvector(f, v2h)); }
__device__ __forceinline__ u32 no_poison(u32 x) { return x < 0xFFFFFFFEu ? x : 0xFFFFFFFEu; }
template <int CTRL> __device__ __forceinline__ float dppf(float x) {
    return __builtin_bit_cast(float, __builtin_amdgcn_mov_dpp(__builtin_bit_cast(int, x), CTRL, 0xf, 0xf, true));
}
template <int CTRL> __device__ __forceinline__ u32 dppu(u32 x) { return (u32)__builtin_amdgcn_mov_dpp((int)x, CTRL, 0xf, 0xf, true); }
__device__ __forceinline__ float f_of(u32 x) { return __builtin_bit_cast(float, x); }
__device__ __forceinline__ u32 u_of(float x) { return __builtin_bit_cast(u32, x); }

__device__ __forceinline__ u32 lds_ld(unsigned char* lds, int off) {
    return __hip_atomic_load((u32*)(lds + off), __ATOMIC_RELAXED, __HIP_MEMORY_SCOPE_WORKGROUP);
}
__device__ __forceinline__ void lds_st(unsigned char* lds, int off, u32 v) {
    __hip_atomic_store((u32*)(lds + off), v, __ATOMIC_RELAXED, __HIP_MEMORY_SCOPE_WORKGROUP);
}
__device__ __forceinline__ void lds_flag_set(int off, u32 v) {
    asm volatile("s_mov_b64 exec, 1\n\tds_write_b32 %0, %1\n\ts_mov_b64 exec, -1" ::"v"(off), "v"(v) : "memory");
}
template <int Q> __device__ __forceinline__ void put_partial(u32 vpart, const v4f& acc) {
    asm volatile("ds_write_b128 %0, %1 offset:%c2" ::"v"(vpart), "v"(acc), "i"(Q * PART_TILE) : "memory");
}
template <int Q> __device__ __forceinline__ void put_partial_flag(u32 vflag, u32 v) {
    asm volatile("ds_write_b32 %0, %1 offset:%c2" ::"v"(vflag), "v"(v), "i"(Q * 16) : "memory");
}
__device__ __forceinline__ void lds_flag_inc(int off) {
    asm volatile("s_mov_b64 exec, 1\n\tds_add_u32 %0, %1\n\ts_mov_b64 exec, -1" ::"v"(off), "v"(1u) : "memory");
}
__device__ __forceinline__ bool spin_give_up(unsigned char* lds, u32 it, u32 cap) {
    if ((it & 63u) != 63u) return false;
    if (it > cap) { lds_st(lds, SY_ABORT, 1u); return true; }
    return __builtin_amdgcn_readfirstlane(lds_ld(lds, SY_ABORT)) != 0;
}
__device__ __forceinline__ void lds_wait_ge(unsigned char* lds, int off, u32 target) {
    if (__builtin_expect((u32)__builtin_amdgcn_readfirstlane(lds_ld(lds, off)) < target, 0)) {
        for (u32 it = 0; (u32)__builtin_amdgcn_readfirstlane(lds_ld(lds, off)) < target; ++it)
            if (spin_give_up(lds, it, LDS_SPIN_CAP)) break;
    }
    asm volatile("" ::: "memory");
}
__device__ __forceinline__ void lds_wait_ge_slow(unsigned char* lds, int off, u32 target) {
    for (u32 it = 0; (u32)__builtin_amdgcn_readfirstlane(lds_ld(lds, off)) < target; ++it) {
        if (spin_give_up(lds, it, LDS_SPIN_CAP)) break;
        __builtin_amdgcn_s_sleep(1);
    }
    asm volatile("" ::: "memory");
}
__device__ __forceinline__ void lds_wait4_ge(unsigned char* lds, int off, u32 target) {
    for (u32 it = 0;; ++it) {
        asm volatile("" ::: "memory");
        const v4u f = *(const v4u*)(lds + off);
        const u32 a = f.x < f.y ? f.x : f.y, b = f.z < f.w ? f.z : f.w;
        if ((u32)__builtin_amdgcn_readfirstlane(a < b ? a : b) >= target) break;
        if (spin_give_up(lds, it, LDS_SPIN_CAP)) break;
    }
    asm volatile("" ::: "memory");
}
__device__ __forceinline__ void store16(v4u v, __amdgpu_buffer_rsrc_t rs, u32 voff, u32 soff) {
    __builtin_amdgcn_raw_buffer_store_b128(v, rs, voff + soff, 0, 16);
}
__device__ __forceinline__ bool valid16(v4u r) {
    u32 m = r.x > r.y ? r.x : r.y;
    m = m > r.z ? m : r.z;
    m = m > r.w ? m : r.w;
    return m != POISON;
}
__device__ __forceinline__ v4u poll16(__amdgpu_buffer_rsrc_t rs, u32 voff, u32 soff, unsigned char* lds) {
    v4u r = __builtin_amdgcn_raw_buffer_load_b128(rs, voff, soff, 16);
    bool ok = valid16(r);
    for (u32 it = 0; !__all(ok); ++it) {
        if (spin_give_up(lds, it * 4 + 3, GLB_SPIN_CAP * 4)) break;
        asm volatile("" ::: "memory");
        if (!ok) { r = __builtin_amdgcn_raw_buffer_load_b128(rs, voff, soff, 16); ok = valid16(r); }
    }
    return r;
}

__device__ __forceinline__ float vmul(float a, float b) {
    float r;
    asm("v_mul_f32 %0, %1, %2" : "=v"(r) : "v"(a), "v"(b));
    return r;
}
#define DPPB(t) " row_newbcast:" #t " row_mask:0xf bank_mask:0xf\n\t"
__device__ __forceinline__ void sub_rows(float (&y)[8], const float (&x)[8], float p) {
    asm("s_nop 1\n\t"
        "v_subrev_f32_dpp %0, %16, %8" DPPB(0) "v_subrev_f32_dpp %1, %16, %9" DPPB(1) "v_subrev_f32_dpp %2, %16, %10" DPPB(2)
        "v_subrev_f32_dpp %3, %16, %11" DPPB(3) "v_subrev_f32_dpp %4, %16, %12" DPPB(4) "v_subrev_f32_dpp %5, %16, %13" DPPB(5)
        "v_subrev_f32_dpp %6, %16, %14" DPPB(6) "v_subrev_f32_dpp %7, %16, %15" DPPB(7)
        : "=&v"(y[0]), "=&v"(y[1]), "=&v"(y[2]), "=&v"(y[3]), "=&v"(y[4]), "=&v"(y[5]), "=&v"(y[6]), "=&v"(y[7])
        : "v"(x[0]), "v"(x[1]), "v"(x[2]), "v"(x[3]), "v"(x[4]), "v"(x[5]), "v"(x[6]), "v"(x[7]), "v"(p));
}
__device__ __forceinline__ void norm_rows(float (&y)[8], float r, float m) {
    asm("s_nop 1\n\t"
        "v_mul_f32_dpp %0, %8, %0" DPPB(0) "v_mul_f32_dpp %1, %8, %1" DPPB(1) "v_mul_f32_dpp %2, %8, %2" DPPB(2) "v_mul_f32_dpp %3, %8, %3" DPPB(3)
        "v_mul_f32_dpp %4, %8, %4" DPPB(4) "v_mul_f32_dpp %5, %8, %5" DPPB(5) "v_mul_f32_dpp %6, %8, %6" DPPB(6) "v_mul_f32_dpp %7, %8, %7" DPPB(7)
        "v_subrev_f32_dpp %0, %9, %0" DPPB(0) "v_subrev_f32_dpp %1, %9, %1" DPPB(1) "v_subrev_f32_dpp %2, %9, %2" DPPB(2)
        "v_subrev_f32_dpp %3, %9, %3" DPPB(3) "v_subrev_f32_dpp %4, %9, %4" DPPB(4) "v_subrev_f32_dpp %5, %9, %5" DPPB(5)
        "v_subrev_f32_dpp %6, %9, %6" DPPB(6) "v_subrev_f32_dpp %7, %9, %7" DPPB(7)
        : "+v"(y[0]), "+v"(y[1]), "+v"(y[2]), "+v"(y[3]), "+v"(y[4]), "+v"(y[5]), "+v"(y[6]), "+v"(y[7])
        : "v"(r), "v"(m));
}
#undef DPPB
__device__ __forceinline__ float vfma(float a, float b, float c) {
    float r;
    asm("v_fma_f32 %0, %1, %2, %3" : "=v"(r) : "v"(a), "v"(b), "v"(c));
    return r;
}
__device__ __forceinline__ float vadd(float a, float b) {
    float r;
    asm("v_add_f32 %0, %1, %2" : "=v"(r) : "v"(a), "v"(b));
    return r;
}

template <class Hook>
__device__ __forceinline__ void row_stats(const float (&y)[8], const v4u& sel_lo, const v4u& sel_hi, unsigned char* lds, int redoff, int w, int l,
                                          float& dmean, float& rstd, Hook after_barrier) {
    const v4u a = {pack_bf(y[0], y[1]), pack_bf(y[2], y[3]), pack_bf(y[4], y[5]), pack_bf(y[6], y[7])};
    float q[8];
    asm("v_mul_f32 %0, %8, %8\n\tv_mul_f32 %1, %9, %9\n\tv_mul_f32 %2, %10, %10\n\tv_mul_f32 %3, %11, %11\n\t"
        "v_mul_f32 %4, %12, %12\n\tv_mul_f32 %5, %13, %13\n\tv_mul_f32 %6, %14, %14\n\tv_mul_f32 %7, %15, %15"
        : "=&v"(q[0]), "=&v"(q[1]), "=&v"(q[2]), "=&v"(q[3]), "=&v"(q[4]), "=&v"(q[5]), "=&v"(q[6]), "=&v"(q[7])
        : "v"(y[0]), "v"(y[1]), "v"(y[2]), "v"(y[3]), "v"(y[4]), "v"(y[5]), "v"(y[6]), "v"(y[7]));
    const v4u s = {pack_bf(q[0], q[1]), pack_bf(q[2], q[3]), pack_bf(q[4], q[5]), pack_bf(q[6], q[7])};
    v4f d = {0, 0, 0, 0};
    d = __builtin_amdgcn_mfma_f32_16x16x32_bf16(__builtin_bit_cast(v8bf, a), __builtin_bit_cast(v8bf, sel_lo), d, 0, 0, 0);
    d = __builtin_amdgcn_mfma_f32_16x16x32_bf16(__builtin_bit_cast(v8bf, s), __builtin_bit_cast(v8bf, sel_hi), d, 0, 0, 0);
    u32 t0 = u_of((d.x + d.y) + (d.z + d.w)), t1 = t0;
    {
        auto r = __builtin_amdgcn_permlane16_swap(t0, t1, false, false);
        const u32 r0 = r[0], r1 = r[1];
        t0 = u_of(f_of(r0) + f_of(r1)); t1 = t0;
    }
    {
        auto r = __builtin_amdgcn_permlane32_swap(t0, t1, false, false);
        const u32 r0 = r[0], r1 = r[1];
        t0 = u_of(f_of(r0) + f_of(r1));
    }
    *(u32*)(lds + redoff + (l & 15) * 32 + w * 4) = t0;
    __syncthreads();
    after_barrier();
    const v4f p0 = *(const v4f*)(lds + redoff + (l & 15) * 32), p1 = *(const v4f*)(lds + redoff + (l & 15) * 32 + 16);
    const v4f pp = p0 + p1;
    const float tot = (pp.x + pp.y) + (pp.z + pp.w);
    const float s2 = dppf<0x128>(tot);
    dmean = tot * (1.0f / D_);
    const float var = fmaxf(s2 * (1.0f / D_) - dmean * dmean, 0.0f);
    rstd = __builtin_amdgcn_rsqf(var + 1e-5f);
}
__device__ __forceinline__ float wave_sum(float v) {
    asm("s_nop 1" : "+v"(v));
    v += dppf<0x128>(v);
    v += dppf<0x141>(v);
    v += dppf<0xB1>(v);
    v += dppf<0x4E>(v);
    u32 a = u_of(v), b = a;
    {
        auto r = __builtin_amdgcn_permlane16_swap(a, b, false, false);
        const u32 r0 = r[0], r1 = r[1];
        a = u_of(f_of(r0) + f_of(r1)); b = a;
    }
    auto r = __builtin_amdgcn_permlane32_swap(a, b, false, false);
    const u32 r0 = r[0], r1 = r[1];
    return f_of(r0) + f_of(r1);
}

__device__ __forceinline__ void load_afrags(v4u (&A)[8][4], unsigned char* lds, int region, int ch, int h, int l) {
    const int b = l & 15, g = l >> 4, par = (1 + b) & 1;
    const unsigned char* base = lds + region + ch * KC_CH + par * KC_COPY + 256 * h + 16 * g + 2 * (1 + b - par);
#pragma unroll
    for (int p = 0; p < 8; ++p)
#pragma unroll
        for (int r = 0; r < 4; ++r) {
            const u32* q = (const u32*)(base + 512 * p + 64 * r);
            A[p][r] = (v4u){q[0], q[1], q[2], q[3]};
        }
}

__device__ __forceinline__ v2f cexp_pow(float al, float be, float m) {
    const float e = __builtin_amdgcn_exp2f(al * m);
    const float ph = __builtin_amdgcn_fractf(be * m);
    return (v2f){e * __builtin_amdgcn_cosf(ph), e * __builtin_amdgcn_sinf(ph)};
}
__device__ __forceinline__ v2f cmul(v2f a, v2f b) {
    v2f t;
    asm("v_pk_mul_f32 %0, %1, %2 op_sel:[0,0] op_sel_hi:[1,0]\n\t"
        "v_pk_fma_f32 %0, %1, %2, %0 op_sel:[1,1,0] op_sel_hi:[0,1,1] neg_lo:[1,0,0]"
        : "=&v"(t) : "v"(a), "v"(b));
    return t;
}
__device__ __forceinline__ v4f mfma_bf(v4u a, v4u b, v4f c) {
    return __builtin_amdgcn_mfma_f32_16x16x32_bf16(__builtin_bit_cast(v8bf, a), __builtin_bit_cast(v8bf, b), c, 0, 0, 0);
}

extern "C" __global__ void __launch_bounds__(512, 2)
h3s4_kernel(const u16* __restrict__ q_, const u16* __restrict__ k_, const u16* __restrict__ v_, const u16* __restrict__ shk_,
            const u16* __restrict__ ssmk_, const u16* __restrict__ ssmD_, const u16* __restrict__ Are_, const u16* __restrict__ Aim_,
            const u16* __restrict__ Bre_, const u16* __restrict__ Bim_, const u16* __restrict__ Cre_, const u16* __restrict__ Cim_,
            const u16* __restrict__ logdt_, const u16* __restrict__ Dskip_, u16* __restrict__ out_, unsigned char* ws_) {
    __shared__ __attribute__((aligned(16))) unsigned char lds[LDS_BYTES];
    asm volatile("" ::"s"(q_), "s"(k_), "s"(v_), "s"(shk_), "s"(ssmk_), "s"(ssmD_), "s"(Are_), "s"(Aim_), "s"(Bre_), "s"(Bim_),
                 "s"(Cre_), "s"(Cim_), "s"(logdt_), "s"(Dskip_), "s"(out_), "s"(ws_));
    const int i = threadIdx.x, l = i & 63;
    const int w = __builtin_amdgcn_readfirstlane(i >> 6);
    u32 code_touch = 0;
    if (w == 0) {
        unsigned long long pc;
        u32 left;
        asm volatile("s_getpc_b64 %0\n.Lh3s4_pc:\n\ts_mov_b32 %1, .Lh3s4_end-.Lh3s4_pc+1536" : "=s"(pc), "=s"(left));
        const u32 nl = (left >> 12) + 1, off = ((blockIdx.x >> 3) * nl + l) * 128;
        if ((u32)l < nl && off + 4 <= left) code_touch = *(const __attribute__((address_space(1))) u32*)((pc & ~3ull) + off);
    }
    const int xcd = blockIdx.x & 7, idx = blockIdx.x >> 3;
    const int me = xcd * 32 + idx;
    const int mytile = idx >> 2;
    const int myr = 32 * mytile + 4 * xcd + (idx & 3);
    const __amdgpu_buffer_rsrc_t rs = __builtin_amdgcn_make_buffer_rsrc(ws_, 0, WS_TOTAL, 0x00020000);
    const v4u poison4 = {POISON, POISON, POISON, POISON};
    STAMP(0);

    const int cp = i & 31, rb = i >> 5;
    const int pcg = (xcd + idx) & 7;
    const int R0 = 64 * idx + 4 * rb, pch = 64 * pcg + 2 * cp;
    u32 kd[7], vd[4];
#pragma unroll
    for (int j = 0; j < 7; ++j) {
        const int Rj = R0 - 3 + j;
        kd[j] = *(const u32*)(k_ + (Rj < 0 ? 0 : Rj) * D_ + pch);
    }
#pragma unroll
    for (int j = 0; j < 4; ++j) vd[j] = *(const u32*)(v_ + (R0 + j) * D_ + pch);
    const v4u wd = *(const v4u*)(shk_ + pch * 4);
    u32 epoch = 0;
    if (w == 0) epoch = __builtin_amdgcn_raw_buffer_load_b32(rs, 0, WS_EPOCH, 16);
    v4u xq[4];

    if (i >= 256 && i < 256 + 64) *(v4u*)(lds + L_UR + ((i >> 5) & 1) * UR_PITCH + 4096 + (i & 31) * 16) = (v4u){0, 0, 0, 0};
    if (i >= 320 && i < 320 + 16) {
        const int e = i - 320;
        *(v4u*)(lds + L_KA + (e >> 1) * KC_COPY + (e & 1) * 16) = (v4u){0, 0, 0, 0};
    }
    if (i >= 384 && i < 384 + 56) *(u32*)(lds + L_SYNC + (i - 384) * 4) = 0;

    STAMP(1);
    {
#pragma unroll
        for (int j = 0; j < 3; ++j) if (R0 - 3 + j < 0) kd[j] = 0;
#pragma unroll
        for (int c = 0; c < 2; ++c) {
            const u32 sel = c ? 0x07060302u : 0x05040100u;
            const u32 w01 = wd[2 * c], w23 = wd[2 * c + 1];
            u32 P[7];
#pragma unroll
            for (int j = 1; j < 7; ++j) P[j] = __builtin_amdgcn_perm(kd[j - 1], kd[j], sel);
            float res[4];
#pragma unroll
            for (int t = 0; t < 4; ++t) {
                float a = __builtin_amdgcn_fdot2_f32_bf16(__builtin_bit_cast(v2bf, P[t + 3]), __builtin_bit_cast(v2bf, w01), 0.0f, false);
                a = __builtin_amdgcn_fdot2_f32_bf16(__builtin_bit_cast(v2bf, P[t + 1]), __builtin_bit_cast(v2bf, w23), a, false);
                res[t] = a * (c ? bf_hi(vd[t]) : bf_lo(vd[t]));
            }
            typedef u32 v2u __attribute__((ext_vector_type(2)));
            const int half = (1 - (rb & 1)) ^ ((cp >> 3) & 1);
            *(v2u*)(lds + L_T + (2 * cp + c) * 128 + (((rb >> 1) ^ (cp & 7)) * 16) + half * 8) = (v2u){pack_bf(res[3], res[2]), pack_bf(res[1], res[0])};
        }
    }
    SCHED_FENCE();
    u32 pp[11], dA, dB;
    if (w >= 4) {
        const int pc = 2 * me + ((w - 4) >> 1), pi = pc * NS_ + (i & 127), pj = pi ^ 64;
        pp[0] = Are_[pi]; pp[1] = Aim_[pi]; pp[2] = Bre_[pi]; pp[3] = Bim_[pi]; pp[4] = Cre_[pi]; pp[5] = Cim_[pi];
        pp[6] = Bre_[pj]; pp[7] = Bim_[pj]; pp[8] = Cre_[pj]; pp[9] = Cim_[pj];
        pp[10] = logdt_[pc];
        dA = ssmD_[2 * me + (l & 1)]; dB = Dskip_[2 * me + (l & 1)];
        if (w >= 6) {
#pragma unroll
            for (int j = 0; j < 4; ++j) xq[j] = *(const v4u*)(q_ + (8 * myr + 4 * (w & 1) + j) * D_ + l * 8);
        }
    }
    STAMP(2);
    __syncthreads();
    STAMP(3);

    v4u A[8][4];
    if (w < 4) {
#pragma unroll
        for (int j = 0; j < 2; ++j) {
            typedef u32 v2u __attribute__((ext_vector_type(2)));
            const int e = i + 256 * j, ch = e >> 3, chunk = e & 7, sw = (ch >> 4) & 1;
            const unsigned char* src = lds + L_T + ch * 128 + ((chunk ^ ((ch >> 1) & 7)) * 16);
            const v2u d0 = *(const v2u*)(src + 8 * sw), d1 = *(const v2u*)(src + 8 * (1 - sw));
            store16((v4u){d0.x, d0.y, d1.x, d1.y}, rs, (64 * pcg + ch) * KV_PITCH + (8 * idx + chunk) * 16, WS_KV);
        }
        SCHED_FENCE();
        asm volatile("" ::: "memory");
        v4u sk[3];
        u32 sknext[3];
#pragma unroll
        for (int j = 0; j < 3; ++j) {
            const int e = (i - 64) + 192 * j, ec = (w == 0 || e > 511) ? 0 : e;
            const u16* src = ssmk_ + (2 * me + (ec >> 8)) * S_ + (ec & 255) * 8;
            if (w != 0) {
                sk[j] = *(const v4u*)src;
                sknext[j] = *(const u32*)(src + ((ec & 255) < 255 ? 8 : 0));
            }
        }
        SCHED_FENCE();
        if (w == 0) asm volatile("s_waitcnt vmcnt(0)" ::: "memory");
        else asm volatile("s_waitcnt vmcnt(6)" ::: "memory");
        lds_flag_inc(SY_KV);
        STAMP(4);
        u32 tag = 0;
        if (w == 0) {
            lds_wait_ge(lds, SY_KV, 4);
            tag = __builtin_amdgcn_readfirstlane(epoch) + 1u;
            if (l == 0) __builtin_amdgcn_raw_buffer_store_b32(tag, rs, me * 128, WS_FLAGS, 16);
            lds_flag_set(SY_TAG, tag);
            lds_flag_set(SY_TAGSET, 1u);
        }
        if (w != 0) {
#pragma unroll
            for (int j = 0; j < 3; ++j) {
                const int e = (i - 64) + 192 * j, sm = e & 255;
                if (e < 512) {
                    unsigned char* c0 = lds + L_KA + (e >> 8) * KC_CH;
                    const u32 nx = sm < 255 ? sknext[j] : 0u;
                    *(v4u*)(c0 + 32 + sm * 16) = sk[j];
                    *(v4u*)(c0 + KC_COPY + 32 + sm * 16) =
                        (v4u){__builtin_amdgcn_alignbit(sk[j].y, sk[j].x, 16), __builtin_amdgcn_alignbit(sk[j].z, sk[j].y, 16),
                              __builtin_amdgcn_alignbit(sk[j].w, sk[j].z, 16), __builtin_amdgcn_alignbit(nx, sk[j].w, 16)};
                    if (sm == 0) *(u32*)(c0 + KC_COPY + 28) = sk[j].x << 16;
                }
            }
            lds_flag_inc(SY_KA);
        }
        if (w == 0) {
            const u32 foff = (((xcd - (l & 31)) & 7) * 32 + (l & 31)) * 128;
            for (u32 it = 0;; ++it) {
                asm volatile("" ::: "memory");
                if (__all(__builtin_amdgcn_raw_buffer_load_b32(rs, foff, WS_FLAGS, 16) == tag)) break;
                if (spin_give_up(lds, it * 4 + 3, GLB_SPIN_CAP * 4)) break;
            }
            lds_flag_set(SY_GO, 1u);
        } else {
            lds_wait_ge(lds, SY_KA, 3);
            load_afrags(A, lds, L_KA, w >> 1, w & 1, l);
            lds_wait_ge(lds, SY_GO, 1u);
        }
        STAMP(5);
        v4u g0, g1;
        {
            const u32 off = (2 * me + (l >> 5)) * KV_PITCH + (64 * w + (l & 31)) * 16;
            g0 = __builtin_amdgcn_raw_buffer_load_b128(rs, off, WS_KV, 16);
            g1 = __builtin_amdgcn_raw_buffer_load_b128(rs, off + 512, WS_KV, 16);
        }
        SCHED_FENCE();
        if (w == 0) {
            lds_wait_ge(lds, SY_KA, 3);
            load_afrags(A, lds, L_KA, 0, 0, l);
        }
        STAMP(6);
        unsigned char* dst = lds + L_UR + (l >> 5) * UR_PITCH + 2 * (2040 - 8 * (64 * w + (l & 31)));
        *(v4u*)dst = g0;
        *(v4u*)(dst - 512) = g1;
        lds_flag_set(SY_INTILE + 8 * w, 1u);
        lds_flag_set(SY_INTILE + 8 * w + 4, 1u);
        WAIT_VM0();

    } else {
        __builtin_amdgcn_s_setprio(2);
        const int wa = w - 4, ch = wa >> 1, n = (i - 256) & 127;
#pragma unroll
        for (int j = 0; j < 11; ++j) asm volatile("" : "+v"(pp[j]));
        asm volatile("" : "+v"(dA), "+v"(dB));
        const float ex = expf(f_of(pp[10] << 16));
        const float dt = bf_lo(pack_bf(ex, ex));
        const float al = dt * f_of(pp[0] << 16) * 1.44269504088896341f, be = dt * f_of(pp[1] << 16) * 0.15915494309189535f;
        v2f W;
        float inv;
        {
            const float br = dt * f_of(pp[2] << 16), bi = dt * f_of(pp[3] << 16), cr = f_of(pp[4] << 16), ci = f_of(pp[5] << 16);
            const float br2 = dt * f_of(pp[6] << 16), bi2 = dt * f_of(pp[7] << 16), cr2 = f_of(pp[8] << 16), ci2 = f_of(pp[9] << 16);
            W = (v2f){cr * br - ci * bi, cr * bi + ci * br};
            float mx = fmaxf(fmaxf(fabsf(W.x), fabsf(W.y)), fmaxf(fabsf(cr2 * br2 - ci2 * bi2), fabsf(cr2 * bi2 + ci2 * br2)));
            mx = fmaxf(mx, dppf<0x128>(mx));
            mx = fmaxf(mx, dppf<0x141>(mx));
            mx = fmaxf(mx, dppf<0xB1>(mx));
            mx = fmaxf(mx, dppf<0x4E>(mx));
            float m0 = f_of((u32)__builtin_amdgcn_readlane((int)u_of(mx), 0)), m1 = f_of((u32)__builtin_amdgcn_readlane((int)u_of(mx), 16));
            float m2 = f_of((u32)__builtin_amdgcn_readlane((int)u_of(mx), 32)), m3 = f_of((u32)__builtin_amdgcn_readlane((int)u_of(mx), 48));
            const int e = __builtin_amdgcn_frexp_expf(fmaxf(fmaxf(m0, m1), fmaxf(m2, m3)));
            W = (v2f){__builtin_amdgcn_ldexpf(W.x, 14 - e), __builtin_amdgcn_ldexpf(W.y, 14 - e)};
            inv = __builtin_amdgcn_ldexpf(1.0f, e - 14);
        }
        unsigned char* tab = lds + L_TAB + ch * TAB_CH + n * 4;
#define TAB_PUT(row, val) (*(u32*)(tab + (row) * TAB_ROW) = pack_h(val))
        const v2f E1 = cexp_pow(al, be, 1.0f), E8 = cexp_pow(al, be, 8.0f), E128 = cexp_pow(al, be, 128.0f);
        const v2f E256 = cmul(E128, E128), E384 = cmul(E256, E128), E512 = cmul(E256, E256), E1024 = cmul(E512, E512);
        {
            const v2f E8c = {E8.x, -E8.y};
            v2f B = {W.x, -W.y}, P = {1.0f, 0.0f}, Q = E512, Rr = E1024, P4 = cmul(cmul(E1, E1), cmul(E1, E1));
#pragma unroll
            for (int r = 0; r < 8; ++r) {
                TAB_PUT(41 + 2 * r, B);
                B = cmul(B, E8c);
                TAB_PUT(42 + 2 * r, B);
                if (r < 7) B = cmul(B, E8c);
                TAB_PUT(r, P);
                TAB_PUT(8 + r, r ? cmul(P, E128) : E128);
                if ((r & 1) == 0) {
                    TAB_PUT(16 + (r >> 1), r ? cmul(P, E256) : E256);
                    TAB_PUT(20 + (r >> 1), r ? cmul(P, E384) : E384);
                }
                if (r < 7) P = r ? cmul(P, E1) : E1;
                TAB_PUT(32 + r, Rr);
                if (r < 7) Rr = cmul(Rr, E128);
                if (r < 4) {
                    TAB_PUT(24 + 2 * r, Q);
                    TAB_PUT(25 + 2 * r, cmul(Q, P4));
                    if (r < 3) Q = cmul(Q, E128);
                }
            }
            TAB_PUT(40, cmul(E1024, E1024));
        }
#undef TAB_PUT
        if (l == 0) *(float*)(lds + L_DCO + 16 + ch * 4) = inv;
        lds_flag_inc(SY_TAB + 4 * ch);
        if (l < 2) { *(u32*)(lds + L_DCO + l * 4) = dA << 16; *(u32*)(lds + L_DCO + 8 + l * 4) = dB << 16; }
        STAMP(4);
        if (w < 6) {
            const int kc = w & 1;
            lds_wait_ge(lds, SY_TAB + 4 * kc, 2);
            {
                const int row = l & 15, g = l >> 4;
                const unsigned char* tb = lds + L_TAB + kc * TAB_CH + row * TAB_ROW + g * 16;
                float* kt = (float*)(lds + L_KT + kc * KT_CH);
                const float scale = *(const float*)(lds + L_DCO + 16 + kc * 4);
                v4u fb[8], fa[8];
                v4f acc[3];
#pragma unroll
                for (int s2 = 0; s2 < 8; ++s2) fb[s2] = *(const v4u*)(tb + 41 * TAB_ROW + s2 * 64);
#pragma unroll
                for (int tile = 0; tile < 3; ++tile) {
#pragma unroll
                    for (int s2 = 0; s2 < 8; ++s2) fa[s2] = *(const v4u*)(tb + tile * 16 * TAB_ROW + s2 * 64);
                    acc[tile] = (v4f){0, 0, 0, 0};
#pragma unroll
                    for (int s2 = 0; s2 < 8; ++s2)
                        acc[tile] = __builtin_amdgcn_mfma_f32_16x16x32_f16(__builtin_bit_cast(v8h, fa[s2]), __builtin_bit_cast(v8h, fb[s2]), acc[tile], 0, 0, 0);
                }
                asm volatile("s_nop 7\n\ts_nop 1" : "+v"(acc[0]), "+v"(acc[1]), "+v"(acc[2]));
#pragma unroll
                for (int r = 0; r < 4; ++r) {
                    const int rw = 4 * g + r;
                    kt[128 * (rw >> 3) + (rw & 7) + 8 * row] = vmul(acc[0][r], scale);
                    kt[rw < 8 ? 256 + 64 * (rw >> 2) + (rw & 3) + 4 * row : 384 + 32 * ((rw - 8) >> 1) + (rw & 1) + 2 * row] = vmul(acc[1][r], scale);
                    kt[rw < 8 ? 512 + 16 * rw + row : ((rw == 8 && row == 0) ? 640 : 644 + (l & 3))] = vmul(acc[2][r], scale);
                }
            }
            asm volatile("s_waitcnt lgkmcnt(0)" ::: "memory");
            {
                const float* t = (const float*)(lds + L_KT + kc * KT_CH);
                unsigned char* c0 = lds + L_KB + kc * KC_CH;
                const float a2 = t[512 + l], b2 = t[513 + l], a3 = t[576 + l], b3 = t[577 + l];
                const float v0 = t[384 + 2 * l], v1 = t[385 + 2 * l], v2 = t[386 + 2 * l];
                float kvv[9];
#pragma unroll
                for (int j = 0; j < 9; ++j) {
                    const int lag = 8 * l + j;
                    const u32 bb = pack_bf((float)lag, 0.0f) & 0xFFFFu;
                    kvv[j] = t[l < 32 ? lag : (int)(bb - 0x4280u)];
                }
                {
                    const u32 e0 = pack_bf(kvv[0], kvv[1]);
                    *(v4u*)(c0 + 32 + l * 16) = (v4u){e0, pack_bf(kvv[2], kvv[3]), pack_bf(kvv[4], kvv[5]), pack_bf(kvv[6], kvv[7])};
                    *(v4u*)(c0 + KC_COPY + 32 + l * 16) = (v4u){pack_bf(kvv[1], kvv[2]), pack_bf(kvv[3], kvv[4]), pack_bf(kvv[5], kvv[6]), pack_bf(kvv[7], kvv[8])};
                    if (l == 0) *(u32*)(c0 + KC_COPY + 28) = e0 << 16;
                }
                {
                    const u32 p00 = pack_bf(v0, v0), p01 = pack_bf(v0, v1), p11 = pack_bf(v1, v1), p12 = pack_bf(v1, v2), p22 = pack_bf(v2, v2);
                    *(v4u*)(c0 + 32 + (64 + l) * 16) = (v4u){p00, p01, p11, p22};
                    *(v4u*)(c0 + KC_COPY + 32 + (64 + l) * 16) = (v4u){p00, p11, p12, p22};
                }
#pragma unroll
                for (int it = 0; it < 2; ++it) {
                    const float a = it ? a3 : a2, b = it ? b3 : b2, c4 = (l & 1) ? b : a;
                    const u32 paa = pack_bf(a, a), pbb = pack_bf(b, b);
                    *(v4u*)(c0 + 32 + (128 + 64 * it + l) * 16) = (v4u){paa, paa, pack_bf(c4, b), pbb};
                    *(v4u*)(c0 + KC_COPY + 32 + (128 + 64 * it + l) * 16) = (v4u){paa, pack_bf(a, c4), pbb, pbb};
                }
            }
            lds_flag_inc(SY_KREADY);
            STAMP(5);
            if (w == 4) {
                lds_wait_ge(lds, SY_TAGSET, 1u);
                const u32 tag = __builtin_amdgcn_readfirstlane(lds_ld(lds, SY_TAG));
                for (u32 it = 0;; ++it) {
                    asm volatile("" ::: "memory");
                    const u32 f0 = __builtin_amdgcn_raw_buffer_load_b32(rs, l * 128, WS_FLAGP, 16);
                    const u32 f1 = __builtin_amdgcn_raw_buffer_load_b32(rs, (l + 64) * 128, WS_FLAGP, 16);
                    const u32 f2 = __builtin_amdgcn_raw_buffer_load_b32(rs, (l + 128) * 128, WS_FLAGP, 16);
                    const u32 f3 = __builtin_amdgcn_raw_buffer_load_b32(rs, (l + 192) * 128, WS_FLAGP, 16);
                    if (__all(f0 == tag && f1 == tag && f2 == tag && f3 == tag)) break;
                    if (spin_give_up(lds, it * 4 + 3, GLB_SPIN_CAP * 4)) break;
                }
                if (me == 0 && l == 0) __builtin_amdgcn_raw_buffer_store_b32(tag, rs, 0, WS_EPOCH, 16);
                lds_flag_set(SY_ALLF, 1u);
            }
            WAIT_VM0();
        } else {
            const int kr = w & 1;
            lds_wait_ge(lds, SY_KV, 4);
#pragma unroll
            for (int j = 0; j < 4; ++j) {
                const int e = (i & 127) + 128 * j;
                store16(poison4, rs, (e >> 3) * C_PITCH + me * 128 + (e & 7) * 16, WS_C);
                store16(poison4, rs, myr * U_PITCH + e * 16, WS_U);
            }
            STAMP(5);
#pragma unroll
            for (int j = 0; j < 4; ++j) asm volatile("" : "+v"(xq[j]));
            {
                float f[4][8], mean[4], rstd[4];
#pragma unroll
                for (int rr = 0; rr < 4; ++rr) {
                    const v4u xr = xq[rr];
                    f[rr][0] = bf_lo(xr.x); f[rr][1] = bf_hi(xr.x); f[rr][2] = bf_lo(xr.y); f[rr][3] = bf_hi(xr.y);
                    f[rr][4] = bf_lo(xr.z); f[rr][5] = bf_hi(xr.z); f[rr][6] = bf_lo(xr.w); f[rr][7] = bf_hi(xr.w);
                    mean[rr] = vadd(vadd(vadd(f[rr][0], f[rr][1]), vadd(f[rr][2], f[rr][3])), vadd(vadd(f[rr][4], f[rr][5]), vadd(f[rr][6], f[rr][7])));
                }
#pragma unroll
                for (int rr = 0; rr < 4; ++rr) mean[rr] = vmul(wave_sum(mean[rr]), -1.0f / D_);
                WAIT_VM0();
                lds_flag_inc(SY_POIS);
#pragma unroll
                for (int rr = 0; rr < 4; ++rr) {
#pragma unroll
                    for (int j = 0; j < 8; ++j) f[rr][j] = vadd(f[rr][j], mean[rr]);
                    float q2 = vmul(f[rr][0], f[rr][0]);
#pragma unroll
                    for (int j = 1; j < 8; ++j) q2 = vfma(f[rr][j], f[rr][j], q2);
                    rstd[rr] = q2;
                }
#pragma unroll
                for (int rr = 0; rr < 4; ++rr) rstd[rr] = __builtin_amdgcn_rsqf(vfma(wave_sum(rstd[rr]), 1.0f / D_, 1e-5f));
                if (w == 6) {
                    lds_wait_ge(lds, SY_POIS, 2);
                    lds_wait_ge(lds, SY_TAGSET, 1u);
                    const u32 tag = __builtin_amdgcn_readfirstlane(lds_ld(lds, SY_TAG));
                    if (l == 0) __builtin_amdgcn_raw_buffer_store_b32(tag, rs, me * 128, WS_FLAGP, 16);
                }
                asm volatile("s_nop 1" : "+v"(rstd[0]), "+v"(rstd[1]), "+v"(rstd[2]), "+v"(rstd[3]));
#pragma unroll
                for (int rr = 0; rr < 4; ++rr) {
                    const v4u xr = xq[rr];
                    u32 o[8];
#pragma unroll
                    for (int j = 0; j < 4; ++j) {
                        const u32 np = pack_bf(vmul(f[rr][2 * j], rstd[rr]), vmul(f[rr][2 * j + 1], rstd[rr]));
                        o[2 * j] = __builtin_amdgcn_perm(np, xr[j], 0x05040100u);
                        o[2 * j + 1] = __builtin_amdgcn_perm(np, xr[j], 0x07060302u);
                    }
                    unsigned char* dstx = lds + L_XN + (4 * kr + rr) * 2048 + l * 32;
                    *(v4u*)dstx = (v4u){o[0], o[1], o[2], o[3]};
                    *(v4u*)(dstx + 16) = (v4u){o[4], o[5], o[6], o[7]};
                    if (l == 0) *(float*)(lds + L_PIV + (4 * kr + rr) * 4) = -mean[rr];
                }
            }
            lds_flag_inc(SY_X0);
            lds_wait_ge(lds, SY_POIS, 2);
            STAMP(6);
        }
    }

    asm volatile("" ::"v"(code_touch));
    WAIT_VM0();
    int redsel = 0;
    const int i_ = i;
    const __amdgpu_buffer_rsrc_t ro = __builtin_amdgcn_make_buffer_rsrc(out_, 0, S_ * D_ * 2, 0x00020000);

#pragma unroll 1
    for (int k = 0; k < 3; ++k) {
        int i = i_;
        asm volatile("" : "+v"(i));
        const int l = i & 63;
        STAMP(7 + 8 * k);
        const u32 cvoff = (myr >> 2) * C_PITCH + (i >> 1) * 128 + (myr & 3) * 32 + (i & 1) * 16, csoff = WS_C + k * C_SIZE;
        v4u cg = poison4;
        bool cissued = false;
        if (w < 4) {
            const int ch = w >> 1, h = w & 1, a = l & 15, g = l >> 4;
            const unsigned char* ub = lds + L_UR + ch * UR_PITCH + 2 * (2032 + 128 * h - 16 * a + 8 * g) - 3584;
            const u32 ready = (u32)(k + 1);
            if (k == 1 && mytile < KLOAD_SPLIT) load_afrags(A, lds, L_KB, ch, h, l);
            v4f acc[8];
#pragma unroll
            for (int c = 0; c < 8; ++c) acc[c] = (v4f){0, 0, 0, 0};
#pragma unroll
            for (int c = 0; c < 8; ++c) asm volatile("" : "+v"(acc[c]));
            v4u B[2][4];
            __builtin_amdgcn_s_setprio(1);
            lds_wait_ge(lds, SY_INTILE, ready);
            STAMP(8 + 8 * k);
            if (k == 0) STAMPC(29);
#pragma unroll
            for (int r = 0; r < 4; ++r) B[0][r] = *(const v4u*)(ub + 512 * 7 + 64 * r);
#define MFA "v_mfma_f32_16x16x32_bf16 "
#define MF1(cur, c, p, r)                                                                                     \
    asm volatile(MFA "%0, %1, %2, %0" : "+v"(acc[c]) : "v"(A[p][r]), "v"(B[cur][r]));                          \
    SCHED_FENCE();
#define MF4(cur, c, p)                                                                                        \
    asm volatile(MFA "%0, %1, %5, %0\n\t" MFA "%0, %2, %6, %0\n\t" MFA "%0, %3, %7, %0\n\t" MFA "%0, %4, %8, %0" \
                 : "+v"(acc[c]) : "v"(A[p][0]), "v"(A[p][1]), "v"(A[p][2]), "v"(A[p][3]),                      \
                   "v"(B[cur][0]), "v"(B[cur][1]), "v"(B[cur][2]), "v"(B[cur][3]));                             \
    SCHED_FENCE();
            u32 vflag = SY_PART + 4 * w, vready = ready, vpart = L_PART + ch * PART_CH + h * 1024 + 16 * (16 * g + ((a + 8 * (g >> 1)) & 15));
            asm volatile("" : "+v"(vflag), "+v"(vready), "+v"(vpart));
            u32 fl = 0;
            MF1(0, 0, 0, 0)
            fl = lds_ld(lds, SY_INTILE + 4); SCHED_FENCE();
            MF1(0, 0, 0, 1)
            B[1][0] = *(const v4u*)(ub + 3072); SCHED_FENCE();
            MF1(0, 0, 0, 2)
            B[1][1] = *(const v4u*)(ub + 3136); SCHED_FENCE();
            MF1(0, 0, 0, 3)
            B[1][2] = *(const v4u*)(ub + 3200); SCHED_FENCE();
            MF1(0, 1, 1, 0)
            B[1][3] = *(const v4u*)(ub + 3264); SCHED_FENCE();
            MF1(0, 1, 1, 1)
            MF1(0, 1, 1, 2)
            put_partial<0>(vpart, acc[0]); SCHED_FENCE();
            MF1(0, 1, 1, 3)
            MF1(0, 2, 2, 0)
            put_partial_flag<0>(vflag, vready); SCHED_FENCE();
            MF1(0, 2, 2, 1)
            const u32 fls0 = (u32)__builtin_amdgcn_readfirstlane(fl); SCHED_FENCE();
            MF1(0, 2, 2, 2)
            MF1(0, 2, 2, 3)
            MF4(0, 3, 3)
            MF4(0, 4, 4)
            MF4(0, 5, 5)
            MF4(0, 6, 6)
            MF4(0, 7, 7)
            if (__builtin_expect(fls0 < ready, 0)) {
                lds_wait_ge(lds, SY_INTILE + 4, ready);
#pragma unroll
                for (int r = 0; r < 4; ++r) B[1][r] = *(const v4u*)(ub + 3072 + 64 * r);
            }
            asm volatile("" ::: "memory");
            SCHED_FENCE();
            MF1(1, 1, 0, 0)
            fl = lds_ld(lds, SY_INTILE + 8); SCHED_FENCE();
            MF1(1, 1, 0, 1)
            B[0][0] = *(const v4u*)(ub + 2560); SCHED_FENCE();
            MF1(1, 1, 0, 2)
            B[0][1] = *(const v4u*)(ub + 2624); SCHED_FENCE();
            MF1(1, 1, 0, 3)
            B[0][2] = *(const v4u*)(ub + 2688); SCHED_FENCE();
            MF1(1, 2, 1, 0)
            B[0][3] = *(const v4u*)(ub + 2752); SCHED_FENCE();
            MF1(1, 2, 1, 1)
            MF1(1, 2, 1, 2)
            put_partial<1>(vpart, acc[1]); SCHED_FENCE();
            MF1(1, 2, 1, 3)
            MF1(1, 3, 2, 0)
            put_partial_flag<1>(vflag, vready); SCHED_FENCE();
            MF1(1, 3, 2, 1)
            const u32 fls1 = (u32)__builtin_amdgcn_readfirstlane(fl); SCHED_FENCE();
            MF1(1, 3, 2, 2)
            MF1(1, 3, 2, 3)
            MF4(1, 4, 3)
            MF4(1, 5, 4)
            MF4(1, 6, 5)
            MF4(1, 7, 6)
            if (__builtin_expect(fls1 < ready, 0)) {
                lds_wait_ge(lds, SY_INTILE + 8, ready);
#pragma unroll
                for (int r = 0; r < 4; ++r) B[0][r] = *(const v4u*)(ub + 2560 + 64 * r);
            }
            asm volatile("" ::: "memory");
            SCHED_FENCE();
            MF1(0, 2, 0, 0)
            fl = lds_ld(lds, SY_INTILE + 12); SCHED_FENCE();
            MF1(0, 2, 0, 1)
            B[1][0] = *(const v4u*)(ub + 2048); SCHED_FENCE();
            MF1(0, 2, 0, 2)
            B[1][1] = *(const v4u*)(ub + 2112); SCHED_FENCE();
            MF1(0, 2, 0, 3)
            B[1][2] = *(const v4u*)(ub + 2176); SCHED_FENCE();
            MF1(0, 3, 1, 0)
            B[1][3] = *(const v4u*)(ub + 2240); SCHED_FENCE();
            MF1(0, 3, 1, 1)
            MF1(0, 3, 1, 2)
            put_partial<2>(vpart, acc[2]); SCHED_FENCE();
            MF1(0, 3, 1, 3)
            MF1(0, 4, 2, 0)
            put_partial_flag<2>(vflag, vready); SCHED_FENCE();
            MF1(0, 4, 2, 1)
            const u32 fls2 = (u32)__builtin_amdgcn_readfirstlane(fl); SCHED_FENCE();
            MF1(0, 4, 2, 2)
            MF1(0, 4, 2, 3)
            MF4(0, 5, 3)
            MF4(0, 6, 4)
            MF4(0, 7, 5)
            if (__builtin_expect(fls2 < ready, 0)) {
                lds_wait_ge(lds, SY_INTILE + 12, ready);
#pragma unroll
                for (int r = 0; r < 4; ++r) B[1][r] = *(const v4u*)(ub + 2048 + 64 * r);
            }
            asm volatile("" ::: "memory");
            SCHED_FENCE();
            MF1(1, 3, 0, 0)
            fl = lds_ld(lds, SY_INTILE + 16); SCHED_FENCE();
            MF1(1, 3, 0, 1)
            B[0][0] = *(const v4u*)(ub + 1536); SCHED_FENCE();
            MF1(1, 3, 0, 2)
            B[0][1] = *(const v4u*)(ub + 1600); SCHED_FENCE();
            MF1(1, 3, 0, 3)
            B[0][2] = *(const v4u*)(ub + 1664); SCHED_FENCE();
            MF1(1, 4, 1, 0)
            B[0][3] = *(const v4u*)(ub + 1728); SCHED_FENCE();
            MF1(1, 4, 1, 1)
            MF1(1, 4, 1, 2)
            put_partial<3>(vpart, acc[3]); SCHED_FENCE();
            MF1(1, 4, 1, 3)
            MF1(1, 5, 2, 0)
            put_partial_flag<3>(vflag, vready); SCHED_FENCE();
            MF1(1, 5, 2, 1)
            const u32 fls3 = (u32)__builtin_amdgcn_readfirstlane(fl); SCHED_FENCE();
            MF1(1, 5, 2, 2)
            MF1(1, 5, 2, 3)
            MF4(1, 6, 3)
            MF4(1, 7, 4)
            if (__builtin_expect(fls3 < ready, 0)) {
                lds_wait_ge(lds, SY_INTILE + 16, ready);
#pragma unroll
                for (int r = 0; r < 4; ++r) B[0][r] = *(const v4u*)(ub + 1536 + 64 * r);
            }
            asm volatile("" ::: "memory");
            SCHED_FENCE();
            MF1(0, 4, 0, 0)
            fl = lds_ld(lds, SY_INTILE + 20); SCHED_FENCE();
            MF1(0, 4, 0, 1)
            B[1][0] = *(const v4u*)(ub + 1024); SCHED_FENCE();
            MF1(0, 4, 0, 2)
            B[1][1] = *(const v4u*)(ub + 1088); SCHED_FENCE();
            MF1(0, 4, 0, 3)
            B[1][2] = *(const v4u*)(ub + 1152); SCHED_FENCE();
            MF1(0, 5, 1, 0)
            B[1][3] = *(const v4u*)(ub + 1216); SCHED_FENCE();
            MF1(0, 5, 1, 1)
            MF1(0, 5, 1, 2)
            put_partial<4>(vpart, acc[4]); SCHED_FENCE();
            MF1(0, 5, 1, 3)
            MF1(0, 6, 2, 0)
            put_partial_flag<4>(vflag, vready); SCHED_FENCE();
            MF1(0, 6, 2, 1)
            const u32 fls4 = (u32)__builtin_amdgcn_readfirstlane(fl); SCHED_FENCE();
            MF1(0, 6, 2, 2)
            MF1(0, 6, 2, 3)
            MF4(0, 7, 3)
            if (__builtin_expect(fls4 < ready, 0)) {
                lds_wait_ge(lds, SY_INTILE + 20, ready);
#pragma unroll
                for (int r = 0; r < 4; ++r) B[1][r] = *(const v4u*)(ub + 1024 + 64 * r);
            }
            asm volatile("" ::: "memory");
            SCHED_FENCE();
            MF1(1, 5, 0, 0)
            fl = lds_ld(lds, SY_INTILE + 24); SCHED_FENCE();
            MF1(1, 5, 0, 1)
            B[0][0] = *(const v4u*)(ub + 512); SCHED_FENCE();
            MF1(1, 5, 0, 2)
            B[0][1] = *(const v4u*)(ub + 576); SCHED_FENCE();
            MF1(1, 5, 0, 3)
            B[0][2] = *(const v4u*)(ub + 640); SCHED_FENCE();
            MF1(1, 6, 1, 0)
            B[0][3] = *(const v4u*)(ub + 704); SCHED_FENCE();
            MF1(1, 6, 1, 1)
            MF1(1, 6, 1, 2)
            put_partial<5>(vpart, acc[5]); SCHED_FENCE();
            MF1(1, 6, 1, 3)
            MF1(1, 7, 2, 0)
            put_partial_flag<5>(vflag, vready); SCHED_FENCE();
            MF1(1, 7, 2, 1)
            const u32 fls5 = (u32)__builtin_amdgcn_readfirstlane(fl); SCHED_FENCE();
            MF1(1, 7, 2, 2)
            MF1(1, 7, 2, 3)
            if (__builtin_expect(fls5 < ready, 0)) {
                lds_wait_ge(lds, SY_INTILE + 24, ready);
#pragma unroll
                for (int r = 0; r < 4; ++r) B[0][r] = *(const v4u*)(ub + 512 + 64 * r);
            }
            asm volatile("" ::: "memory");
            SCHED_FENCE();
            MF1(0, 6, 0, 0)
            fl = lds_ld(lds, SY_INTILE + 28); SCHED_FENCE();
            MF1(0, 6, 0, 1)
            B[1][0] = *(const v4u*)(ub + 0); SCHED_FENCE();
            MF1(0, 6, 0, 2)
            B[1][1] = *(const v4u*)(ub + 64); SCHED_FENCE();
            MF1(0, 6, 0, 3)
            B[1][2] = *(const v4u*)(ub + 128); SCHED_FENCE();
            MF1(0, 7, 1, 0)
            B[1][3] = *(const v4u*)(ub + 192); SCHED_FENCE();
            MF1(0, 7, 1, 1)
            MF1(0, 7, 1, 2)
            put_partial<6>(vpart, acc[6]); SCHED_FENCE();
            MF1(0, 7, 1, 3)
            put_partial_flag<6>(vflag, vready); SCHED_FENCE();
            const u32 fls6 = (u32)__builtin_amdgcn_readfirstlane(fl); SCHED_FENCE();
            if (__builtin_expect(fls6 < ready, 0)) {
                lds_wait_ge(lds, SY_INTILE + 28, ready);
#pragma unroll
                for (int r = 0; r < 4; ++r) B[1][r] = *(const v4u*)(ub + 0 + 64 * r);
            }
            asm volatile("" ::: "memory");
            STAMP(9 + 8 * k); if (k == 0) STAMPC(30);
            SCHED_FENCE();
            {
                const u32 pub = __builtin_amdgcn_readfirstlane(lds_ld(lds, SY_PUB)), allf = __builtin_amdgcn_readfirstlane(lds_ld(lds, SY_ALLF));
                if (pub >= ready && allf) { cg = __builtin_amdgcn_raw_buffer_load_b128(rs, cvoff, csoff, 16); cissued = true; }
                SCHED_FENCE();
            }
            MF4(1, 7, 0)
            asm volatile("s_nop 7" ::: "memory");
            put_partial<7>(vpart, acc[7]); put_partial_flag<7>(vflag, vready);
#undef MF1
#undef MF4
#undef MFA
            __builtin_amdgcn_s_setprio(0);
            if (k == 0 && mytile >= KLOAD_SPLIT) {
                lds_wait_ge_slow(lds, SY_KREADY, 2);
                load_afrags(A, lds, L_KB, ch, h, l);
            }
        } else if (w >= 6) {
            const int ch = l & 1, tl = 32 * (l >> 3) + 8 * ((l >> 1) & 3);
            const float dco = *(const float*)(lds + L_DCO + (k == 0 ? 0 : 8) + ch * 4);
            __builtin_amdgcn_s_setprio(3);
#pragma unroll 1
            for (int c = w & 1; c < 8; c += 2) {
                if (c >= 6) {
                    const u32 pub = __builtin_amdgcn_readfirstlane(lds_ld(lds, SY_PUB)), allf = __builtin_amdgcn_readfirstlane(lds_ld(lds, SY_ALLF));
                    if (pub >= (u32)(k + 1) && allf) { cg = __builtin_amdgcn_raw_buffer_load_b128(rs, cvoff, csoff, 16); cissued = true; }
                }
                const int sb = (l >> 1) & 3, pa = 2 * (l >> 3) + (sb >> 1);
                const unsigned char* pp2 = lds + L_PART + c * PART_TILE + ch * PART_CH + 16 * (32 * (sb & 1) + ((pa + 8 * (sb & 1)) & 15));
                v4f a0, a1, b0, b1;
                v4u u;
                for (u32 it = 0;; ++it) {
                    asm volatile("" ::: "memory");
                    const v4u f = *(const v4u*)(lds + SY_PART + 16 * c);
                    asm volatile("" ::: "memory");
                    a0 = *(const v4f*)pp2; a1 = *(const v4f*)(pp2 + 256);
                    b0 = *(const v4f*)(pp2 + 1024); b1 = *(const v4f*)(pp2 + 1280);
                    u = *(const v4u*)(lds + L_UR + ch * UR_PITCH + 2 * (2040 - (256 * c + tl)));
                    const u32 fa = f.x < f.y ? f.x : f.y, fb = f.z < f.w ? f.z : f.w;
                    if ((u32)__builtin_amdgcn_readfirstlane(fa < fb ? fa : fb) >= (u32)(k + 1)) break;
                    if (spin_give_up(lds, it, LDS_SPIN_CAP)) break;
                }
                v4u o;
                o.x = no_poison(pack_bf(vfma(dco, bf_hi(u.w), vadd(a0.x, b0.x)), vfma(dco, bf_lo(u.w), vadd(a0.y, b0.y))));
                o.y = no_poison(pack_bf(vfma(dco, bf_hi(u.z), vadd(a0.z, b0.z)), vfma(dco, bf_lo(u.z), vadd(a0.w, b0.w))));
                o.z = no_poison(pack_bf(vfma(dco, bf_hi(u.y), vadd(a1.x, b1.x)), vfma(dco, bf_lo(u.y), vadd(a1.y, b1.y))));
                o.w = no_poison(pack_bf(vfma(dco, bf_hi(u.x), vadd(a1.z, b1.z)), vfma(dco, bf_lo(u.x), vadd(a1.w, b1.w))));
                store16(o, rs, (8 * c + (l >> 3)) * C_PITCH + me * 128 + (l & 7) * 16, WS_C + k * C_SIZE);
                if (c - 2 == mytile) { asm volatile("s_waitcnt vmcnt(1)" ::: "memory"); lds_flag_set(SY_PUB, (u32)(k + 1)); }
                if (c == mytile && c >= 6) {
                    asm volatile("s_waitcnt vmcnt(0)" ::: "memory");
                    lds_flag_set(SY_PUB, (u32)(k + 1));
                }
                if (c < 2) STAMP(8 + 8 * k);
                if (c >= 6) STAMP(9 + 8 * k);
            }
            __builtin_amdgcn_s_setprio(2);
        }

        asm volatile("" : "+v"(i));
        STAMP(10 + 8 * k);
        float x[8], y[8], n0[8], dm, rsd;
        float piv;
        v4u yap = {0, 0, 0, 0};
        if (!cissued) {
            if (k == 0) lds_wait_ge(lds, SY_ALLF, 1u);
            lds_wait_ge(lds, SY_PUB, (u32)(k + 1));
            cg = __builtin_amdgcn_raw_buffer_load_b128(rs, cvoff, csoff, 16);
        }
        STAMP(11 + 8 * k);
        SCHED_FENCE();
        if (k == 0) {
            store16(poison4, rs, (i >> 3) * C_PITCH + me * 128 + (i & 7) * 16, WS_C + C_SIZE);
            store16(poison4, rs, myr * U_PITCH + i * 16, WS_U + U_SIZE);
            lds_wait_ge(lds, SY_X0, 2);
#pragma unroll
            for (int t = 0; t < 8; ++t) {
                const u32 xn = *(const u32*)(lds + L_XN + t * 2048 + i * 4);
                x[t] = bf_lo(xn);
                n0[t] = bf_hi(xn);
            }
            piv = *(const float*)(lds + L_PIV + (l & 7) * 4);
        } else {
            if (k == 1) store16(poison4, rs, (i >> 3) * C_PITCH + me * 128 + (i & 7) * 16, WS_C + 2 * C_SIZE);
            const v4f xa = *(const v4f*)(lds + L_PARK + i * 16), xb = *(const v4f*)(lds + L_PARK + 8192 + i * 16);
            x[0] = xa.x; x[1] = xa.y; x[2] = xa.z; x[3] = xa.w; x[4] = xb.x; x[5] = xb.y; x[6] = xb.z; x[7] = xb.w;
            yap = *(const v4u*)(lds + L_PARK + 16384 + i * 16);
            piv = *(const float*)(lds + L_PARK + 24576 + i * 4);
        }
        v4u sel_lo, sel_hi;
        {
            const u32 one = 0x3F80u << (16 * (l & 1)), dsel = (l & 7) >> 1;
            const v4u pat = {dsel == 0 ? one : 0u, dsel == 1 ? one : 0u, dsel == 2 ? one : 0u, dsel == 3 ? one : 0u};
            const v4u zero = {0, 0, 0, 0};
            sel_lo = (l & 8) ? zero : pat;
            sel_hi = (l & 8) ? pat : zero;
        }
        {
            bool ok = valid16(cg);
            for (u32 it = 0; !__all(ok); ++it) {
                if (spin_give_up(lds, it * 4 + 3, GLB_SPIN_CAP * 4)) break;
                asm volatile("" ::: "memory");
                if (!ok) { cg = __builtin_amdgcn_raw_buffer_load_b128(rs, cvoff, csoff, 16); ok = valid16(cg); }
            }
        }
        STAMP(12 + 8 * k);
        const float c[8] = {bf_lo(cg.x), bf_hi(cg.x), bf_lo(cg.y), bf_hi(cg.y), bf_lo(cg.z), bf_hi(cg.z), bf_lo(cg.w), bf_hi(cg.w)};
        if (k == 2) {
#pragma unroll
            for (int t = 0; t < 8; t += 2) {
                const u32 o = pack_bf(x[t] + c[t], x[t + 1] + c[t + 1]);
                __builtin_amdgcn_raw_buffer_store_b16((u16)o, ro, i * 2 + t * (D_ * 2), myr * (8 * D_ * 2), 0);
                __builtin_amdgcn_raw_buffer_store_b16((u16)(o >> 16), ro, i * 2 + (t + 1) * (D_ * 2), myr * (8 * D_ * 2), 0);
            }
            break;
        }
        if (k == 0) {
#pragma unroll
            for (int t = 0; t < 8; ++t) x[t] = vfma(n0[t], c[t], x[t]);
            yap = cg;
        } else {
#pragma unroll
            for (int t = 0; t < 8; ++t) x[t] = vadd(x[t], c[t]);
            sub_rows(y, x, piv);
            row_stats(y, sel_lo, sel_hi, lds, L_RED + redsel * 512, w, l, dm, rsd, [] {});
            redsel ^= 1;
            norm_rows(y, rsd, dm * rsd);
            const float ya[8] = {bf_lo(yap.x), bf_hi(yap.x), bf_lo(yap.y), bf_hi(yap.y), bf_lo(yap.z), bf_hi(yap.z), bf_lo(yap.w), bf_hi(yap.w)};
#pragma unroll
            for (int t = 0; t < 8; ++t) x[t] = vfma(y[t], ya[t], x[t]);
            piv += dm;
        }
        sub_rows(y, x, piv);
        WAIT_VM0();
        auto tail = [&](auto npre_c) {
            constexpr int NPRE = decltype(npre_c)::value;
            const u32 gsoff = WS_U + k * U_SIZE;
            const u32 gvoff = (32 * (w & 1) + (l >> 1)) * U_PITCH + (2 * me + (l & 1)) * 16;
            v4u d[4];
            row_stats(y, sel_lo, sel_hi, lds, L_RED + redsel * 512, w, l, dm, rsd, [&] {
                if (NPRE >= 1) d[0] = __builtin_amdgcn_raw_buffer_load_b128(rs, gvoff, gsoff, 16);
                SCHED_FENCE();
            });
            norm_rows(y, rsd, dm * rsd);
            v4u uo;
            uo.x = no_poison(pack_bf(y[7], y[6]));
            uo.y = no_poison(pack_bf(y[5], y[4]));
            uo.z = no_poison(pack_bf(y[3], y[2]));
            uo.w = no_poison(pack_bf(y[1], y[0]));
            SCHED_FENCE();
#pragma unroll
            for (int t = 1; t < NPRE; ++t) d[t] = __builtin_amdgcn_raw_buffer_load_b128(rs, gvoff + t * 64 * U_PITCH, gsoff, 16);
            SCHED_FENCE();
            store16(uo, rs, myr * U_PITCH + i * 16, WS_U + k * U_SIZE);
            piv += dm;
            *(v4f*)(lds + L_PARK + i * 16) = (v4f){x[0], x[1], x[2], x[3]};
            *(v4f*)(lds + L_PARK + 8192 + i * 16) = (v4f){x[4], x[5], x[6], x[7]};
            *(v4u*)(lds + L_PARK + 16384 + i * 16) = yap;
            *(float*)(lds + L_PARK + 24576 + i * 4) = piv;
            STAMP(13 + 8 * k);
            if (NPRE >= 0) {
                __builtin_amdgcn_s_setprio(3);
                unsigned char* dst = lds + L_UR + (l & 1) * UR_PITCH + 2 * (2040 - 8 * (32 * (w & 1) + (l >> 1)));
#pragma unroll
                for (int t = 0; t < 4; ++t) {
                    const int q = 2 * t + (w & 1);
                    if (t == NPRE) {
                        WAIT_VM0();
#pragma unroll
                        for (int u = t; u < 4; ++u) {
                            d[u] = poison4;
                            if (2 * u + (w & 1) <= mytile + GATHER_AHEAD) d[u] = __builtin_amdgcn_raw_buffer_load_b128(rs, gvoff + u * 64 * U_PITCH, gsoff, 16);
                        }
                    }
                    if (__builtin_expect(!__all(valid16(d[t])), 0)) {
                        for (u32 it = 0; !__all(valid16(d[t])); ++it) {
                            if (spin_give_up(lds, it * 4 + 3, GLB_SPIN_CAP * 4)) break;
                            asm volatile("" ::: "memory");
                            if (!valid16(d[t])) d[t] = __builtin_amdgcn_raw_buffer_load_b128(rs, gvoff + t * 64 * U_PITCH, gsoff, 16);
                        }
                    }
                    *(v4u*)(dst - t * 1024) = d[t];
                    lds_flag_set(SY_INTILE + 4 * q, (u32)(k + 2));
                    if (t == 0) STAMP(16 + 8 * k);
                    if (t == 3) STAMP(17 + 8 * k);
                }
                WAIT_VM0();
                __builtin_amdgcn_s_setprio(2);
            }
        };
        if (w == 4 || w == 5) {
            if (mytile >= 6) tail(std::integral_constant<int, 3>());
            else if (mytile >= 4) tail(std::integral_constant<int, 2>());
            else if (mytile >= 2) tail(std::integral_constant<int, 1>());
            else tail(std::integral_constant<int, 0>());
        } else {
            tail(std::integral_constant<int, -1>());
        }
        redsel ^= 1;
    }
    asm volatile(".Lh3s4_end:");
}

extern "C" void kernel_launch(void* const* d_in, const int* in_sizes, int n_in, void* d_out, int out_size, void* d_ws, size_t ws_size,
                              hipStream_t stream) {
    (void)in_sizes; (void)n_in; (void)out_size; (void)ws_size;
    h3s4_kernel<<<256, 512, 0, stream>>>(
        (const u16*)d_in[0], (const u16*)d_in[1], (const u16*)d_in[2], (const u16*)d_in[3], (const u16*)d_in[4], (const u16*)d_in[5],
        (const u16*)d_in[7], (const u16*)d_in[8], (const u16*)d_in[9], (const u16*)d_in[10], (const u16*)d_in[11], (const u16*)d_in[12],
        (const u16*)d_in[13], (const u16*)d_in[14], (u16*)d_out, (unsigned char*)d_ws);
}
```

```cpp
#include <hip/hip_runtime.h>
#include <stdint.h>
#include <type_traits>

typedef unsigned short u16;
typedef unsigned u32;
typedef u32 v4u __attribute__((ext_vector_type(4)));
typedef float v2f __attribute__((ext_vector_type(2)));
typedef float v4f __attribute__((ext_vector_type(4)));
typedef __bf16 v2bf __attribute__((ext_vector_type(2)));
typedef __bf16 v8bf __attribute__((ext_vector_type(8)));
typedef _Float16 v2h __attribute__((ext_vector_type(2)));
typedef _Float16 v8h __attribute__((ext_vector_type(8)));

constexpr int S_ = 2048, D_ = 512, NS_ = 128;
constexpr u32 POISON = 0xFFFFFFFFu;

constexpr u32 WS_EPOCH = 0;
constexpr u32 WS_FLAGS = 4096;
constexpr u32 KV_PITCH = 4096 + 128;
constexpr u32 WS_FLAGP = WS_FLAGS + 256 * 128;
constexpr u32 WS_KV = WS_FLAGP + 256 * 128 + 4096;
constexpr u32 C_PITCH = 32768 + 128;
constexpr u32 C_SIZE = 64 * C_PITCH;
constexpr u32 WS_C = WS_KV + 512 * KV_PITCH;
constexpr u32 U_PITCH = 8192 + 128;
constexpr u32 U_SIZE = 256 * U_PITCH;
constexpr u32 WS_U = WS_C + 3 * C_SIZE;
constexpr u32 WS_TOTAL = WS_U + 2 * U_SIZE;

constexpr int UR_PITCH = 4608;
constexpr int L_UR = 0;
constexpr int PART_CH = 2048 + 64;
constexpr int PART_TILE = 2 * PART_CH;
constexpr int L_PART = L_UR + 2 * UR_PITCH;
constexpr int KC_COPY = 4160;
constexpr int KC_CH = 2 * KC_COPY;
constexpr int L_KA = L_PART + 8 * PART_TILE;
constexpr int L_KB = L_KA + 2 * KC_CH;
constexpr int KT_CH = 648 * 4;
constexpr int L_KT = L_KB + 2 * KC_CH;
constexpr int TAB_ROW = 528;
constexpr int TAB_CH = 57 * TAB_ROW;
constexpr int L_TAB = L_KT + 2 * KT_CH;
constexpr int L_T = L_TAB + 2 * TAB_CH;
constexpr int L_XN = L_T;
constexpr int L_PARK = L_TAB;
constexpr int L_RED = L_T + 16384;
constexpr int L_SYNC = L_RED + 2 * 512;
constexpr int SY_PART = L_SYNC;
constexpr int SY_INTILE = L_SYNC + 128;
constexpr int SY_KV = L_SYNC + 160;
constexpr int SY_GO = L_SYNC + 164;
constexpr int SY_TAB = L_SYNC + 168;

constexpr int SY_KREADY = L_SYNC + 184;
constexpr int SY_ABORT = L_SYNC + 188;
constexpr int SY_TAG = L_SYNC + 192;
constexpr int SY_ALLF = L_SYNC + 196;
constexpr int SY_PUB = L_SYNC + 200;
constexpr int SY_X0 = L_SYNC + 204;
constexpr int SY_KA = L_SYNC + 176;
constexpr int SY_TAGSET = L_SYNC + 180;
constexpr int SY_POIS = L_SYNC + 164 + 44;
constexpr int SY_PUBT = L_SYNC + 212;
constexpr int L_DCO = L_SYNC + 224;
constexpr int L_PIV = L_DCO + 32;
constexpr int LDS_BYTES = L_PIV + 32;
static_assert(LDS_BYTES > 82 * 1024 && LDS_BYTES <= 160 * 1024, "one workgroup per CU");

#define GATHER_AHEAD 1
#define ALLF_TILE 1
#define KV_DELAY 0
#define KV_BACKOFF 0
#define C_DELAY 65
#define U_DELAY 55
#define PUB_EXTRA 0
#define GATH_EXTRA 0
#define POLL_BACKOFF 0
#define PUB_SLEEP 0
#define GATH_SLEEP 0
#define KLOAD_SPLIT 5
#define GATHER_LAG 2
#define GATHER_LOOK 0
#define OUT_AUX 16
#define CODE_PREFETCH 1
constexpr u32 LDS_SPIN_CAP = 1u << 19;
constexpr u32 GLB_SPIN_CAP = 1u << 16;

#define STAMP(n) do {} while (0)
#define STAMPC(n) do {} while (0)
#define SCHED_FENCE() __builtin_amdgcn_sched_barrier(0)
#define WAIT_VM0() do { asm volatile("" ::: "memory"); __builtin_amdgcn_s_waitcnt(0x0F70); asm volatile("" ::: "memory"); } while (0)

__device__ __forceinline__ float bf_lo(u32 x) { return __builtin_bit_cast(float, x << 16); }
__device__ __forceinline__ float bf_hi(u32 x) { return __builtin_bit_cast(float, x & 0xFFFF0000u); }
__device__ __forceinline__ float bf2f(u16 x) { return __builtin_bit_cast(float, ((u32)x) << 16); }
__device__ __forceinline__ u32 pack_bf(float lo, float hi) {
    v2f f = {lo, hi};
    return __builtin_bit_cast(u32, __builtin_convertvector(f, v2bf));
}
__device__ __forceinline__ u32 pack_h(v2f f) { return __builtin_bit_cast(u32, __builtin_convertvector(f, v2h)); }
__device__ __forceinline__ u32 no_poison(u32 x) { return x < 0xFFFFFFFEu ? x : 0xFFFFFFFEu; }
template <int CTRL> __device__ __forceinline__ float dppf(float x) {
    return __builtin_bit_cast(float, __builtin_amdgcn_mov_dpp(__builtin_bit_cast(int, x), CTRL, 0xf, 0xf, true));
}
template <int CTRL> __device__ __forceinline__ u32 dppu(u32 x) { return (u32)__builtin_amdgcn_mov_dpp((int)x, CTRL, 0xf, 0xf, true); }
__device__ __forceinline__ float f_of(u32 x) { return __builtin_bit_cast(float, x); }
__device__ __forceinline__ u32 u_of(float x) { return __builtin_bit_cast(u32, x); }

__device__ __forceinline__ u32 lds_ld(unsigned char* lds, int off) {
    return __hip_atomic_load((u32*)(lds + off), __ATOMIC_RELAXED, __HIP_MEMORY_SCOPE_WORKGROUP);
}
__device__ __forceinline__ void lds_st(unsigned char* lds, int off, u32 v) {
    __hip_atomic_store((u32*)(lds + off), v, __ATOMIC_RELAXED, __HIP_MEMORY_SCOPE_WORKGROUP);
}
__device__ __forceinline__ void lds_flag_set(int off, u32 v) {
    asm volatile("s_mov_b64 exec, 1\n\tds_write_b32 %0, %1\n\ts_mov_b64 exec, -1" ::"v"(off), "v"(v) : "memory");
}
template <int Q> __device__ __forceinline__ void put_partial(u32 vpart, const v4f& acc) {
    asm volatile("ds_write_b128 %0, %1 offset:%c2" ::"v"(vpart), "v"(acc), "i"(Q * PART_TILE) : "memory");
}
template <int Q> __device__ __forceinline__ void put_partial_flag(u32 vflag, u32 v) {
    asm volatile("ds_write_b32 %0, %1 offset:%c2" ::"v"(vflag), "v"(v), "i"(Q * 16) : "memory");
}
__device__ __forceinline__ void lds_flag_inc(int off) {
    asm volatile("s_mov_b64 exec, 1\n\tds_add_u32 %0, %1\n\ts_mov_b64 exec, -1" ::"v"(off), "v"(1u) : "memory");
}
__device__ __forceinline__ bool spin_give_up(unsigned char* lds, u32 it, u32 cap) {
    if ((it & 63u) != 63u) return false;
    if (it > cap) { lds_st(lds, SY_ABORT, 1u); return true; }
    return __builtin_amdgcn_readfirstlane(lds_ld(lds, SY_ABORT)) != 0;
}
__device__ __forceinline__ void lds_wait_ge(unsigned char* lds, int off, u32 target) {
    if (__builtin_expect((u32)__builtin_amdgcn_readfirstlane(lds_ld(lds, off)) < target, 0)) {
        for (u32 it = 0; (u32)__builtin_amdgcn_readfirstlane(lds_ld(lds, off)) < target; ++it)
            if (spin_give_up(lds, it, LDS_SPIN_CAP)) break;
    }
    asm volatile("" ::: "memory");
}
__device__ __forceinline__ void lds_wait_ge_slow(unsigned char* lds, int off, u32 target) {
    for (u32 it = 0; (u32)__builtin_amdgcn_readfirstlane(lds_ld(lds, off)) < target; ++it) {
        if (spin_give_up(lds, it, LDS_SPIN_CAP)) break;
        __builtin_amdgcn_s_sleep(1);
    }
    asm volatile("" ::: "memory");
}
__device__ __forceinline__ void lds_wait4_ge(unsigned char* lds, int off, u32 target) {
    for (u32 it = 0;; ++it) {
        asm volatile("" ::: "memory");
        const v4u f = *(const v4u*)(lds + off);
        const u32 a = f.x < f.y ? f.x : f.y, b = f.z < f.w ? f.z : f.w;
        if ((u32)__builtin_amdgcn_readfirstlane(a < b ? a : b) >= target) break;
        if (spin_give_up(lds, it, LDS_SPIN_CAP)) break;
    }
    asm volatile("" ::: "memory");
}
__device__ __forceinline__ void store16(v4u v, __amdgpu_buffer_rsrc_t rs, u32 voff, u32 soff) {
    __builtin_amdgcn_raw_buffer_store_b128(v, rs, voff + soff, 0, 16);
}
__device__ __forceinline__ u32 now_ticks() { return (u32)__builtin_amdgcn_s_memrealtime(); }
__device__ __forceinline__ void wait_until(u32 t0, u32 delay) {
    for (u32 it = 0; now_ticks() - t0 < delay && it < 4096; ++it) __builtin_amdgcn_s_sleep(1);
}
__device__ __forceinline__ bool valid16(v4u r) {
    u32 m = r.x > r.y ? r.x : r.y;
    m = m > r.z ? m : r.z;
    m = m > r.w ? m : r.w;
    return m != POISON;
}
__device__ __forceinline__ v4u poll16(__amdgpu_buffer_rsrc_t rs, u32 voff, u32 soff, unsigned char* lds) {
    v4u r = __builtin_amdgcn_raw_buffer_load_b128(rs, voff, soff, 16);
    bool ok = valid16(r);
    for (u32 it = 0; !__all(ok); ++it) {
        if (spin_give_up(lds, it * 4 + 3, GLB_SPIN_CAP * 4)) break;
        asm volatile("" ::: "memory");
        if (!ok) { r = __builtin_amdgcn_raw_buffer_load_b128(rs, voff, soff, 16); ok = valid16(r); }
    }
    return r;
}

__device__ __forceinline__ float vmul(float a, float b) {
    float r;
    asm("v_mul_f32 %0, %1, %2" : "=v"(r) : "v"(a), "v"(b));
    return r;
}
#define DPPB(t) " row_newbcast:" #t " row_mask:0xf bank_mask:0xf\n\t"
__device__ __forceinline__ void sub_rows(float (&y)[8], const float (&x)[8], float p) {
    asm("s_nop 1\n\t"
        "v_subrev_f32_dpp %0, %16, %8" DPPB(0) "v_subrev_f32_dpp %1, %16, %9" DPPB(1) "v_subrev_f32_dpp %2, %16, %10" DPPB(2)
        "v_subrev_f32_dpp %3, %16, %11" DPPB(3) "v_subrev_f32_dpp %4, %16, %12" DPPB(4) "v_subrev_f32_dpp %5, %16, %13" DPPB(5)
        "v_subrev_f32_dpp %6, %16, %14" DPPB(6) "v_subrev_f32_dpp %7, %16, %15" DPPB(7)
        : "=&v"(y[0]), "=&v"(y[1]), "=&v"(y[2]), "=&v"(y[3]), "=&v"(y[4]), "=&v"(y[5]), "=&v"(y[6]), "=&v"(y[7])
        : "v"(x[0]), "v"(x[1]), "v"(x[2]), "v"(x[3]), "v"(x[4]), "v"(x[5]), "v"(x[6]), "v"(x[7]), "v"(p));
}
__device__ __forceinline__ void norm_rows(float (&y)[8], float r, float m) {
    asm("s_nop 1\n\t"
        "v_mul_f32_dpp %0, %8, %0" DPPB(0) "v_mul_f32_dpp %1, %8, %1" DPPB(1) "v_mul_f32_dpp %2, %8, %2" DPPB(2) "v_mul_f32_dpp %3, %8, %3" DPPB(3)
        "v_mul_f32_dpp %4, %8, %4" DPPB(4) "v_mul_f32_dpp %5, %8, %5" DPPB(5) "v_mul_f32_dpp %6, %8, %6" DPPB(6) "v_mul_f32_dpp %7, %8, %7" DPPB(7)
        "v_subrev_f32_dpp %0, %9, %0" DPPB(0) "v_subrev_f32_dpp %1, %9, %1" DPPB(1) "v_subrev_f32_dpp %2, %9, %2" DPPB(2)
        "v_subrev_f32_dpp %3, %9, %3" DPPB(3) "v_subrev_f32_dpp %4, %9, %4" DPPB(4) "v_subrev_f32_dpp %5, %9, %5" DPPB(5)
        "v_subrev_f32_dpp %6, %9, %6" DPPB(6) "v_subrev_f32_dpp %7, %9, %7" DPPB(7)
        : "+v"(y[0]), "+v"(y[1]), "+v"(y[2]), "+v"(y[3]), "+v"(y[4]), "+v"(y[5]), "+v"(y[6]), "+v"(y[7])
        : "v"(r), "v"(m));
}
#undef DPPB
__device__ __forceinline__ float vfma(float a, float b, float c) {
    float r;
    asm("v_fma_f32 %0, %1, %2, %3" : "=v"(r) : "v"(a), "v"(b), "v"(c));
    return r;
}
__device__ __forceinline__ float vadd(float a, float b) {
    float r;
    asm("v_add_f32 %0, %1, %2" : "=v"(r) : "v"(a), "v"(b));
    return r;
}

template <class Hook>
__device__ __forceinline__ void row_stats(const float (&y)[8], const v4u& sel_lo, const v4u& sel_hi, unsigned char* lds, int redoff, int w, int l,
                                          float& dmean, float& rstd, Hook after_barrier) {
    const v4u a = {pack_bf(y[0], y[1]), pack_bf(y[2], y[3]), pack_bf(y[4], y[5]), pack_bf(y[6], y[7])};
    float q[8];
    asm("v_mul_f32 %0, %8, %8\n\tv_mul_f32 %1, %9, %9\n\tv_mul_f32 %2, %10, %10\n\tv_mul_f32 %3, %11, %11\n\t"
        "v_mul_f32 %4, %12, %12\n\tv_mul_f32 %5, %13, %13\n\tv_mul_f32 %6, %14, %14\n\tv_mul_f32 %7, %15, %15"
        : "=&v"(q[0]), "=&v"(q[1]), "=&v"(q[2]), "=&v"(q[3]), "=&v"(q[4]), "=&v"(q[5]), "=&v"(q[6]), "=&v"(q[7])
        : "v"(y[0]), "v"(y[1]), "v"(y[2]), "v"(y[3]), "v"(y[4]), "v"(y[5]), "v"(y[6]), "v"(y[7]));
    const v4u s = {pack_bf(q[0], q[1]), pack_bf(q[2], q[3]), pack_bf(q[4], q[5]), pack_bf(q[6], q[7])};
    v4f d = {0, 0, 0, 0};
    d = __builtin_amdgcn_mfma_f32_16x16x32_bf16(__builtin_bit_cast(v8bf, a), __builtin_bit_cast(v8bf, sel_lo), d, 0, 0, 0);
    d = __builtin_amdgcn_mfma_f32_16x16x32_bf16(__builtin_bit_cast(v8bf, s), __builtin_bit_cast(v8bf, sel_hi), d, 0, 0, 0);
    u32 t0 = u_of((d.x + d.y) + (d.z + d.w)), t1 = t0;
    {
        auto r = __builtin_amdgcn_permlane16_swap(t0, t1, false, false);
        const u32 r0 = r[0], r1 = r[1];
        t0 = u_of(f_of(r0) + f_of(r1)); t1 = t0;
    }
    {
        auto r = __builtin_amdgcn_permlane32_swap(t0, t1, false, false);
        const u32 r0 = r[0], r1 = r[1];
        t0 = u_of(f_of(r0) + f_of(r1));
    }
    *(u32*)(lds + redoff + (l & 15) * 32 + w * 4) = t0;
    __syncthreads();
    after_barrier();
    const v4f p0 = *(const v4f*)(lds + redoff + (l & 15) * 32), p1 = *(const v4f*)(lds + redoff + (l & 15) * 32 + 16);
    const v4f pp = p0 + p1;
    const float tot = (pp.x + pp.y) + (pp.z + pp.w);
    const float s2 = dppf<0x128>(tot);
    dmean = tot * (1.0f / D_);
    const float var = fmaxf(s2 * (1.0f / D_) - dmean * dmean, 0.0f);
    rstd = __builtin_amdgcn_rsqf(var + 1e-5f);
}
__device__ __forceinline__ float wave_sum(float v) {
    asm("s_nop 1" : "+v"(v));
    v += dppf<0x128>(v);
    v += dppf<0x141>(v);
    v += dppf<0xB1>(v);
    v += dppf<0x4E>(v);
    u32 a = u_of(v), b = a;
    {
        auto r = __builtin_amdgcn_permlane16_swap(a, b, false, false);
        const u32 r0 = r[0], r1 = r[1];
        a = u_of(f_of(r0) + f_of(r1)); b = a;
    }
    auto r = __builtin_amdgcn_permlane32_swap(a, b, false, false);
    const u32 r0 = r[0], r1 = r[1];
    return f_of(r0) + f_of(r1);
}

__device__ __forceinline__ void load_afrags(v4u (&A)[8][4], unsigned char* lds, int region, int ch, int h, int l) {
    const int b = l & 15, g = l >> 4, par = (1 + b) & 1;
    const unsigned char* base = lds + region + ch * KC_CH + par * KC_COPY + 256 * h + 16 * g + 2 * (1 + b - par);
#pragma unroll
    for (int p = 0; p < 8; ++p)
#pragma unroll
        for (int r = 0; r < 4; ++r) {
            const u32* q = (const u32*)(base + 512 * p + 64 * r);
            A[p][r] = (v4u){q[0], q[1], q[2], q[3]};
        }
}

__device__ __forceinline__ v2f cexp_pow(float al, float be, float m) {
    const float e = __builtin_amdgcn_exp2f(al * m);
    const float ph = __builtin_amdgcn_fractf(be * m);
    return (v2f){e * __builtin_amdgcn_cosf(ph), e * __builtin_amdgcn_sinf(ph)};
}
__device__ __forceinline__ v2f cmul(v2f a, v2f b) {
    v2f t;
    asm("v_pk_mul_f32 %0, %1, %2 op_sel:[0,0] op_sel_hi:[1,0]\n\t"
        "v_pk_fma_f32 %0, %1, %2, %0 op_sel:[1,1,0] op_sel_hi:[0,1,1] neg_lo:[1,0,0]"
        : "=&v"(t) : "v"(a), "v"(b));
    return t;
}
__device__ __forceinline__ v4f mfma_bf(v4u a, v4u b, v4f c) {
    return __builtin_amdgcn_mfma_f32_16x16x32_bf16(__builtin_bit_cast(v8bf, a), __builtin_bit_cast(v8bf, b), c, 0, 0, 0);
}

extern "C" __global__ void __launch_bounds__(512, 2)
h3s4_kernel(const u16* __restrict__ q_, const u16* __restrict__ k_, const u16* __restrict__ v_, const u16* __restrict__ shk_,
            const u16* __restrict__ ssmk_, const u16* __restrict__ ssmD_, const u16* __restrict__ Are_, const u16* __restrict__ Aim_,
            const u16* __restrict__ Bre_, const u16* __restrict__ Bim_, const u16* __restrict__ Cre_, const u16* __restrict__ Cim_,
            const u16* __restrict__ logdt_, const u16* __restrict__ Dskip_, u16* __restrict__ out_, unsigned char* ws_) {
    __shared__ __attribute__((aligned(16))) unsigned char lds[LDS_BYTES];
    asm volatile("" ::"s"(q_), "s"(k_), "s"(v_), "s"(shk_), "s"(ssmk_), "s"(ssmD_), "s"(Are_), "s"(Aim_), "s"(Bre_), "s"(Bim_),
                 "s"(Cre_), "s"(Cim_), "s"(logdt_), "s"(Dskip_), "s"(out_), "s"(ws_));
    const int i = threadIdx.x, l = i & 63;
    const int w = __builtin_amdgcn_readfirstlane(i >> 6);
    u32 code_touch = 0;
    if (w == 0) {
        unsigned long long pc;
        u32 left;
        asm volatile("s_getpc_b64 %0\n.Lh3s4_pc:\n\ts_mov_b32 %1, .Lh3s4_end-.Lh3s4_pc+1536" : "=s"(pc), "=s"(left));
        const u32 nl = (left >> 12) + 1, off = ((blockIdx.x >> 3) * nl + l) * 128;
        if ((u32)l < nl && off + 4 <= left) code_touch = *(const __attribute__((address_space(1))) u32*)((pc & ~3ull) + off);
    }
    const int xcd = blockIdx.x & 7, idx = blockIdx.x >> 3;
    const int me = xcd * 32 + idx;
    const int mytile = idx >> 2;
    const int myr = 32 * mytile + 4 * xcd + (idx & 3);
    const __amdgpu_buffer_rsrc_t rs = __builtin_amdgcn_make_buffer_rsrc(ws_, 0, WS_TOTAL, 0x00020000);
    const v4u poison4 = {POISON, POISON, POISON, POISON};
    STAMP(0);

    const int cp = i & 31, rb = i >> 5;
    const int pcg = (xcd + idx) & 7;
    const int R0 = 64 * idx + 4 * rb, pch = 64 * pcg + 2 * cp;
    u32 kd[7], vd[4];
#pragma unroll
    for (int j = 0; j < 7; ++j) {
        const int Rj = R0 - 3 + j;
        kd[j] = *(const u32*)(k_ + (Rj < 0 ? 0 : Rj) * D_ + pch);
    }
#pragma unroll
    for (int j = 0; j < 4; ++j) vd[j] = *(const u32*)(v_ + (R0 + j) * D_ + pch);
    const v4u wd = *(const v4u*)(shk_ + pch * 4);
    u32 epoch = 0;
    if (w == 0) epoch = __builtin_amdgcn_raw_buffer_load_b32(rs, 0, WS_EPOCH, 16);
    v4u xq[4];

    if (i >= 256 && i < 256 + 64) *(v4u*)(lds + L_UR + ((i >> 5) & 1) * UR_PITCH + 4096 + (i & 31) * 16) = (v4u){0, 0, 0, 0};
    if (i >= 320 && i < 320 + 16) {
        const int e = i - 320;
        *(v4u*)(lds + L_KA + (e >> 1) * KC_COPY + (e & 1) * 16) = (v4u){0, 0, 0, 0};
    }
    if (i >= 384 && i < 384 + 56) *(u32*)(lds + L_SYNC + (i - 384) * 4) = 0;

    STAMP(1);
    {
#pragma unroll
        for (int j = 0; j < 3; ++j) if (R0 - 3 + j < 0) kd[j] = 0;
#pragma unroll
        for (int c = 0; c < 2; ++c) {
            const u32 sel = c ? 0x07060302u : 0x05040100u;
            const u32 w01 = wd[2 * c], w23 = wd[2 * c + 1];
            u32 P[7];
#pragma unroll
            for (int j = 1; j < 7; ++j) P[j] = __builtin_amdgcn_perm(kd[j - 1], kd[j], sel);
            float res[4];
#pragma unroll
            for (int t = 0; t < 4; ++t) {
                float a = __builtin_amdgcn_fdot2_f32_bf16(__builtin_bit_cast(v2bf, P[t + 3]), __builtin_bit_cast(v2bf, w01), 0.0f, false);
                a = __builtin_amdgcn_fdot2_f32_bf16(__builtin_bit_cast(v2bf, P[t + 1]), __builtin_bit_cast(v2bf, w23), a, false);
                res[t] = a * (c ? bf_hi(vd[t]) : bf_lo(vd[t]));
            }
            typedef u32 v2u __attribute__((ext_vector_type(2)));
            const int half = (1 - (rb & 1)) ^ ((cp >> 3) & 1);
            *(v2u*)(lds + L_T + (2 * cp + c) * 128 + (((rb >> 1) ^ (cp & 7)) * 16) + half * 8) = (v2u){pack_bf(res[3], res[2]), pack_bf(res[1], res[0])};
        }
    }
    SCHED_FENCE();
    u32 pp[11], dA, dB;
    if (w >= 4) {
        const int pc = 2 * me + ((w - 4) >> 1), pi = pc * NS_ + (i & 127), pj = pi ^ 64;
        pp[0] = Are_[pi]; pp[1] = Aim_[pi]; pp[2] = Bre_[pi]; pp[3] = Bim_[pi]; pp[4] = Cre_[pi]; pp[5] = Cim_[pi];
        pp[6] = Bre_[pj]; pp[7] = Bim_[pj]; pp[8] = Cre_[pj]; pp[9] = Cim_[pj];
        pp[10] = logdt_[pc];
        dA = ssmD_[2 * me + (l & 1)]; dB = Dskip_[2 * me + (l & 1)];
        if (w >= 6) {
#pragma unroll
            for (int j = 0; j < 4; ++j) xq[j] = *(const v4u*)(q_ + (8 * myr + 4 * (w & 1) + j) * D_ + l * 8);
        }
    }
    STAMP(2);
    __syncthreads();
    STAMP(3);

    v4u A[8][4];
    if (w < 4) {
#pragma unroll
        for (int j = 0; j < 2; ++j) {
            typedef u32 v2u __attribute__((ext_vector_type(2)));
            const int e = i + 256 * j, ch = e >> 3, chunk = e & 7, sw = (ch >> 4) & 1;
            const unsigned char* src = lds + L_T + ch * 128 + ((chunk ^ ((ch >> 1) & 7)) * 16);
            const v2u d0 = *(const v2u*)(src + 8 * sw), d1 = *(const v2u*)(src + 8 * (1 - sw));
            store16((v4u){d0.x, d0.y, d1.x, d1.y}, rs, (64 * pcg + ch) * KV_PITCH + (8 * idx + chunk) * 16, WS_KV);
        }
        SCHED_FENCE();
        asm volatile("" ::: "memory");
        v4u sk[3];
        u32 sknext[3];
#pragma unroll
        for (int j = 0; j < 3; ++j) {
            const int e = (i - 64) + 192 * j, ec = (w == 0 || e > 511) ? 0 : e;
            const u16* src = ssmk_ + (2 * me + (ec >> 8)) * S_ + (ec & 255) * 8;
            if (w != 0) {
                sk[j] = *(const v4u*)src;
                sknext[j] = *(const u32*)(src + ((ec & 255) < 255 ? 8 : 0));
            }
        }
        SCHED_FENCE();
        if (w == 0) asm volatile("s_waitcnt vmcnt(0)" ::: "memory");
        else asm volatile("s_waitcnt vmcnt(6)" ::: "memory");
        lds_flag_inc(SY_KV);
        STAMP(4);
        u32 tag = 0, tflag = 0;
        if (w == 0) {
            lds_wait_ge(lds, SY_KV, 4);
            tag = __builtin_amdgcn_readfirstlane(epoch) + 1u;
            if (l == 0) __builtin_amdgcn_raw_buffer_store_b32(tag, rs, me * 128, WS_FLAGS, 16);
            tflag = now_ticks();
            lds_flag_set(SY_TAG, tag);
            lds_flag_set(SY_TAGSET, 1u);
        }
        if (w != 0) {
#pragma unroll
            for (int j = 0; j < 3; ++j) {
                const int e = (i - 64) + 192 * j, sm = e & 255;
                if (e < 512) {
                    unsigned char* c0 = lds + L_KA + (e >> 8) * KC_CH;
                    const u32 nx = sm < 255 ? sknext[j] : 0u;
                    *(v4u*)(c0 + 32 + sm * 16) = sk[j];
                    *(v4u*)(c0 + KC_COPY + 32 + sm * 16) =
                        (v4u){__builtin_amdgcn_alignbit(sk[j].y, sk[j].x, 16), __builtin_amdgcn_alignbit(sk[j].z, sk[j].y, 16),
                              __builtin_amdgcn_alignbit(sk[j].w, sk[j].z, 16), __builtin_amdgcn_alignbit(nx, sk[j].w, 16)};
                    if (sm == 0) *(u32*)(c0 + KC_COPY + 28) = sk[j].x << 16;
                }
            }
            lds_flag_inc(SY_KA);
        }
        if (w == 0) {
            const u32 foff = (((xcd - (l & 31)) & 7) * 32 + (l & 31)) * 128;
            wait_until(tflag, KV_DELAY);
            for (u32 it = 0;; ++it) {
                asm volatile("" ::: "memory");
                if (__all(__builtin_amdgcn_raw_buffer_load_b32(rs, foff, WS_FLAGS, 16) == tag)) break;
                if (spin_give_up(lds, it * 4 + 3, GLB_SPIN_CAP * 4)) break;
            }
            lds_flag_set(SY_GO, 1u);
        } else {
            lds_wait_ge(lds, SY_KA, 3);
            load_afrags(A, lds, L_KA, w >> 1, w & 1, l);
            lds_wait_ge(lds, SY_GO, 1u);
        }
        STAMP(5);
        v4u g0, g1;
        {
            const u32 off = (2 * me + (l >> 5)) * KV_PITCH + (64 * w + (l & 31)) * 16;
            g0 = __builtin_amdgcn_raw_buffer_load_b128(rs, off, WS_KV, 16);
            g1 = __builtin_amdgcn_raw_buffer_load_b128(rs, off + 512, WS_KV, 16);
        }
        SCHED_FENCE();
        if (w == 0) {
            lds_wait_ge(lds, SY_KA, 3);
            load_afrags(A, lds, L_KA, 0, 0, l);
        }
        STAMP(6);
        unsigned char* dst = lds + L_UR + (l >> 5) * UR_PITCH + 2 * (2040 - 8 * (64 * w + (l & 31)));
        *(v4u*)dst = g0;
        *(v4u*)(dst - 512) = g1;
        lds_flag_set(SY_INTILE + 8 * w, 1u);
        lds_flag_set(SY_INTILE + 8 * w + 4, 1u);
        WAIT_VM0();

    } else {
        __builtin_amdgcn_s_setprio(2);
        const int wa = w - 4, ch = wa >> 1, n = (i - 256) & 127;
#pragma unroll
        for (int j = 0; j < 11; ++j) asm volatile("" : "+v"(pp[j]));
        asm volatile("" : "+v"(dA), "+v"(dB));
        const float ex = expf(f_of(pp[10] << 16));
        const float dt = bf_lo(pack_bf(ex, ex));
        const float al = dt * f_of(pp[0] << 16) * 1.44269504088896341f, be = dt * f_of(pp[1] << 16) * 0.15915494309189535f;
        v2f W;
        float inv;
        {
            const float br = dt * f_of(pp[2] << 16), bi = dt * f_of(pp[3] << 16), cr = f_of(pp[4] << 16), ci = f_of(pp[5] << 16);
            const float br2 = dt * f_of(pp[6] << 16), bi2 = dt * f_of(pp[7] << 16), cr2 = f_of(pp[8] << 16), ci2 = f_of(pp[9] << 16);
            W = (v2f){cr * br - ci * bi, cr * bi + ci * br};
            float mx = fmaxf(fmaxf(fabsf(W.x), fabsf(W.y)), fmaxf(fabsf(cr2 * br2 - ci2 * bi2), fabsf(cr2 * bi2 + ci2 * br2)));
            mx = fmaxf(mx, dppf<0x128>(mx));
            mx = fmaxf(mx, dppf<0x141>(mx));
            mx = fmaxf(mx, dppf<0xB1>(mx));
            mx = fmaxf(mx, dppf<0x4E>(mx));
            float m0 = f_of((u32)__builtin_amdgcn_readlane((int)u_of(mx), 0)), m1 = f_of((u32)__builtin_amdgcn_readlane((int)u_of(mx), 16));
            float m2 = f_of((u32)__builtin_amdgcn_readlane((int)u_of(mx), 32)), m3 = f_of((u32)__builtin_amdgcn_readlane((int)u_of(mx), 48));
            const int e = __builtin_amdgcn_frexp_expf(fmaxf(fmaxf(m0, m1), fmaxf(m2, m3)));
            W = (v2f){__builtin_amdgcn_ldexpf(W.x, 14 - e), __builtin_amdgcn_ldexpf(W.y, 14 - e)};
            inv = __builtin_amdgcn_ldexpf(1.0f, e - 14);
        }
        unsigned char* tab = lds + L_TAB + ch * TAB_CH + n * 4;
#define TAB_PUT(row, val) (*(u32*)(tab + (row) * TAB_ROW) = pack_h(val))
        const v2f E1 = cexp_pow(al, be, 1.0f), E8 = cexp_pow(al, be, 8.0f), E128 = cexp_pow(al, be, 128.0f);
        const v2f E256 = cmul(E128, E128), E384 = cmul(E256, E128), E512 = cmul(E256, E256), E1024 = cmul(E512, E512);
        {
            const v2f E8c = {E8.x, -E8.y};
            v2f B = {W.x, -W.y}, P = {1.0f, 0.0f}, Q = E512, Rr = E1024, P4 = cmul(cmul(E1, E1), cmul(E1, E1));
#pragma unroll
            for (int r = 0; r < 8; ++r) {
                TAB_PUT(41 + 2 * r, B);
                B = cmul(B, E8c);
                TAB_PUT(42 + 2 * r, B);
                if (r < 7) B = cmul(B, E8c);
                TAB_PUT(r, P);
                TAB_PUT(8 + r, r ? cmul(P, E128) : E128);
                if ((r & 1) == 0) {
                    TAB_PUT(16 + (r >> 1), r ? cmul(P, E256) : E256);
                    TAB_PUT(20 + (r >> 1), r ? cmul(P, E384) : E384);
                }
                if (r < 7) P = r ? cmul(P, E1) : E1;
                TAB_PUT(32 + r, Rr);
                if (r < 7) Rr = cmul(Rr, E128);
                if (r < 4) {
                    TAB_PUT(24 + 2 * r, Q);
                    TAB_PUT(25 + 2 * r, cmul(Q, P4));
                    if (r < 3) Q = cmul(Q, E128);
                }
            }
            TAB_PUT(40, cmul(E1024, E1024));
        }
#undef TAB_PUT
        if (l == 0) *(float*)(lds + L_DCO + 16 + ch * 4) = inv;
        lds_flag_inc(SY_TAB + 4 * ch);
        if (l < 2) { *(u32*)(lds + L_DCO + l * 4) = dA << 16; *(u32*)(lds + L_DCO + 8 + l * 4) = dB << 16; }
        STAMP(4);
        if (w < 6) {
            const int kc = w & 1;
            lds_wait_ge(lds, SY_TAB + 4 * kc, 2);
            {
                const int row = l & 15, g = l >> 4;
                const unsigned char* tb = lds + L_TAB + kc * TAB_CH + row * TAB_ROW + g * 16;
                float* kt = (float*)(lds + L_KT + kc * KT_CH);
                const float scale = *(const float*)(lds + L_DCO + 16 + kc * 4);
                v4u fb[8], fa[8];
                v4f acc[3];
#pragma unroll
                for (int s2 = 0; s2 < 8; ++s2) fb[s2] = *(const v4u*)(tb + 41 * TAB_ROW + s2 * 64);
#pragma unroll
                for (int tile = 0; tile < 3; ++tile) {
#pragma unroll
                    for (int s2 = 0; s2 < 8; ++s2) fa[s2] = *(const v4u*)(tb + tile * 16 * TAB_ROW + s2 * 64);
                    acc[tile] = (v4f){0, 0, 0, 0};
#pragma unroll
                    for (int s2 = 0; s2 < 8; ++s2)
                        acc[tile] = __builtin_amdgcn_mfma_f32_16x16x32_f16(__builtin_bit_cast(v8h, fa[s2]), __builtin_bit_cast(v8h, fb[s2]), acc[tile], 0, 0, 0);
                }
                asm volatile("s_nop 7\n\ts_nop 1" : "+v"(acc[0]), "+v"(acc[1]), "+v"(acc[2]));
#pragma unroll
                for (int r = 0; r < 4; ++r) {
                    const int rw = 4 * g + r;
                    kt[128 * (rw >> 3) + (rw & 7) + 8 * row] = vmul(acc[0][r], scale);
                    kt[rw < 8 ? 256 + 64 * (rw >> 2) + (rw & 3) + 4 * row : 384 + 32 * ((rw - 8) >> 1) + (rw & 1) + 2 * row] = vmul(acc[1][r], scale);
                    kt[rw < 8 ? 512 + 16 * rw + row : ((rw == 8 && row == 0) ? 640 : 644 + (l & 3))] = vmul(acc[2][r], scale);
                }
            }
            asm volatile("s_waitcnt lgkmcnt(0)" ::: "memory");
            {
                const float* t = (const float*)(lds + L_KT + kc * KT_CH);
                unsigned char* c0 = lds + L_KB + kc * KC_CH;
                const float a2 = t[512 + l], b2 = t[513 + l], a3 = t[576 + l], b3 = t[577 + l];
                const float v0 = t[384 + 2 * l], v1 = t[385 + 2 * l], v2 = t[386 + 2 * l];
                float kvv[9];
#pragma unroll
                for (int j = 0; j < 9; ++j) {
                    const int lag = 8 * l + j;
                    const u32 bb = pack_bf((float)lag, 0.0f) & 0xFFFFu;
                    kvv[j] = t[l < 32 ? lag : (int)(bb - 0x4280u)];
                }
                {
                    const u32 e0 = pack_bf(kvv[0], kvv[1]);
                    *(v4u*)(c0 + 32 + l * 16) = (v4u){e0, pack_bf(kvv[2], kvv[3]), pack_bf(kvv[4], kvv[5]), pack_bf(kvv[6], kvv[7])};
                    *(v4u*)(c0 + KC_COPY + 32 + l * 16) = (v4u){pack_bf(kvv[1], kvv[2]), pack_bf(kvv[3], kvv[4]), pack_bf(kvv[5], kvv[6]), pack_bf(kvv[7], kvv[8])};
                    if (l == 0) *(u32*)(c0 + KC_COPY + 28) = e0 << 16;
                }
                {
                    const u32 p00 = pack_bf(v0, v0), p01 = pack_bf(v0, v1), p11 = pack_bf(v1, v1), p12 = pack_bf(v1, v2), p22 = pack_bf(v2, v2);
                    *(v4u*)(c0 + 32 + (64 + l) * 16) = (v4u){p00, p01, p11, p22};
                    *(v4u*)(c0 + KC_COPY + 32 + (64 + l) * 16) = (v4u){p00, p11, p12, p22};
                }
#pragma unroll
                for (int it = 0; it < 2; ++it) {
                    const float a = it ? a3 : a2, b = it ? b3 : b2, c4 = (l & 1) ? b : a;
                    const u32 paa = pack_bf(a, a), pbb = pack_bf(b, b);
                    *(v4u*)(c0 + 32 + (128 + 64 * it + l) * 16) = (v4u){paa, paa, pack_bf(c4, b), pbb};
                    *(v4u*)(c0 + KC_COPY + 32 + (128 + 64 * it + l) * 16) = (v4u){paa, pack_bf(a, c4), pbb, pbb};
                }
            }
            lds_flag_inc(SY_KREADY);
            STAMP(5);
            if (w == 4) {
                lds_wait_ge(lds, SY_TAGSET, 1u);
                const u32 tag = __builtin_amdgcn_readfirstlane(lds_ld(lds, SY_TAG));
                lds_wait_ge_slow(lds, SY_PART + 16 * ALLF_TILE, 1u);
                for (u32 it = 0;; ++it) {
                    asm volatile("" ::: "memory");
                    if (it) __builtin_amdgcn_s_sleep(8);
                    const u32 f0 = __builtin_amdgcn_raw_buffer_load_b32(rs, l * 128, WS_FLAGP, 16);
                    const u32 f1 = __builtin_amdgcn_raw_buffer_load_b32(rs, (l + 64) * 128, WS_FLAGP, 16);
                    const u32 f2 = __builtin_amdgcn_raw_buffer_load_b32(rs, (l + 128) * 128, WS_FLAGP, 16);
                    const u32 f3 = __builtin_amdgcn_raw_buffer_load_b32(rs, (l + 192) * 128, WS_FLAGP, 16);
                    if (__all(f0 == tag && f1 == tag && f2 == tag && f3 == tag)) break;
                    if (spin_give_up(lds, it * 4 + 3, GLB_SPIN_CAP * 4)) break;
                }
                if (me == 0 && l == 0) __builtin_amdgcn_raw_buffer_store_b32(tag, rs, 0, WS_EPOCH, 16);
                lds_flag_set(SY_ALLF, 1u);
            }
            WAIT_VM0();
        } else {
            const int kr = w & 1;
            lds_wait_ge(lds, SY_KV, 4);
#pragma unroll
            for (int j = 0; j < 4; ++j) {
                const int e = (i & 127) + 128 * j;
                store16(poison4, rs, (e >> 3) * C_PITCH + me * 128 + (e & 7) * 16, WS_C);
                store16(poison4, rs, myr * U_PITCH + e * 16, WS_U);
            }
            STAMP(5);
#pragma unroll
            for (int j = 0; j < 4; ++j) asm volatile("" : "+v"(xq[j]));
            {
                float f[4][8], mean[4], rstd[4];
#pragma unroll
                for (int rr = 0; rr < 4; ++rr) {
                    const v4u xr = xq[rr];
                    f[rr][0] = bf_lo(xr.x); f[rr][1] = bf_hi(xr.x); f[rr][2] = bf_lo(xr.y); f[rr][3] = bf_hi(xr.y);
                    f[rr][4] = bf_lo(xr.z); f[rr][5] = bf_hi(xr.z); f[rr][6] = bf_lo(xr.w); f[rr][7] = bf_hi(xr.w);
                    mean[rr] = vadd(vadd(vadd(f[rr][0], f[rr][1]), vadd(f[rr][2], f[rr][3])), vadd(vadd(f[rr][4], f[rr][5]), vadd(f[rr][6], f[rr][7])));
                }
#pragma unroll
                for (int rr = 0; rr < 4; ++rr) mean[rr] = vmul(wave_sum(mean[rr]), -1.0f / D_);
                WAIT_VM0();
                lds_flag_inc(SY_POIS);
#pragma unroll
                for (int rr = 0; rr < 4; ++rr) {
#pragma unroll
                    for (int j = 0; j < 8; ++j) f[rr][j] = vadd(f[rr][j], mean[rr]);
                    float q2 = vmul(f[rr][0], f[rr][0]);
#pragma unroll
                    for (int j = 1; j < 8; ++j) q2 = vfma(f[rr][j], f[rr][j], q2);
                    rstd[rr] = q2;
                }
#pragma unroll
                for (int rr = 0; rr < 4; ++rr) rstd[rr] = __builtin_amdgcn_rsqf(vfma(wave_sum(rstd[rr]), 1.0f / D_, 1e-5f));
                if (w == 6) {
                    lds_wait_ge(lds, SY_POIS, 2);
                    lds_wait_ge(lds, SY_TAGSET, 1u);
                    const u32 tag = __builtin_amdgcn_readfirstlane(lds_ld(lds, SY_TAG));
                    if (l == 0) __builtin_amdgcn_raw_buffer_store_b32(tag, rs, me * 128, WS_FLAGP, 16);
                }
                asm volatile("s_nop 1" : "+v"(rstd[0]), "+v"(rstd[1]), "+v"(rstd[2]), "+v"(rstd[3]));
#pragma unroll
                for (int rr = 0; rr < 4; ++rr) {
                    const v4u xr = xq[rr];
                    u32 o[8];
#pragma unroll
                    for (int j = 0; j < 4; ++j) {
                        const u32 np = pack_bf(vmul(f[rr][2 * j], rstd[rr]), vmul(f[rr][2 * j + 1], rstd[rr]));
                        o[2 * j] = __builtin_amdgcn_perm(np, xr[j], 0x05040100u);
                        o[2 * j + 1] = __builtin_amdgcn_perm(np, xr[j], 0x07060302u);
                    }
                    unsigned char* dstx = lds + L_XN + (4 * kr + rr) * 2048 + l * 32;
                    *(v4u*)dstx = (v4u){o[0], o[1], o[2], o[3]};
                    *(v4u*)(dstx + 16) = (v4u){o[4], o[5], o[6], o[7]};
                    if (l == 0) *(float*)(lds + L_PIV + (4 * kr + rr) * 4) = -mean[rr];
                }
            }
            lds_flag_inc(SY_X0);
            lds_wait_ge(lds, SY_POIS, 2);
            STAMP(6);
        }
    }

    asm volatile("" ::"v"(code_touch));
    WAIT_VM0();
    int redsel = 0;
    const int i_ = i;
    const __amdgpu_buffer_rsrc_t ro = __builtin_amdgcn_make_buffer_rsrc(out_, 0, S_ * D_ * 2, 0x00020000);

#pragma unroll 1
    for (int k = 0; k < 3; ++k) {
        int i = i_;
        asm volatile("" : "+v"(i));
        const int l = i & 63;
        STAMP(7 + 8 * k);
        const u32 cvoff = (myr >> 2) * C_PITCH + (i >> 1) * 128 + (myr & 3) * 32 + (i & 1) * 16, csoff = WS_C + k * C_SIZE;
        v4u cg = poison4;
        bool cissued = false;
        if (w < 4) {
            const int ch = w >> 1, h = w & 1, a = l & 15, g = l >> 4;
            const unsigned char* ub = lds + L_UR + ch * UR_PITCH + 2 * (2032 + 128 * h - 16 * a + 8 * g) - 3584;
            const u32 ready = (u32)(k + 1);
            if (k == 1 && mytile < KLOAD_SPLIT) load_afrags(A, lds, L_KB, ch, h, l);
            v4f acc[8];
#pragma unroll
            for (int c = 0; c < 8; ++c) acc[c] = (v4f){0, 0, 0, 0};
#pragma unroll
            for (int c = 0; c < 8; ++c) asm volatile("" : "+v"(acc[c]));
            v4u B[2][4];
            __builtin_amdgcn_s_setprio(1);
            lds_wait_ge(lds, SY_INTILE, ready);
            STAMP(8 + 8 * k);
            if (k == 0) STAMPC(29);
#pragma unroll
            for (int r = 0; r < 4; ++r) B[0][r] = *(const v4u*)(ub + 512 * 7 + 64 * r);
#define MFA "v_mfma_f32_16x16x32_bf16 "
#define MF1(cur, c, p, r)                                                                                     \
    asm volatile(MFA "%0, %1, %2, %0" : "+v"(acc[c]) : "v"(A[p][r]), "v"(B[cur][r]));                          \
    SCHED_FENCE();
#define MF4(cur, c, p)                                                                                        \
    asm volatile(MFA "%0, %1, %5, %0\n\t" MFA "%0, %2, %6, %0\n\t" MFA "%0, %3, %7, %0\n\t" MFA "%0, %4, %8, %0" \
                 : "+v"(acc[c]) : "v"(A[p][0]), "v"(A[p][1]), "v"(A[p][2]), "v"(A[p][3]),                      \
                   "v"(B[cur][0]), "v"(B[cur][1]), "v"(B[cur][2]), "v"(B[cur][3]));                             \
    SCHED_FENCE();
            u32 vflag = SY_PART + 4 * w, vready = ready, vpart = L_PART + ch * PART_CH + h * 1024 + 16 * (16 * g + ((a + 8 * (g >> 1)) & 15));
            asm volatile("" : "+v"(vflag), "+v"(vready), "+v"(vpart));
            u32 fl = 0;
            MF1(0, 0, 0, 0)
            fl = lds_ld(lds, SY_INTILE + 4); SCHED_FENCE();
            MF1(0, 0, 0, 1)
            B[1][0] = *(const v4u*)(ub + 3072); SCHED_FENCE();
            MF1(0, 0, 0, 2)
            B[1][1] = *(const v4u*)(ub + 3136); SCHED_FENCE();
            MF1(0, 0, 0, 3)
            B[1][2] = *(const v4u*)(ub + 3200); SCHED_FENCE();
            MF1(0, 1, 1, 0)
            B[1][3] = *(const v4u*)(ub + 3264); SCHED_FENCE();
            MF1(0, 1, 1, 1)
            MF1(0, 1, 1, 2)
            put_partial<0>(vpart, acc[0]); SCHED_FENCE();
            MF1(0, 1, 1, 3)
            MF1(0, 2, 2, 0)
            put_partial_flag<0>(vflag, vready); SCHED_FENCE();
            MF1(0, 2, 2, 1)
            const u32 fls0 = (u32)__builtin_amdgcn_readfirstlane(fl); SCHED_FENCE();
            MF1(0, 2, 2, 2)
            MF1(0, 2, 2, 3)
            MF4(0, 3, 3)
            MF4(0, 4, 4)
            MF4(0, 5, 5)
            MF4(0, 6, 6)
            MF4(0, 7, 7)
            if (__builtin_expect(fls0 < ready, 0)) {
                lds_wait_ge(lds, SY_INTILE + 4, ready);
#pragma unroll
                for (int r = 0; r < 4; ++r) B[1][r] = *(const v4u*)(ub + 3072 + 64 * r);
            }
            asm volatile("" ::: "memory");
            SCHED_FENCE();
            MF1(1, 1, 0, 0)
            fl = lds_ld(lds, SY_INTILE + 8); SCHED_FENCE();
            MF1(1, 1, 0, 1)
            B[0][0] = *(const v4u*)(ub + 2560); SCHED_FENCE();
            MF1(1, 1, 0, 2)
            B[0][1] = *(const v4u*)(ub + 2624); SCHED_FENCE();
            MF1(1, 1, 0, 3)
            B[0][2] = *(const v4u*)(ub + 2688); SCHED_FENCE();
            MF1(1, 2, 1, 0)
            B[0][3] = *(const v4u*)(ub + 2752); SCHED_FENCE();
            MF1(1, 2, 1, 1)
            MF1(1, 2, 1, 2)
            put_partial<1>(vpart, acc[1]); SCHED_FENCE();
            MF1(1, 2, 1, 3)
            MF1(1, 3, 2, 0)
            put_partial_flag<1>(vflag, vready); SCHED_FENCE();
            MF1(1, 3, 2, 1)
            const u32 fls1 = (u32)__builtin_amdgcn_readfirstlane(fl); SCHED_FENCE();
            MF1(1, 3, 2, 2)
            MF1(1, 3, 2, 3)
            MF4(1, 4, 3)
            MF4(1, 5, 4)
            MF4(1, 6, 5)
            MF4(1, 7, 6)
            if (__builtin_expect(fls1 < ready, 0)) {
                lds_wait_ge(lds, SY_INTILE + 8, ready);
#pragma unroll
                for (int r = 0; r < 4; ++r) B[0][r] = *(const v4u*)(ub + 2560 + 64 * r);
            }
            asm volatile("" ::: "memory");
            SCHED_FENCE();
            MF1(0, 2, 0, 0)
            fl = lds_ld(lds, SY_INTILE + 12); SCHED_FENCE();
            MF1(0, 2, 0, 1)
            B[1][0] = *(const v4u*)(ub + 2048); SCHED_FENCE();
            MF1(0, 2, 0, 2)
            B[1][1] = *(const v4u*)(ub + 2112); SCHED_FENCE();
            MF1(0, 2, 0, 3)
            B[1][2] = *(const v4u*)(ub + 2176); SCHED_FENCE();
            MF1(0, 3, 1, 0)
            B[1][3] = *(const v4u*)(ub + 2240); SCHED_FENCE();
            MF1(0, 3, 1, 1)
            MF1(0, 3, 1, 2)
            put_partial<2>(vpart, acc[2]); SCHED_FENCE();
            MF1(0, 3, 1, 3)
            MF1(0, 4, 2, 0)
            put_partial_flag<2>(vflag, vready); SCHED_FENCE();
            MF1(0, 4, 2, 1)
            const u32 fls2 = (u32)__builtin_amdgcn_readfirstlane(fl); SCHED_FENCE();
            MF1(0, 4, 2, 2)
            MF1(0, 4, 2, 3)
            MF4(0, 5, 3)
            MF4(0, 6, 4)
            MF4(0, 7, 5)
            if (__builtin_expect(fls2 < ready, 0)) {
                lds_wait_ge(lds, SY_INTILE + 12, ready);
#pragma unroll
                for (int r = 0; r < 4; ++r) B[1][r] = *(const v4u*)(ub + 2048 + 64 * r);
            }
            asm volatile("" ::: "memory");
            SCHED_FENCE();
            MF1(1, 3, 0, 0)
            fl = lds_ld(lds, SY_INTILE + 16); SCHED_FENCE();
            MF1(1, 3, 0, 1)
            B[0][0] = *(const v4u*)(ub + 1536); SCHED_FENCE();
            MF1(1, 3, 0, 2)
            B[0][1] = *(const v4u*)(ub + 1600); SCHED_FENCE();
            MF1(1, 3, 0, 3)
            B[0][2] = *(const v4u*)(ub + 1664); SCHED_FENCE();
            MF1(1, 4, 1, 0)
            B[0][3] = *(const v4u*)(ub + 1728); SCHED_FENCE();
            MF1(1, 4, 1, 1)
            MF1(1, 4, 1, 2)
            put_partial<3>(vpart, acc[3]); SCHED_FENCE();
            MF1(1, 4, 1, 3)
            MF1(1, 5, 2, 0)
            put_partial_flag<3>(vflag, vready); SCHED_FENCE();
            MF1(1, 5, 2, 1)
            const u32 fls3 = (u32)__builtin_amdgcn_readfirstlane(fl); SCHED_FENCE();
            MF1(1, 5, 2, 2)
            MF1(1, 5, 2, 3)
            MF4(1, 6, 3)
            MF4(1, 7, 4)
            if (__builtin_expect(fls3 < ready, 0)) {
                lds_wait_ge(lds, SY_INTILE + 16, ready);
#pragma unroll
                for (int r = 0; r < 4; ++r) B[0][r] = *(const v4u*)(ub + 1536 + 64 * r);
            }
            asm volatile("" ::: "memory");
            SCHED_FENCE();
            MF1(0, 4, 0, 0)
            fl = lds_ld(lds, SY_INTILE + 20); SCHED_FENCE();
            MF1(0, 4, 0, 1)
            B[1][0] = *(const v4u*)(ub + 1024); SCHED_FENCE();
            MF1(0, 4, 0, 2)
            B[1][1] = *(const v4u*)(ub + 1088); SCHED_FENCE();
            MF1(0, 4, 0, 3)
            B[1][2] = *(const v4u*)(ub + 1152); SCHED_FENCE();
            MF1(0, 5, 1, 0)
            B[1][3] = *(const v4u*)(ub + 1216); SCHED_FENCE();
            MF1(0, 5, 1, 1)
            MF1(0, 5, 1, 2)
            put_partial<4>(vpart, acc[4]); SCHED_FENCE();
            MF1(0, 5, 1, 3)
            MF1(0, 6, 2, 0)
            put_partial_flag<4>(vflag, vready); SCHED_FENCE();
            MF1(0, 6, 2, 1)
            const u32 fls4 = (u32)__builtin_amdgcn_readfirstlane(fl); SCHED_FENCE();
            MF1(0, 6, 2, 2)
            MF1(0, 6, 2, 3)
            MF4(0, 7, 3)
            if (__builtin_expect(fls4 < ready, 0)) {
                lds_wait_ge(lds, SY_INTILE + 20, ready);
#pragma unroll
                for (int r = 0; r < 4; ++r) B[1][r] = *(const v4u*)(ub + 1024 + 64 * r);
            }
            asm volatile("" ::: "memory");
            SCHED_FENCE();
            MF1(1, 5, 0, 0)
            fl = lds_ld(lds, SY_INTILE + 24); SCHED_FENCE();
            MF1(1, 5, 0, 1)
            B[0][0] = *(const v4u*)(ub + 512); SCHED_FENCE();
            MF1(1, 5, 0, 2)
            B[0][1] = *(const v4u*)(ub + 576); SCHED_FENCE();
            MF1(1, 5, 0, 3)
            B[0][2] = *(const v4u*)(ub + 640); SCHED_FENCE();
            MF1(1, 6, 1, 0)
            B[0][3] = *(const v4u*)(ub + 704); SCHED_FENCE();
            MF1(1, 6, 1, 1)
            MF1(1, 6, 1, 2)
            put_partial<5>(vpart, acc[5]); SCHED_FENCE();
            MF1(1, 6, 1, 3)
            MF1(1, 7, 2, 0)
            put_partial_flag<5>(vflag, vready); SCHED_FENCE();
            MF1(1, 7, 2, 1)
            const u32 fls5 = (u32)__builtin_amdgcn_readfirstlane(fl); SCHED_FENCE();
            MF1(1, 7, 2, 2)
            MF1(1, 7, 2, 3)
            if (__builtin_expect(fls5 < ready, 0)) {
                lds_wait_ge(lds, SY_INTILE + 24, ready);
#pragma unroll
                for (int r = 0; r < 4; ++r) B[0][r] = *(const v4u*)(ub + 512 + 64 * r);
            }
            asm volatile("" ::: "memory");
            SCHED_FENCE();
            MF1(0, 6, 0, 0)
            fl = lds_ld(lds, SY_INTILE + 28); SCHED_FENCE();
            MF1(0, 6, 0, 1)
            B[1][0] = *(const v4u*)(ub + 0); SCHED_FENCE();
            MF1(0, 6, 0, 2)
            B[1][1] = *(const v4u*)(ub + 64); SCHED_FENCE();
            MF1(0, 6, 0, 3)
            B[1][2] = *(const v4u*)(ub + 128); SCHED_FENCE();
            MF1(0, 7, 1, 0)
            B[1][3] = *(const v4u*)(ub + 192); SCHED_FENCE();
            MF1(0, 7, 1, 1)
            MF1(0, 7, 1, 2)
            put_partial<6>(vpart, acc[6]); SCHED_FENCE();
            MF1(0, 7, 1, 3)
            put_partial_flag<6>(vflag, vready); SCHED_FENCE();
            const u32 fls6 = (u32)__builtin_amdgcn_readfirstlane(fl); SCHED_FENCE();
            if (__builtin_expect(fls6 < ready, 0)) {
                lds_wait_ge(lds, SY_INTILE + 28, ready);
#pragma unroll
                for (int r = 0; r < 4; ++r) B[1][r] = *(const v4u*)(ub + 0 + 64 * r);
            }
            asm volatile("" ::: "memory");
            STAMP(9 + 8 * k); if (k == 0) STAMPC(30);
            SCHED_FENCE();
            {
                const u32 pub = __builtin_amdgcn_readfirstlane(lds_ld(lds, SY_PUB)), allf = __builtin_amdgcn_readfirstlane(lds_ld(lds, SY_ALLF));
                const u32 pubt = __builtin_amdgcn_readfirstlane(lds_ld(lds, SY_PUBT));
                if (pub >= ready && allf && now_ticks() - pubt >= C_DELAY) { cg = __builtin_amdgcn_raw_buffer_load_b128(rs, cvoff, csoff, 16); cissued = true; }
                SCHED_FENCE();
            }
            MF4(1, 7, 0)
            asm volatile("s_nop 7" ::: "memory");
            put_partial<7>(vpart, acc[7]); put_partial_flag<7>(vflag, vready);
#undef MF1
#undef MF4
#undef MFA
            __builtin_amdgcn_s_setprio(0);
            if (k == 0 && mytile >= KLOAD_SPLIT) {
                lds_wait_ge_slow(lds, SY_KREADY, 2);
                load_afrags(A, lds, L_KB, ch, h, l);
            }
        } else if (w >= 6) {
            const int ch = l & 1, tl = 32 * (l >> 3) + 8 * ((l >> 1) & 3);
            const float dco = *(const float*)(lds + L_DCO + (k == 0 ? 0 : 8) + ch * 4);
            __builtin_amdgcn_s_setprio(3);
#pragma unroll 1
            for (int c = w & 1; c < 8; c += 2) {
                if (c >= 6) {
                    const u32 pub = __builtin_amdgcn_readfirstlane(lds_ld(lds, SY_PUB)), allf = __builtin_amdgcn_readfirstlane(lds_ld(lds, SY_ALLF));
                    const u32 pubt = __builtin_amdgcn_readfirstlane(lds_ld(lds, SY_PUBT));
                    if (pub >= (u32)(k + 1) && allf && now_ticks() - pubt >= C_DELAY) { cg = __builtin_amdgcn_raw_buffer_load_b128(rs, cvoff, csoff, 16); cissued = true; }
                }
                const int sb = (l >> 1) & 3, pa = 2 * (l >> 3) + (sb >> 1);
                const unsigned char* pp2 = lds + L_PART + c * PART_TILE + ch * PART_CH + 16 * (32 * (sb & 1) + ((pa + 8 * (sb & 1)) & 15));
                v4f a0, a1, b0, b1;
                v4u u;
                for (u32 it = 0;; ++it) {
                    asm volatile("" ::: "memory");
                    const v4u f = *(const v4u*)(lds + SY_PART + 16 * c);
                    asm volatile("" ::: "memory");
                    a0 = *(const v4f*)pp2; a1 = *(const v4f*)(pp2 + 256);
                    b0 = *(const v4f*)(pp2 + 1024); b1 = *(const v4f*)(pp2 + 1280);
                    u = *(const v4u*)(lds + L_UR + ch * UR_PITCH + 2 * (2040 - (256 * c + tl)));
                    const u32 fa = f.x < f.y ? f.x : f.y, fb = f.z < f.w ? f.z : f.w;
                    if ((u32)__builtin_amdgcn_readfirstlane(fa < fb ? fa : fb) >= (u32)(k + 1)) break;
                    if (spin_give_up(lds, it, LDS_SPIN_CAP)) break;
                }
                v4u o;
                o.x = no_poison(pack_bf(vfma(dco, bf_hi(u.w), vadd(a0.x, b0.x)), vfma(dco, bf_lo(u.w), vadd(a0.y, b0.y))));
                o.y = no_poison(pack_bf(vfma(dco, bf_hi(u.z), vadd(a0.z, b0.z)), vfma(dco, bf_lo(u.z), vadd(a0.w, b0.w))));
                o.z = no_poison(pack_bf(vfma(dco, bf_hi(u.y), vadd(a1.x, b1.x)), vfma(dco, bf_lo(u.y), vadd(a1.y, b1.y))));
                o.w = no_poison(pack_bf(vfma(dco, bf_hi(u.x), vadd(a1.z, b1.z)), vfma(dco, bf_lo(u.x), vadd(a1.w, b1.w))));
                store16(o, rs, (8 * c + (l >> 3)) * C_PITCH + me * 128 + (l & 7) * 16, WS_C + k * C_SIZE);
                if (c == mytile) { lds_flag_set(SY_PUBT, now_ticks()); lds_flag_set(SY_PUB, (u32)(k + 1)); }
                if (c < 2) STAMP(8 + 8 * k);
                if (c >= 6) STAMP(9 + 8 * k);
            }
            __builtin_amdgcn_s_setprio(2);
        }

        asm volatile("" : "+v"(i));
        STAMP(10 + 8 * k);
        float x[8], y[8], n0[8], dm, rsd;
        float piv;
        v4u yap = {0, 0, 0, 0};
        if (!cissued) {
            if (k == 0) lds_wait_ge(lds, SY_ALLF, 1u);
            lds_wait_ge(lds, SY_PUB, (u32)(k + 1));
            wait_until(__builtin_amdgcn_readfirstlane(lds_ld(lds, SY_PUBT)), C_DELAY);
            cg = __builtin_amdgcn_raw_buffer_load_b128(rs, cvoff, csoff, 16);
        }
        STAMP(11 + 8 * k);
        SCHED_FENCE();
        if (k == 0) {
            store16(poison4, rs, (i >> 3) * C_PITCH + me * 128 + (i & 7) * 16, WS_C + C_SIZE);
            store16(poison4, rs, myr * U_PITCH + i * 16, WS_U + U_SIZE);
            lds_wait_ge(lds, SY_X0, 2);
#pragma unroll
            for (int t = 0; t < 8; ++t) {
                const u32 xn = *(const u32*)(lds + L_XN + t * 2048 + i * 4);
                x[t] = bf_lo(xn);
                n0[t] = bf_hi(xn);
            }
            piv = *(const float*)(lds + L_PIV + (l & 7) * 4);
        } else {
            if (k == 1) store16(poison4, rs, (i >> 3) * C_PITCH + me * 128 + (i & 7) * 16, WS_C + 2 * C_SIZE);
            const v4f xa = *(const v4f*)(lds + L_PARK + i * 16), xb = *(const v4f*)(lds + L_PARK + 8192 + i * 16);
            x[0] = xa.x; x[1] = xa.y; x[2] = xa.z; x[3] = xa.w; x[4] = xb.x; x[5] = xb.y; x[6] = xb.z; x[7] = xb.w;
            yap = *(const v4u*)(lds + L_PARK + 16384 + i * 16);
            piv = *(const float*)(lds + L_PARK + 24576 + i * 4);
        }
        v4u sel_lo, sel_hi;
        {
            const u32 one = 0x3F80u << (16 * (l & 1)), dsel = (l & 7) >> 1;
            const v4u pat = {dsel == 0 ? one : 0u, dsel == 1 ? one : 0u, dsel == 2 ? one : 0u, dsel == 3 ? one : 0u};
            const v4u zero = {0, 0, 0, 0};
            sel_lo = (l & 8) ? zero : pat;
            sel_hi = (l & 8) ? pat : zero;
        }
        {
            bool ok = valid16(cg);
            for (u32 it = 0; !__all(ok); ++it) {
                if (spin_give_up(lds, it * 4 + 3, GLB_SPIN_CAP * 4)) break;
                asm volatile("" ::: "memory");
                if (!ok) { cg = __builtin_amdgcn_raw_buffer_load_b128(rs, cvoff, csoff, 16); ok = valid16(cg); }
            }
        }
        STAMP(12 + 8 * k);
        const float c[8] = {bf_lo(cg.x), bf_hi(cg.x), bf_lo(cg.y), bf_hi(cg.y), bf_lo(cg.z), bf_hi(cg.z), bf_lo(cg.w), bf_hi(cg.w)};
        if (k == 2) {
#pragma unroll
            for (int t = 0; t < 8; t += 2) {
                const u32 o = pack_bf(x[t] + c[t], x[t + 1] + c[t + 1]);
                __builtin_amdgcn_raw_buffer_store_b16((u16)o, ro, i * 2 + t * (D_ * 2), myr * (8 * D_ * 2), OUT_AUX);
                __builtin_amdgcn_raw_buffer_store_b16((u16)(o >> 16), ro, i * 2 + (t + 1) * (D_ * 2), myr * (8 * D_ * 2), OUT_AUX);
            }
            break;
        }
        if (k == 0) {
#pragma unroll
            for (int t = 0; t < 8; ++t) x[t] = vfma(n0[t], c[t], x[t]);
            yap = cg;
        } else {
#pragma unroll
            for (int t = 0; t < 8; ++t) x[t] = vadd(x[t], c[t]);
            sub_rows(y, x, piv);
            row_stats(y, sel_lo, sel_hi, lds, L_RED + redsel * 512, w, l, dm, rsd, [] {});
            redsel ^= 1;
            norm_rows(y, rsd, dm * rsd);
            const float ya[8] = {bf_lo(yap.x), bf_hi(yap.x), bf_lo(yap.y), bf_hi(yap.y), bf_lo(yap.z), bf_hi(yap.z), bf_lo(yap.w), bf_hi(yap.w)};
#pragma unroll
            for (int t = 0; t < 8; ++t) x[t] = vfma(y[t], ya[t], x[t]);
            piv += dm;
        }
        sub_rows(y, x, piv);
        WAIT_VM0();
        auto tail = [&](auto npre_c) {
            constexpr int NPRE = decltype(npre_c)::value;
            const u32 gsoff = WS_U + k * U_SIZE;
            const u32 gvoff = (32 * (w & 1) + (l >> 1)) * U_PITCH + (2 * me + (l & 1)) * 16;
            v4u d[4];
            row_stats(y, sel_lo, sel_hi, lds, L_RED + redsel * 512, w, l, dm, rsd, [&] {
                if (NPRE >= 1) d[0] = __builtin_amdgcn_raw_buffer_load_b128(rs, gvoff, gsoff, 16);
                SCHED_FENCE();
            });
            norm_rows(y, rsd, dm * rsd);
            v4u uo;
            uo.x = no_poison(pack_bf(y[7], y[6]));
            uo.y = no_poison(pack_bf(y[5], y[4]));
            uo.z = no_poison(pack_bf(y[3], y[2]));
            uo.w = no_poison(pack_bf(y[1], y[0]));
            SCHED_FENCE();
#pragma unroll
            for (int t = 1; t < NPRE; ++t) d[t] = __builtin_amdgcn_raw_buffer_load_b128(rs, gvoff + t * 64 * U_PITCH, gsoff, 16);
            SCHED_FENCE();
            store16(uo, rs, myr * U_PITCH + i * 16, WS_U + k * U_SIZE);
            const u32 ustore_t = NPRE >= 0 ? now_ticks() : 0u;
            piv += dm;
            *(v4f*)(lds + L_PARK + i * 16) = (v4f){x[0], x[1], x[2], x[3]};
            *(v4f*)(lds + L_PARK + 8192 + i * 16) = (v4f){x[4], x[5], x[6], x[7]};
            *(v4u*)(lds + L_PARK + 16384 + i * 16) = yap;
            *(float*)(lds + L_PARK + 24576 + i * 4) = piv;
            STAMP(13 + 8 * k);
            if (NPRE >= 0) {
                __builtin_amdgcn_s_setprio(3);
                unsigned char* dst = lds + L_UR + (l & 1) * UR_PITCH + 2 * (2040 - 8 * (32 * (w & 1) + (l >> 1)));
#pragma unroll
                for (int t = 0; t < 4; ++t) {
                    const int q = 2 * t + (w & 1);
                    if (t == NPRE) {
                        wait_until(ustore_t, U_DELAY);
#pragma unroll
                        for (int u = t; u < 4; ++u) {
                            d[u] = poison4;
                            if (2 * u + (w & 1) <= mytile + GATHER_AHEAD) d[u] = __builtin_amdgcn_raw_buffer_load_b128(rs, gvoff + u * 64 * U_PITCH, gsoff, 16);
                        }
                    }
                    if (__builtin_expect(!__all(valid16(d[t])), 0)) {
                        for (u32 it = 0; !__all(valid16(d[t])); ++it) {
                            if (spin_give_up(lds, it * 4 + 3, GLB_SPIN_CAP * 4)) break;
                            asm volatile("" ::: "memory");
                            if (!valid16(d[t])) d[t] = __builtin_amdgcn_raw_buffer_load_b128(rs, gvoff + t * 64 * U_PITCH, gsoff, 16);
                        }
                    }
                    *(v4u*)(dst - t * 1024) = d[t];
                    lds_flag_set(SY_INTILE + 4 * q, (u32)(k + 2));
                    if (t == 0) STAMP(16 + 8 * k);
                    if (t == 3) STAMP(17 + 8 * k);
                }
                WAIT_VM0();
                __builtin_amdgcn_s_setprio(2);
            }
        };
        if (w == 4 || w == 5) {
            if (mytile >= 6) tail(std::integral_constant<int, 3>());
            else if (mytile >= 4) tail(std::integral_constant<int, 2>());
            else if (mytile >= 2) tail(std::integral_constant<int, 1>());
            else tail(std::integral_constant<int, 0>());
        } else {
            tail(std::integral_constant<int, -1>());
        }
        redsel ^= 1;
    }
    asm volatile(".Lh3s4_end:");
}

extern "C" void kernel_launch(void* const* d_in, const int* in_sizes, int n_in, void* d_out, int out_size, void* d_ws, size_t ws_size,
                              hipStream_t stream) {
    (void)in_sizes; (void)n_in; (void)out_size; (void)ws_size;
    h3s4_kernel<<<256, 512, 0, stream>>>(
        (const u16*)d_in[0], (const u16*)d_in[1], (const u16*)d_in[2], (const u16*)d_in[3], (const u16*)d_in[4], (const u16*)d_in[5],
        (const u16*)d_in[7], (const u16*)d_in[8], (const u16*)d_in[9], (const u16*)d_in[10], (const u16*)d_in[11], (const u16*)d_in[12],
        (const u16*)d_in[13], (const u16*)d_in[14], (u16*)d_out, (unsigned char*)d_ws);
}
```

```cpp
#include <hip/hip_runtime.h>
#include <stdint.h>
#include <type_traits>

typedef unsigned short u16;
typedef unsigned u32;
typedef u32 v4u __attribute__((ext_vector_type(4)));
typedef float v2f __attribute__((ext_vector_type(2)));
typedef float v4f __attribute__((ext_vector_type(4)));
typedef __bf16 v2bf __attribute__((ext_vector_type(2)));
typedef __bf16 v8bf __attribute__((ext_vector_type(8)));
typedef _Float16 v2h __attribute__((ext_vector_type(2)));
typedef _Float16 v8h __attribute__((ext_vector_type(8)));

constexpr int S_ = 2048, D_ = 512, NS_ = 128;
constexpr u32 POISON = 0xFFFFFFFFu;

constexpr u32 WS_EPOCH = 0;
constexpr u32 WS_FLAGS = 4096;
constexpr u32 KV_PITCH = 4096 + 128;
constexpr u32 WS_FLAGP = WS_FLAGS + 256 * 128;
constexpr u32 WS_KV = WS_FLAGP + 256 * 128 + 4096;
constexpr u32 C_PITCH = 32768 + 128;
constexpr u32 C_SIZE = 64 * C_PITCH;
constexpr u32 WS_C = WS_KV + 512 * KV_PITCH;
constexpr u32 U_PITCH = 8192 + 128;
constexpr u32 U_SIZE = 256 * U_PITCH;
constexpr u32 WS_U = WS_C + 3 * C_SIZE;
constexpr u32 WS_TOTAL = WS_U + 2 * U_SIZE;

constexpr int UR_PITCH = 4608;
constexpr int L_UR = 0;
constexpr int PART_CH = 2048 + 64;
constexpr int PART_TILE = 2 * PART_CH;
constexpr int L_PART = L_UR + 2 * UR_PITCH;
constexpr int KC_COPY = 4160;
constexpr int KC_CH = 2 * KC_COPY;
constexpr int L_KA = L_PART + 8 * PART_TILE;
constexpr int L_KB = L_KA + 2 * KC_CH;
constexpr int KT_CH = 648 * 4;
constexpr int L_KT = L_KB + 2 * KC_CH;
constexpr int TAB_ROW = 528;
constexpr int TAB_CH = 57 * TAB_ROW;
constexpr int L_TAB = L_KT + 2 * KT_CH;
constexpr int L_T = L_TAB + 2 * TAB_CH;
constexpr int L_XN = L_T;
constexpr int L_PARK = L_TAB;
constexpr int L_RED = L_T + 16384;
constexpr int L_SYNC = L_RED + 2 * 512;
constexpr int SY_PART = L_SYNC;
constexpr int SY_INTILE = L_SYNC + 128;
constexpr int SY_KV = L_SYNC + 160;
constexpr int SY_GO = L_SYNC + 164;
constexpr int SY_TAB = L_SYNC + 168;

constexpr int SY_KREADY = L_SYNC + 184;
constexpr int SY_ABORT = L_SYNC + 188;
constexpr int SY_TAG = L_SYNC + 192;
constexpr int SY_ALLF = L_SYNC + 196;
constexpr int SY_PUB = L_SYNC + 200;
constexpr int SY_X0 = L_SYNC + 204;
constexpr int SY_KA = L_SYNC + 176;
constexpr int SY_TAGSET = L_SYNC + 180;
constexpr int SY_POIS = L_SYNC + 164 + 44;
constexpr int SY_PUBT = L_SYNC + 212;
constexpr int L_DCO = L_SYNC + 224;
constexpr int L_PIV = L_DCO + 32;
constexpr int LDS_BYTES = L_PIV + 32;
static_assert(LDS_BYTES > 82 * 1024 && LDS_BYTES <= 160 * 1024, "one workgroup per CU");

#define GATHER_AHEAD 1
#define ALLF_TILE 1
#define KV_DELAY 0
#define KV_BACKOFF 0
#define YA_POISON_AT 1
#define ALLF_DELAY 30
#define C_DELAY 65
#define U_DELAY 55
#define PUB_EXTRA 0
#define GATH_EXTRA 0
#define POLL_BACKOFF 0
#define PUB_SLEEP 0
#define GATH_SLEEP 0
#define KLOAD_SPLIT 5
#define GATHER_LAG 2
#define GATHER_LOOK 0
#define OUT_AUX 16
#define CODE_PREFETCH 1
constexpr u32 LDS_SPIN_CAP = 1u << 19;
constexpr u32 GLB_SPIN_CAP = 1u << 16;

#define STAMP(n) do {} while (0)
#define STAMPC(n) do {} while (0)
#define SCHED_FENCE() __builtin_amdgcn_sched_barrier(0)
#define WAIT_VM0() do { asm volatile("" ::: "memory"); __builtin_amdgcn_s_waitcnt(0x0F70); asm volatile("" ::: "memory"); } while (0)

__device__ __forceinline__ float bf_lo(u32 x) { return __builtin_bit_cast(float, x << 16); }
__device__ __forceinline__ float bf_hi(u32 x) { return __builtin_bit_cast(float, x & 0xFFFF0000u); }
__device__ __forceinline__ float bf2f(u16 x) { return __builtin_bit_cast(float, ((u32)x) << 16); }
__device__ __forceinline__ u32 pack_bf(float lo, float hi) {
    v2f f = {lo, hi};
    return __builtin_bit_cast(u32, __builtin_convertvector(f, v2bf));
}
__device__ __forceinline__ u32 pack_h(v2f f) { return __builtin_bit_cast(u32, __builtin_convertvector(f, v2h)); }
__device__ __forceinline__ u32 no_poison(u32 x) { return x < 0xFFFFFFFEu ? x : 0xFFFFFFFEu; }
template <int CTRL> __device__ __forceinline__ float dppf(float x) {
    return __builtin_bit_cast(float, __builtin_amdgcn_mov_dpp(__builtin_bit_cast(int, x), CTRL, 0xf, 0xf, true));
}
template <int CTRL> __device__ __forceinline__ u32 dppu(u32 x) { return (u32)__builtin_amdgcn_mov_dpp((int)x, CTRL, 0xf, 0xf, true); }
__device__ __forceinline__ float f_of(u32 x) { return __builtin_bit_cast(float, x); }
__device__ __forceinline__ u32 u_of(float x) { return __builtin_bit_cast(u32, x); }

__device__ __forceinline__ u32 lds_ld(unsigned char* lds, int off) {
    return __hip_atomic_load((u32*)(lds + off), __ATOMIC_RELAXED, __HIP_MEMORY_SCOPE_WORKGROUP);
}
__device__ __forceinline__ void lds_st(unsigned char* lds, int off, u32 v) {
    __hip_atomic_store((u32*)(lds + off), v, __ATOMIC_RELAXED, __HIP_MEMORY_SCOPE_WORKGROUP);
}
__device__ __forceinline__ void lds_flag_set(int off, u32 v) {
    asm volatile("s_mov_b64 exec, 1\n\tds_write_b32 %0, %1\n\ts_mov_b64 exec, -1" ::"v"(off), "v"(v) : "memory");
}
template <int Q> __device__ __forceinline__ void put_partial(u32 vpart, const v4f& acc) {
    asm volatile("ds_write_b128 %0, %1 offset:%c2" ::"v"(vpart), "v"(acc), "i"(Q * PART_TILE) : "memory");
}
template <int Q> __device__ __forceinline__ void put_partial_flag(u32 vflag, u32 v) {
    asm volatile("ds_write_b32 %0, %1 offset:%c2" ::"v"(vflag), "v"(v), "i"(Q * 16) : "memory");
}
__device__ __forceinline__ void lds_flag_inc(int off) {
    asm volatile("s_mov_b64 exec, 1\n\tds_add_u32 %0, %1\n\ts_mov_b64 exec, -1" ::"v"(off), "v"(1u) : "memory");
}
__device__ __forceinline__ bool spin_give_up(unsigned char* lds, u32 it, u32 cap) {
    if ((it & 63u) != 63u) return false;
    if (it > cap) { lds_st(lds, SY_ABORT, 1u); return true; }
    return __builtin_amdgcn_readfirstlane(lds_ld(lds, SY_ABORT)) != 0;
}
__device__ __forceinline__ void lds_wait_ge(unsigned char* lds, int off, u32 target) {
    if (__builtin_expect((u32)__builtin_amdgcn_readfirstlane(lds_ld(lds, off)) < target, 0)) {
        for (u32 it = 0; (u32)__builtin_amdgcn_readfirstlane(lds_ld(lds, off)) < target; ++it)
            if (spin_give_up(lds, it, LDS_SPIN_CAP)) break;
    }
    asm volatile("" ::: "memory");
}
__device__ __forceinline__ void lds_wait_ge_slow(unsigned char* lds, int off, u32 target) {
    for (u32 it = 0; (u32)__builtin_amdgcn_readfirstlane(lds_ld(lds, off)) < target; ++it) {
        if (spin_give_up(lds, it, LDS_SPIN_CAP)) break;
        __builtin_amdgcn_s_sleep(1);
    }
    asm volatile("" ::: "memory");
}
__device__ __forceinline__ void lds_wait4_ge(unsigned char* lds, int off, u32 target) {
    for (u32 it = 0;; ++it) {
        asm volatile("" ::: "memory");
        const v4u f = *(const v4u*)(lds + off);
        const u32 a = f.x < f.y ? f.x : f.y, b = f.z < f.w ? f.z : f.w;
        if ((u32)__builtin_amdgcn_readfirstlane(a < b ? a : b) >= target) break;
        if (spin_give_up(lds, it, LDS_SPIN_CAP)) break;
    }
    asm volatile("" ::: "memory");
}
__device__ __forceinline__ void store16(v4u v, __amdgpu_buffer_rsrc_t rs, u32 voff, u32 soff) {
    __builtin_amdgcn_raw_buffer_store_b128(v, rs, voff + soff, 0, 16);
}
__device__ __forceinline__ u32 now_ticks() { return (u32)__builtin_amdgcn_s_memrealtime(); }
__device__ __forceinline__ void wait_until(u32 t0, u32 delay) {
    for (u32 it = 0; now_ticks() - t0 < delay && it < 4096; ++it) __builtin_amdgcn_s_sleep(1);
}
__device__ __forceinline__ bool valid16(v4u r) {
    u32 m = r.x > r.y ? r.x : r.y;
    m = m > r.z ? m : r.z;
    m = m > r.w ? m : r.w;
    return m != POISON;
}
__device__ __forceinline__ v4u poll16(__amdgpu_buffer_rsrc_t rs, u32 voff, u32 soff, unsigned char* lds) {
    v4u r = __builtin_amdgcn_raw_buffer_load_b128(rs, voff, soff, 16);
    bool ok = valid16(r);
    for (u32 it = 0; !__all(ok); ++it) {
        if (spin_give_up(lds, it * 4 + 3, GLB_SPIN_CAP * 4)) break;
        asm volatile("" ::: "memory");
        if (!ok) { r = __builtin_amdgcn_raw_buffer_load_b128(rs, voff, soff, 16); ok = valid16(r); }
    }
    return r;
}

__device__ __forceinline__ float vmul(float a, float b) {
    float r;
    asm("v_mul_f32 %0, %1, %2" : "=v"(r) : "v"(a), "v"(b));
    return r;
}
#define DPPB(t) " row_newbcast:" #t " row_mask:0xf bank_mask:0xf\n\t"
__device__ __forceinline__ void sub_rows(float (&y)[8], const float (&x)[8], float p) {
    asm("s_nop 1\n\t"
        "v_subrev_f32_dpp %0, %16, %8" DPPB(0) "v_subrev_f32_dpp %1, %16, %9" DPPB(1) "v_subrev_f32_dpp %2, %16, %10" DPPB(2)
        "v_subrev_f32_dpp %3, %16, %11" DPPB(3) "v_subrev_f32_dpp %4, %16, %12" DPPB(4) "v_subrev_f32_dpp %5, %16, %13" DPPB(5)
        "v_subrev_f32_dpp %6, %16, %14" DPPB(6) "v_subrev_f32_dpp %7, %16, %15" DPPB(7)
        : "=&v"(y[0]), "=&v"(y[1]), "=&v"(y[2]), "=&v"(y[3]), "=&v"(y[4]), "=&v"(y[5]), "=&v"(y[6]), "=&v"(y[7])
        : "v"(x[0]), "v"(x[1]), "v"(x[2]), "v"(x[3]), "v"(x[4]), "v"(x[5]), "v"(x[6]), "v"(x[7]), "v"(p));
}
__device__ __forceinline__ void norm_rows(float (&y)[8], float r, float m) {
    asm("s_nop 1\n\t"
        "v_mul_f32_dpp %0, %8, %0" DPPB(0) "v_mul_f32_dpp %1, %8, %1" DPPB(1) "v_mul_f32_dpp %2, %8, %2" DPPB(2) "v_mul_f32_dpp %3, %8, %3" DPPB(3)
        "v_mul_f32_dpp %4, %8, %4" DPPB(4) "v_mul_f32_dpp %5, %8, %5" DPPB(5) "v_mul_f32_dpp %6, %8, %6" DPPB(6) "v_mul_f32_dpp %7, %8, %7" DPPB(7)
        "v_subrev_f32_dpp %0, %9, %0" DPPB(0) "v_subrev_f32_dpp %1, %9, %1" DPPB(1) "v_subrev_f32_dpp %2, %9, %2" DPPB(2)
        "v_subrev_f32_dpp %3, %9, %3" DPPB(3) "v_subrev_f32_dpp %4, %9, %4" DPPB(4) "v_subrev_f32_dpp %5, %9, %5" DPPB(5)
        "v_subrev_f32_dpp %6, %9, %6" DPPB(6) "v_subrev_f32_dpp %7, %9, %7" DPPB(7)
        : "+v"(y[0]), "+v"(y[1]), "+v"(y[2]), "+v"(y[3]), "+v"(y[4]), "+v"(y[5]), "+v"(y[6]), "+v"(y[7])
        : "v"(r), "v"(m));
}
#undef DPPB
__device__ __forceinline__ void dot_rows(float (&x)[8], const u32 (&p)[4], const u32 (&m)[8]) {
    asm("v_dot2_f32_bf16 %0, %8, %12, %0\n\tv_dot2_f32_bf16 %1, %8, %13, %1\n\t"
        "v_dot2_f32_bf16 %2, %9, %14, %2\n\tv_dot2_f32_bf16 %3, %9, %15, %3\n\t"
        "v_dot2_f32_bf16 %4, %10, %16, %4\n\tv_dot2_f32_bf16 %5, %10, %17, %5\n\t"
        "v_dot2_f32_bf16 %6, %11, %18, %6\n\tv_dot2_f32_bf16 %7, %11, %19, %7\n\ts_nop 2"
        : "+v"(x[0]), "+v"(x[1]), "+v"(x[2]), "+v"(x[3]), "+v"(x[4]), "+v"(x[5]), "+v"(x[6]), "+v"(x[7])
        : "v"(p[0]), "v"(p[1]), "v"(p[2]), "v"(p[3]), "v"(m[0]), "v"(m[1]), "v"(m[2]), "v"(m[3]), "v"(m[4]), "v"(m[5]), "v"(m[6]), "v"(m[7]));
}
__device__ __forceinline__ void fma_rows(float (&x)[8], const float (&a)[8], const float (&b)[8]) {
    asm("v_fmac_f32 %0, %8, %16\n\tv_fmac_f32 %1, %9, %17\n\tv_fmac_f32 %2, %10, %18\n\tv_fmac_f32 %3, %11, %19\n\t"
        "v_fmac_f32 %4, %12, %20\n\tv_fmac_f32 %5, %13, %21\n\tv_fmac_f32 %6, %14, %22\n\tv_fmac_f32 %7, %15, %23"
        : "+v"(x[0]), "+v"(x[1]), "+v"(x[2]), "+v"(x[3]), "+v"(x[4]), "+v"(x[5]), "+v"(x[6]), "+v"(x[7])
        : "v"(a[0]), "v"(a[1]), "v"(a[2]), "v"(a[3]), "v"(a[4]), "v"(a[5]), "v"(a[6]), "v"(a[7]),
          "v"(b[0]), "v"(b[1]), "v"(b[2]), "v"(b[3]), "v"(b[4]), "v"(b[5]), "v"(b[6]), "v"(b[7]));
}
__device__ __forceinline__ float vfma(float a, float b, float c) {
    float r;
    asm("v_fma_f32 %0, %1, %2, %3" : "=v"(r) : "v"(a), "v"(b), "v"(c));
    return r;
}
__device__ __forceinline__ float vadd(float a, float b) {
    float r;
    asm("v_add_f32 %0, %1, %2" : "=v"(r) : "v"(a), "v"(b));
    return r;
}

template <class Hook>
__device__ __forceinline__ void row_stats(const float (&y)[8], const v4u& sel_lo, const v4u& sel_hi, unsigned char* lds, u32 red_rd, u32 red_wr,
                                          float& dmean, float& rstd, Hook after_barrier) {
    const v4u a = {pack_bf(y[0], y[1]), pack_bf(y[2], y[3]), pack_bf(y[4], y[5]), pack_bf(y[6], y[7])};
    float q[8];
    asm("v_mul_f32 %0, %8, %8\n\tv_mul_f32 %1, %9, %9\n\tv_mul_f32 %2, %10, %10\n\tv_mul_f32 %3, %11, %11\n\t"
        "v_mul_f32 %4, %12, %12\n\tv_mul_f32 %5, %13, %13\n\tv_mul_f32 %6, %14, %14\n\tv_mul_f32 %7, %15, %15"
        : "=&v"(q[0]), "=&v"(q[1]), "=&v"(q[2]), "=&v"(q[3]), "=&v"(q[4]), "=&v"(q[5]), "=&v"(q[6]), "=&v"(q[7])
        : "v"(y[0]), "v"(y[1]), "v"(y[2]), "v"(y[3]), "v"(y[4]), "v"(y[5]), "v"(y[6]), "v"(y[7]));
    const v4u s = {pack_bf(q[0], q[1]), pack_bf(q[2], q[3]), pack_bf(q[4], q[5]), pack_bf(q[6], q[7])};
    v4f d = {0, 0, 0, 0};
    d = __builtin_amdgcn_mfma_f32_16x16x32_bf16(__builtin_bit_cast(v8bf, a), __builtin_bit_cast(v8bf, sel_lo), d, 0, 0, 0);
    d = __builtin_amdgcn_mfma_f32_16x16x32_bf16(__builtin_bit_cast(v8bf, s), __builtin_bit_cast(v8bf, sel_hi), d, 0, 0, 0);
    u32 t0 = u_of((d.x + d.z) + (d.y + d.w)), t1 = t0;
    {
        auto r = __builtin_amdgcn_permlane16_swap(t0, t1, false, false);
        const u32 r0 = r[0], r1 = r[1];
        t0 = u_of(f_of(r0) + f_of(r1)); t1 = t0;
    }
    {
        auto r = __builtin_amdgcn_permlane32_swap(t0, t1, false, false);
        const u32 r0 = r[0], r1 = r[1];
        t0 = u_of(f_of(r0) + f_of(r1));
    }
    *(u32*)(lds + red_wr) = t0;
    __syncthreads();
    after_barrier();
    const v4f p0 = *(const v4f*)(lds + red_rd), p1 = *(const v4f*)(lds + red_rd + 16);
    const v4f pp = p0 + p1;
    const float tot = (pp.x + pp.z) + (pp.y + pp.w);
    const float s2 = dppf<0x128>(tot);
    dmean = tot * (1.0f / D_);
    const float var = fmaxf(s2 * (1.0f / D_) - dmean * dmean, 0.0f);
    rstd = __builtin_amdgcn_rsqf(var + 1e-5f);
}
__device__ __forceinline__ float wave_sum(float v) {
    asm("s_nop 1" : "+v"(v));
    v += dppf<0x128>(v);
    v += dppf<0x141>(v);
    v += dppf<0xB1>(v);
    v += dppf<0x4E>(v);
    u32 a = u_of(v), b = a;
    {
        auto r = __builtin_amdgcn_permlane16_swap(a, b, false, false);
        const u32 r0 = r[0], r1 = r[1];
        a = u_of(f_of(r0) + f_of(r1)); b = a;
    }
    auto r = __builtin_amdgcn_permlane32_swap(a, b, false, false);
    const u32 r0 = r[0], r1 = r[1];
    return f_of(r0) + f_of(r1);
}

__device__ __forceinline__ void load_afrags(v4u (&A)[8][4], unsigned char* lds, int region, int ch, int h, int l) {
    const int b = l & 15, g = l >> 4, par = (1 + b) & 1;
    const unsigned char* base = lds + region + ch * KC_CH + par * KC_COPY + 256 * h + 16 * g + 2 * (1 + b - par);
#pragma unroll
    for (int p = 0; p < 8; ++p)
#pragma unroll
        for (int r = 0; r < 4; ++r) {
            const u32* q = (const u32*)(base + 512 * p + 64 * r);
            A[p][r] = (v4u){q[0], q[1], q[2], q[3]};
        }
}

__device__ __forceinline__ v2f cexp_pow(float al, float be, float m) {
    const float e = __builtin_amdgcn_exp2f(al * m);
    const float ph = __builtin_amdgcn_fractf(be * m);
    return (v2f){e * __builtin_amdgcn_cosf(ph), e * __builtin_amdgcn_sinf(ph)};
}
__device__ __forceinline__ v2f cmul(v2f a, v2f b) {
    v2f t;
    asm("v_pk_mul_f32 %0, %1, %2 op_sel:[0,0] op_sel_hi:[1,0]\n\t"
        "v_pk_fma_f32 %0, %1, %2, %0 op_sel:[1,1,0] op_sel_hi:[0,1,1] neg_lo:[1,0,0]"
        : "=&v"(t) : "v"(a), "v"(b));
    return t;
}
__device__ __forceinline__ v4f mfma_bf(v4u a, v4u b, v4f c) {
    return __builtin_amdgcn_mfma_f32_16x16x32_bf16(__builtin_bit_cast(v8bf, a), __builtin_bit_cast(v8bf, b), c, 0, 0, 0);
}

extern "C" __global__ void __launch_bounds__(512, 2)
h3s4_kernel(const u16* __restrict__ q_, const u16* __restrict__ k_, const u16* __restrict__ v_, const u16* __restrict__ shk_,
            const u16* __restrict__ ssmk_, const u16* __restrict__ ssmD_, const u16* __restrict__ Are_, const u16* __restrict__ Aim_,
            const u16* __restrict__ Bre_, const u16* __restrict__ Bim_, const u16* __restrict__ Cre_, const u16* __restrict__ Cim_,
            const u16* __restrict__ logdt_, const u16* __restrict__ Dskip_, u16* __restrict__ out_, unsigned char* ws_) {
    __shared__ __attribute__((aligned(16))) unsigned char lds[LDS_BYTES];
    asm volatile("" ::"s"(q_), "s"(k_), "s"(v_), "s"(shk_), "s"(ssmk_), "s"(ssmD_), "s"(Are_), "s"(Aim_), "s"(Bre_), "s"(Bim_),
                 "s"(Cre_), "s"(Cim_), "s"(logdt_), "s"(Dskip_), "s"(out_), "s"(ws_));
    const int i = threadIdx.x, l = i & 63;
    const int w = __builtin_amdgcn_readfirstlane(i >> 6);
    u32 code_touch = 0;
    if (w == 0) {
        unsigned long long pc;
        u32 left;
        asm volatile("s_getpc_b64 %0\n.Lh3s4_pc:\n\ts_mov_b32 %1, .Lh3s4_end-.Lh3s4_pc+1536" : "=s"(pc), "=s"(left));
        const u32 nl = (left >> 12) + 1, off = ((blockIdx.x >> 3) * nl + l) * 128;
        if ((u32)l < nl && off + 4 <= left) code_touch = *(const __attribute__((address_space(1))) u32*)((pc & ~3ull) + off);
    }
    const int xcd = blockIdx.x & 7, idx = blockIdx.x >> 3;
    const int me = xcd * 32 + idx;
    const int mytile = idx >> 2;
    const int myr = 32 * mytile + 4 * xcd + (idx & 3);
    const __amdgpu_buffer_rsrc_t rs = __builtin_amdgcn_make_buffer_rsrc(ws_, 0, WS_TOTAL, 0x00020000);
    const v4u poison4 = {POISON, POISON, POISON, POISON};
    STAMP(0);

    const int cp = i & 31, rb = i >> 5;
    const int pcg = (xcd + idx) & 7;
    const int R0 = 64 * idx + 4 * rb, pch = 64 * pcg + 2 * cp;
    u32 kd[7], vd[4];
#pragma unroll
    for (int j = 0; j < 7; ++j) {
        const int Rj = R0 - 3 + j;
        kd[j] = *(const u32*)(k_ + (Rj < 0 ? 0 : Rj) * D_ + pch);
    }
#pragma unroll
    for (int j = 0; j < 4; ++j) vd[j] = *(const u32*)(v_ + (R0 + j) * D_ + pch);
    const v4u wd = *(const v4u*)(shk_ + pch * 4);
    u32 epoch = 0;
    if (w == 0) epoch = __builtin_amdgcn_raw_buffer_load_b32(rs, 0, WS_EPOCH, 16);
    v4u xq[4];

    if (i >= 256 && i < 256 + 64) *(v4u*)(lds + L_UR + ((i >> 5) & 1) * UR_PITCH + 4096 + (i & 31) * 16) = (v4u){0, 0, 0, 0};
    if (i >= 320 && i < 320 + 16) {
        const int e = i - 320;
        *(v4u*)(lds + L_KA + (e >> 1) * KC_COPY + (e & 1) * 16) = (v4u){0, 0, 0, 0};
    }
    if (i >= 384 && i < 384 + 56) *(u32*)(lds + L_SYNC + (i - 384) * 4) = 0;

    STAMP(1);
    {
#pragma unroll
        for (int j = 0; j < 3; ++j) if (R0 - 3 + j < 0) kd[j] = 0;
#pragma unroll
        for (int c = 0; c < 2; ++c) {
            const u32 sel = c ? 0x07060302u : 0x05040100u;
            const u32 w01 = wd[2 * c], w23 = wd[2 * c + 1];
            u32 P[7];
#pragma unroll
            for (int j = 1; j < 7; ++j) P[j] = __builtin_amdgcn_perm(kd[j - 1], kd[j], sel);
            float res[4];
#pragma unroll
            for (int t = 0; t < 4; ++t) {
                float a = __builtin_amdgcn_fdot2_f32_bf16(__builtin_bit_cast(v2bf, P[t + 3]), __builtin_bit_cast(v2bf, w01), 0.0f, false);
                a = __builtin_amdgcn_fdot2_f32_bf16(__builtin_bit_cast(v2bf, P[t + 1]), __builtin_bit_cast(v2bf, w23), a, false);
                res[t] = a * (c ? bf_hi(vd[t]) : bf_lo(vd[t]));
            }
            typedef u32 v2u __attribute__((ext_vector_type(2)));
            const int half = (1 - (rb & 1)) ^ ((cp >> 3) & 1);
            *(v2u*)(lds + L_T + (2 * cp + c) * 128 + (((rb >> 1) ^ (cp & 7)) * 16) + half * 8) = (v2u){pack_bf(res[3], res[2]), pack_bf(res[1], res[0])};
        }
    }
    SCHED_FENCE();
    if (w < 4) {
#pragma unroll
        for (int j = 0; j < 2; ++j) {
            const int e = i + 256 * j;
            store16(poison4, rs, (e >> 3) * C_PITCH + me * 128 + (e & 7) * 16, WS_C);
        }
    }
    u32 pp[11], dA, dB;
    if (w >= 4) {
        const int pc = 2 * me + ((w - 4) >> 1), pi = pc * NS_ + (i & 127), pj = pi ^ 64;
        pp[0] = Are_[pi]; pp[1] = Aim_[pi]; pp[2] = Bre_[pi]; pp[3] = Bim_[pi]; pp[4] = Cre_[pi]; pp[5] = Cim_[pi];
        pp[6] = Bre_[pj]; pp[7] = Bim_[pj]; pp[8] = Cre_[pj]; pp[9] = Cim_[pj];
        pp[10] = logdt_[pc];
        dA = ssmD_[2 * me + (l & 1)]; dB = Dskip_[2 * me + (l & 1)];
        if (w >= 6) {
#pragma unroll
            for (int j = 0; j < 4; ++j) xq[j] = *(const v4u*)(q_ + (8 * myr + 4 * (w & 1) + j) * D_ + l * 8);
        }
    }
    STAMP(2);
    __syncthreads();
    STAMP(3);

    v4u A[8][4];
    if (w < 4) {
#pragma unroll
        for (int j = 0; j < 2; ++j) {
            typedef u32 v2u __attribute__((ext_vector_type(2)));
            const int e = i + 256 * j, ch = e >> 3, chunk = e & 7, sw = (ch >> 4) & 1;
            const unsigned char* src = lds + L_T + ch * 128 + ((chunk ^ ((ch >> 1) & 7)) * 16);
            const v2u d0 = *(const v2u*)(src + 8 * sw), d1 = *(const v2u*)(src + 8 * (1 - sw));
            store16((v4u){d0.x, d0.y, d1.x, d1.y}, rs, (64 * pcg + ch) * KV_PITCH + (8 * idx + chunk) * 16, WS_KV);
        }
        SCHED_FENCE();
        asm volatile("" ::: "memory");
        v4u sk[3];
        u32 sknext[3];
#pragma unroll
        for (int j = 0; j < 3; ++j) {
            const int e = (i - 64) + 192 * j, ec = (w == 0 || e > 511) ? 0 : e;
            const u16* src = ssmk_ + (2 * me + (ec >> 8)) * S_ + (ec & 255) * 8;
            if (w != 0) {
                sk[j] = *(const v4u*)src;
                sknext[j] = *(const u32*)(src + ((ec & 255) < 255 ? 8 : 0));
            }
        }
        SCHED_FENCE();
        if (w == 0) asm volatile("s_waitcnt vmcnt(0)" ::: "memory");
        else asm volatile("s_waitcnt vmcnt(6)" ::: "memory");
        lds_flag_inc(SY_KV);
        STAMP(4);
        u32 tag = 0, tflag = 0;
        if (w == 0) {
            lds_wait_ge(lds, SY_KV, 4);
            tag = __builtin_amdgcn_readfirstlane(epoch) + 1u;
            if (l == 0) __builtin_amdgcn_raw_buffer_store_b32(tag, rs, me * 128, WS_FLAGS, 16);
            tflag = now_ticks();
            lds_flag_set(SY_TAG, tag);
            lds_flag_set(SY_TAGSET, 1u);
        }
        if (w != 0) {
#pragma unroll
            for (int j = 0; j < 3; ++j) {
                const int e = (i - 64) + 192 * j, sm = e & 255;
                if (e < 512) {
                    unsigned char* c0 = lds + L_KA + (e >> 8) * KC_CH;
                    const u32 nx = sm < 255 ? sknext[j] : 0u;
                    *(v4u*)(c0 + 32 + sm * 16) = sk[j];
                    *(v4u*)(c0 + KC_COPY + 32 + sm * 16) =
                        (v4u){__builtin_amdgcn_alignbit(sk[j].y, sk[j].x, 16), __builtin_amdgcn_alignbit(sk[j].z, sk[j].y, 16),
                              __builtin_amdgcn_alignbit(sk[j].w, sk[j].z, 16), __builtin_amdgcn_alignbit(nx, sk[j].w, 16)};
                    if (sm == 0) *(u32*)(c0 + KC_COPY + 28) = sk[j].x << 16;
                }
            }
            lds_flag_inc(SY_KA);
        }
        if (w == 0) {
            const u32 foff = (((xcd - (l & 31)) & 7) * 32 + (l & 31)) * 128;
            wait_until(tflag, KV_DELAY);
            for (u32 it = 0;; ++it) {
                asm volatile("" ::: "memory");
                if (__all(__builtin_amdgcn_raw_buffer_load_b32(rs, foff, WS_FLAGS, 16) == tag)) break;
                if (spin_give_up(lds, it * 4 + 3, GLB_SPIN_CAP * 4)) break;
            }
            lds_flag_set(SY_GO, 1u);
        } else {
            lds_wait_ge(lds, SY_KA, 3);
            load_afrags(A, lds, L_KA, w >> 1, w & 1, l);
            lds_wait_ge(lds, SY_GO, 1u);
        }
        STAMP(5);
        v4u g0, g1;
        {
            const u32 off = (2 * me + (l >> 5)) * KV_PITCH + (64 * w + (l & 31)) * 16;
            g0 = __builtin_amdgcn_raw_buffer_load_b128(rs, off, WS_KV, 16);
            g1 = __builtin_amdgcn_raw_buffer_load_b128(rs, off + 512, WS_KV, 16);
        }
        SCHED_FENCE();
        if (w == 0) {
            lds_wait_ge(lds, SY_KA, 3);
            load_afrags(A, lds, L_KA, 0, 0, l);
        }
        STAMP(6);
        unsigned char* dst = lds + L_UR + (l >> 5) * UR_PITCH + 2 * (2040 - 8 * (64 * w + (l & 31)));
        *(v4u*)dst = g0;
        *(v4u*)(dst - 512) = g1;
        lds_flag_set(SY_INTILE + 8 * w, 1u);
        lds_flag_set(SY_INTILE + 8 * w + 4, 1u);
        WAIT_VM0();

    } else {
        __builtin_amdgcn_s_setprio(2);
        const int wa = w - 4, ch = wa >> 1, n = (i - 256) & 127;
#pragma unroll
        for (int j = 0; j < 11; ++j) asm volatile("" : "+v"(pp[j]));
        asm volatile("" : "+v"(dA), "+v"(dB));
        const float ex = expf(f_of(pp[10] << 16));
        const float dt = bf_lo(pack_bf(ex, ex));
        const float al = dt * f_of(pp[0] << 16) * 1.44269504088896341f, be = dt * f_of(pp[1] << 16) * 0.15915494309189535f;
        v2f W;
        float inv;
        {
            const float br = dt * f_of(pp[2] << 16), bi = dt * f_of(pp[3] << 16), cr = f_of(pp[4] << 16), ci = f_of(pp[5] << 16);
            const float br2 = dt * f_of(pp[6] << 16), bi2 = dt * f_of(pp[7] << 16), cr2 = f_of(pp[8] << 16), ci2 = f_of(pp[9] << 16);
            W = (v2f){cr * br - ci * bi, cr * bi + ci * br};
            float mx = fmaxf(fmaxf(fabsf(W.x), fabsf(W.y)), fmaxf(fabsf(cr2 * br2 - ci2 * bi2), fabsf(cr2 * bi2 + ci2 * br2)));
            mx = fmaxf(mx, dppf<0x128>(mx));
            mx = fmaxf(mx, dppf<0x141>(mx));
            mx = fmaxf(mx, dppf<0xB1>(mx));
            mx = fmaxf(mx, dppf<0x4E>(mx));
            float m0 = f_of((u32)__builtin_amdgcn_readlane((int)u_of(mx), 0)), m1 = f_of((u32)__builtin_amdgcn_readlane((int)u_of(mx), 16));
            float m2 = f_of((u32)__builtin_amdgcn_readlane((int)u_of(mx), 32)), m3 = f_of((u32)__builtin_amdgcn_readlane((int)u_of(mx), 48));
            const int e = __builtin_amdgcn_frexp_expf(fmaxf(fmaxf(m0, m1), fmaxf(m2, m3)));
            W = (v2f){__builtin_amdgcn_ldexpf(W.x, 14 - e), __builtin_amdgcn_ldexpf(W.y, 14 - e)};
            inv = __builtin_amdgcn_ldexpf(1.0f, e - 14);
        }
        unsigned char* tab = lds + L_TAB + ch * TAB_CH + n * 4;
#define TAB_PUT(row, val) (*(u32*)(tab + (row) * TAB_ROW) = pack_h(val))
        const v2f E1 = cexp_pow(al, be, 1.0f), E8 = cexp_pow(al, be, 8.0f), E128 = cexp_pow(al, be, 128.0f);
        const v2f E256 = cmul(E128, E128), E384 = cmul(E256, E128), E512 = cmul(E256, E256), E1024 = cmul(E512, E512);
        {
            const v2f E8c = {E8.x, -E8.y};
            v2f B = {W.x, -W.y}, P = {1.0f, 0.0f}, Q = E512, Rr = E1024, P4 = cmul(cmul(E1, E1), cmul(E1, E1));
#pragma unroll
            for (int r = 0; r < 8; ++r) {
                TAB_PUT(41 + 2 * r, B);
                B = cmul(B, E8c);
                TAB_PUT(42 + 2 * r, B);
                if (r < 7) B = cmul(B, E8c);
                TAB_PUT(r, P);
                TAB_PUT(8 + r, r ? cmul(P, E128) : E128);
                if ((r & 1) == 0) {
                    TAB_PUT(16 + (r >> 1), r ? cmul(P, E256) : E256);
                    TAB_PUT(20 + (r >> 1), r ? cmul(P, E384) : E384);
                }
                if (r < 7) P = r ? cmul(P, E1) : E1;
                TAB_PUT(32 + r, Rr);
                if (r < 7) Rr = cmul(Rr, E128);
                if (r < 4) {
                    TAB_PUT(24 + 2 * r, Q);
                    TAB_PUT(25 + 2 * r, cmul(Q, P4));
                    if (r < 3) Q = cmul(Q, E128);
                }
            }
            TAB_PUT(40, cmul(E1024, E1024));
        }
#undef TAB_PUT
        if (l == 0) *(float*)(lds + L_DCO + 16 + ch * 4) = inv;
        lds_flag_inc(SY_TAB + 4 * ch);
        if (l < 2) { *(u32*)(lds + L_DCO + l * 4) = dA << 16; *(u32*)(lds + L_DCO + 8 + l * 4) = dB << 16; }
        STAMP(4);
        if (w < 6) {
            const int kc = w & 1;
            lds_wait_ge(lds, SY_TAB + 4 * kc, 2);
            {
                const int row = l & 15, g = l >> 4;
                const unsigned char* tb = lds + L_TAB + kc * TAB_CH + row * TAB_ROW + g * 16;
                float* kt = (float*)(lds + L_KT + kc * KT_CH);
                const float scale = *(const float*)(lds + L_DCO + 16 + kc * 4);
                v4u fb[8], fa[8];
                v4f acc[3];
#pragma unroll
                for (int s2 = 0; s2 < 8; ++s2) fb[s2] = *(const v4u*)(tb + 41 * TAB_ROW + s2 * 64);
#pragma unroll
                for (int tile = 0; tile < 3; ++tile) {
#pragma unroll
                    for (int s2 = 0; s2 < 8; ++s2) fa[s2] = *(const v4u*)(tb + tile * 16 * TAB_ROW + s2 * 64);
                    acc[tile] = (v4f){0, 0, 0, 0};
#pragma unroll
                    for (int s2 = 0; s2 < 8; ++s2)
                        acc[tile] = __builtin_amdgcn_mfma_f32_16x16x32_f16(__builtin_bit_cast(v8h, fa[s2]), __builtin_bit_cast(v8h, fb[s2]), acc[tile], 0, 0, 0);
                }
                asm volatile("s_nop 7\n\ts_nop 1" : "+v"(acc[0]), "+v"(acc[1]), "+v"(acc[2]));
#pragma unroll
                for (int r = 0; r < 4; ++r) {
                    const int rw = 4 * g + r;
                    kt[128 * (rw >> 3) + (rw & 7) + 8 * row] = vmul(acc[0][r], scale);
                    kt[rw < 8 ? 256 + 64 * (rw >> 2) + (rw & 3) + 4 * row : 384 + 32 * ((rw - 8) >> 1) + (rw & 1) + 2 * row] = vmul(acc[1][r], scale);
                    kt[rw < 8 ? 512 + 16 * rw + row : ((rw == 8 && row == 0) ? 640 : 644 + (l & 3))] = vmul(acc[2][r], scale);
                }
            }
            asm volatile("s_waitcnt lgkmcnt(0)" ::: "memory");
            {
                const float* t = (const float*)(lds + L_KT + kc * KT_CH);
                unsigned char* c0 = lds + L_KB + kc * KC_CH;
                const float a2 = t[512 + l], b2 = t[513 + l], a3 = t[576 + l], b3 = t[577 + l];
                const float v0 = t[384 + 2 * l], v1 = t[385 + 2 * l], v2 = t[386 + 2 * l];
                float kvv[9];
#pragma unroll
                for (int j = 0; j < 9; ++j) {
                    const int lag = 8 * l + j;
                    const u32 bb = pack_bf((float)lag, 0.0f) & 0xFFFFu;
                    kvv[j] = t[l < 32 ? lag : (int)(bb - 0x4280u)];
                }
                {
                    const u32 e0 = pack_bf(kvv[0], kvv[1]);
                    *(v4u*)(c0 + 32 + l * 16) = (v4u){e0, pack_bf(kvv[2], kvv[3]), pack_bf(kvv[4], kvv[5]), pack_bf(kvv[6], kvv[7])};
                    *(v4u*)(c0 + KC_COPY + 32 + l * 16) = (v4u){pack_bf(kvv[1], kvv[2]), pack_bf(kvv[3], kvv[4]), pack_bf(kvv[5], kvv[6]), pack_bf(kvv[7], kvv[8])};
                    if (l == 0) *(u32*)(c0 + KC_COPY + 28) = e0 << 16;
                }
                {
                    const u32 p00 = pack_bf(v0, v0), p01 = pack_bf(v0, v1), p11 = pack_bf(v1, v1), p12 = pack_bf(v1, v2), p22 = pack_bf(v2, v2);
                    *(v4u*)(c0 + 32 + (64 + l) * 16) = (v4u){p00, p01, p11, p22};
                    *(v4u*)(c0 + KC_COPY + 32 + (64 + l) * 16) = (v4u){p00, p11, p12, p22};
                }
#pragma unroll
                for (int it = 0; it < 2; ++it) {
                    const float a = it ? a3 : a2, b = it ? b3 : b2, c4 = (l & 1) ? b : a;
                    const u32 paa = pack_bf(a, a), pbb = pack_bf(b, b);
                    *(v4u*)(c0 + 32 + (128 + 64 * it + l) * 16) = (v4u){paa, paa, pack_bf(c4, b), pbb};
                    *(v4u*)(c0 + KC_COPY + 32 + (128 + 64 * it + l) * 16) = (v4u){paa, pack_bf(a, c4), pbb, pbb};
                }
            }
            lds_flag_inc(SY_KREADY);
            STAMP(5);
            if (w == 4) {
                lds_wait_ge(lds, SY_TAGSET, 1u);
                const u32 tag = __builtin_amdgcn_readfirstlane(lds_ld(lds, SY_TAG));
                lds_wait_ge_slow(lds, SY_GO, 1u);
                wait_until(now_ticks(), ALLF_DELAY);
                for (u32 it = 0;; ++it) {
                    asm volatile("" ::: "memory");
                    if (it) __builtin_amdgcn_s_sleep(8);
                    const u32 f0 = __builtin_amdgcn_raw_buffer_load_b32(rs, l * 128, WS_FLAGS, 16);
                    const u32 f1 = __builtin_amdgcn_raw_buffer_load_b32(rs, (l + 64) * 128, WS_FLAGS, 16);
                    const u32 f2 = __builtin_amdgcn_raw_buffer_load_b32(rs, (l + 128) * 128, WS_FLAGS, 16);
                    const u32 f3 = __builtin_amdgcn_raw_buffer_load_b32(rs, (l + 192) * 128, WS_FLAGS, 16);
                    if (__all(f0 == tag && f1 == tag && f2 == tag && f3 == tag)) break;
                    if (spin_give_up(lds, it * 4 + 3, GLB_SPIN_CAP * 4)) break;
                }
                if (me == 0 && l == 0) __builtin_amdgcn_raw_buffer_store_b32(tag, rs, 0, WS_EPOCH, 16);
                lds_flag_set(SY_ALLF, 1u);
            }
            WAIT_VM0();
        } else {
            const int kr = w & 1;
            lds_wait_ge(lds, SY_KV, 4);
#pragma unroll
            for (int j = 0; j < 4; ++j) store16(poison4, rs, myr * U_PITCH + ((i & 127) + 128 * j) * 16, WS_U);
            STAMP(5);
#pragma unroll
            for (int j = 0; j < 4; ++j) asm volatile("" : "+v"(xq[j]));
            {
                float f[4][8], mean[4], rstd[4];
#pragma unroll
                for (int rr = 0; rr < 4; ++rr) {
                    const v4u xr = xq[rr];
                    f[rr][0] = bf_lo(xr.x); f[rr][1] = bf_hi(xr.x); f[rr][2] = bf_lo(xr.y); f[rr][3] = bf_hi(xr.y);
                    f[rr][4] = bf_lo(xr.z); f[rr][5] = bf_hi(xr.z); f[rr][6] = bf_lo(xr.w); f[rr][7] = bf_hi(xr.w);
                    mean[rr] = vadd(vadd(vadd(f[rr][0], f[rr][1]), vadd(f[rr][2], f[rr][3])), vadd(vadd(f[rr][4], f[rr][5]), vadd(f[rr][6], f[rr][7])));
                }
#pragma unroll
                for (int rr = 0; rr < 4; ++rr) mean[rr] = vmul(wave_sum(mean[rr]), -1.0f / D_);
#pragma unroll
                for (int rr = 0; rr < 4; ++rr) {
#pragma unroll
                    for (int j = 0; j < 8; ++j) f[rr][j] = vadd(f[rr][j], mean[rr]);
                    float q2 = vmul(f[rr][0], f[rr][0]);
#pragma unroll
                    for (int j = 1; j < 8; ++j) q2 = vfma(f[rr][j], f[rr][j], q2);
                    rstd[rr] = q2;
                }
#pragma unroll
                for (int rr = 0; rr < 4; ++rr) rstd[rr] = __builtin_amdgcn_rsqf(vfma(wave_sum(rstd[rr]), 1.0f / D_, 1e-5f));
                asm volatile("s_nop 1" : "+v"(rstd[0]), "+v"(rstd[1]), "+v"(rstd[2]), "+v"(rstd[3]));
#pragma unroll
                for (int rr = 0; rr < 4; ++rr) {
                    const v4u xr = xq[rr];
                    u32 o[8];
#pragma unroll
                    for (int j = 0; j < 4; ++j) {
                        const u32 np = pack_bf(vmul(f[rr][2 * j], rstd[rr]), vmul(f[rr][2 * j + 1], rstd[rr]));
                        o[2 * j] = __builtin_amdgcn_perm(np, xr[j], 0x05040100u);
                        o[2 * j + 1] = __builtin_amdgcn_perm(np, xr[j], 0x07060302u);
                    }
                    unsigned char* dstx = lds + L_XN + (4 * kr + rr) * 2048 + l * 32;
                    *(v4u*)dstx = (v4u){o[0], o[1], o[2], o[3]};
                    *(v4u*)(dstx + 16) = (v4u){o[4], o[5], o[6], o[7]};
                    if (l == 0) *(float*)(lds + L_PIV + (4 * kr + rr) * 4) = -mean[rr];
                }
            }
            lds_flag_inc(SY_X0);
            WAIT_VM0();
            lds_flag_inc(SY_POIS);
            lds_wait_ge(lds, SY_POIS, 2);
            STAMP(6);
        }
    }

    asm volatile("" ::"v"(code_touch));
    WAIT_VM0();
    const int i_ = i;
    const __amdgpu_buffer_rsrc_t ro = __builtin_amdgcn_make_buffer_rsrc(out_, 0, S_ * D_ * 2, 0x00020000);

#pragma unroll 1
    for (int k = 0; k < 3; ++k) {
        int i = i_;
        asm volatile("" : "+v"(i));
        const int l = i & 63;
        STAMP(7 + 8 * k);
        const u32 cvoff = (myr >> 2) * C_PITCH + (i >> 1) * 128 + (myr & 3) * 32 + (i & 1) * 16, csoff = WS_C + k * C_SIZE;
        v4u cg = poison4;
        bool cissued = false;
        if (w < 4) {
            const int ch = w >> 1, h = w & 1, a = l & 15, g = l >> 4;
            const unsigned char* ub = lds + L_UR + ch * UR_PITCH + 2 * (2032 + 128 * h - 16 * a + 8 * g) - 3584;
            const u32 ready = (u32)(k + 1);
            if (k == 1 && mytile < KLOAD_SPLIT) load_afrags(A, lds, L_KB, ch, h, l);
            v4f acc[8];
#pragma unroll
            for (int c = 0; c < 8; ++c) acc[c] = (v4f){0, 0, 0, 0};
#pragma unroll
            for (int c = 0; c < 8; ++c) asm volatile("" : "+v"(acc[c]));
            v4u B[2][4];
            __builtin_amdgcn_s_setprio(1);
            lds_wait_ge(lds, SY_INTILE, ready);
            STAMP(8 + 8 * k);
            if (k == 0) STAMPC(29);
#pragma unroll
            for (int r = 0; r < 4; ++r) B[0][r] = *(const v4u*)(ub + 512 * 7 + 64 * r);
#define MFA "v_mfma_f32_16x16x32_bf16 "
#define MF1(cur, c, p, r)                                                                                     \
    asm volatile(MFA "%0, %1, %2, %0" : "+v"(acc[c]) : "v"(A[p][r]), "v"(B[cur][r]));                          \
    SCHED_FENCE();
#define MF4(cur, c, p)                                                                                        \
    asm volatile(MFA "%0, %1, %5, %0\n\t" MFA "%0, %2, %6, %0\n\t" MFA "%0, %3, %7, %0\n\t" MFA "%0, %4, %8, %0" \
                 : "+v"(acc[c]) : "v"(A[p][0]), "v"(A[p][1]), "v"(A[p][2]), "v"(A[p][3]),                      \
                   "v"(B[cur][0]), "v"(B[cur][1]), "v"(B[cur][2]), "v"(B[cur][3]));                             \
    SCHED_FENCE();
            u32 vflag = SY_PART + 4 * w, vready = ready, vpart = L_PART + ch * PART_CH + h * 1024 + 16 * (16 * g + ((a + 8 * (g >> 1)) & 15));
            asm volatile("" : "+v"(vflag), "+v"(vready), "+v"(vpart));
            u32 fl = 0;
            MF1(0, 0, 0, 0)
            fl = lds_ld(lds, SY_INTILE + 4); SCHED_FENCE();
            MF1(0, 0, 0, 1)
            B[1][0] = *(const v4u*)(ub + 3072); SCHED_FENCE();
            MF1(0, 0, 0, 2)
            B[1][1] = *(const v4u*)(ub + 3136); SCHED_FENCE();
            MF1(0, 0, 0, 3)
            B[1][2] = *(const v4u*)(ub + 3200); SCHED_FENCE();
            MF1(0, 1, 1, 0)
            B[1][3] = *(const v4u*)(ub + 3264); SCHED_FENCE();
            MF1(0, 1, 1, 1)
            MF1(0, 1, 1, 2)
            put_partial<0>(vpart, acc[0]); SCHED_FENCE();
            MF1(0, 1, 1, 3)
            MF1(0, 2, 2, 0)
            put_partial_flag<0>(vflag, vready); SCHED_FENCE();
            MF1(0, 2, 2, 1)
            const u32 fls0 = (u32)__builtin_amdgcn_readfirstlane(fl); SCHED_FENCE();
            MF1(0, 2, 2, 2)
            MF1(0, 2, 2, 3)
            MF4(0, 3, 3)
            MF4(0, 4, 4)
            MF4(0, 5, 5)
            MF4(0, 6, 6)
            MF4(0, 7, 7)
            if (__builtin_expect(fls0 < ready, 0)) {
                lds_wait_ge(lds, SY_INTILE + 4, ready);
#pragma unroll
                for (int r = 0; r < 4; ++r) B[1][r] = *(const v4u*)(ub + 3072 + 64 * r);
            }
            asm volatile("" ::: "memory");
            SCHED_FENCE();
            MF1(1, 1, 0, 0)
            fl = lds_ld(lds, SY_INTILE + 8); SCHED_FENCE();
            MF1(1, 1, 0, 1)
            B[0][0] = *(const v4u*)(ub + 2560); SCHED_FENCE();
            MF1(1, 1, 0, 2)
            B[0][1] = *(const v4u*)(ub + 2624); SCHED_FENCE();
            MF1(1, 1, 0, 3)
            B[0][2] = *(const v4u*)(ub + 2688); SCHED_FENCE();
            MF1(1, 2, 1, 0)
            B[0][3] = *(const v4u*)(ub + 2752); SCHED_FENCE();
            MF1(1, 2, 1, 1)
            MF1(1, 2, 1, 2)
            put_partial<1>(vpart, acc[1]); SCHED_FENCE();
            MF1(1, 2, 1, 3)
            MF1(1, 3, 2, 0)
            put_partial_flag<1>(vflag, vready); SCHED_FENCE();
            MF1(1, 3, 2, 1)
            const u32 fls1 = (u32)__builtin_amdgcn_readfirstlane(fl); SCHED_FENCE();
            MF1(1, 3, 2, 2)
            MF1(1, 3, 2, 3)
            MF4(1, 4, 3)
            MF4(1, 5, 4)
            MF4(1, 6, 5)
            MF4(1, 7, 6)
            if (__builtin_expect(fls1 < ready, 0)) {
                lds_wait_ge(lds, SY_INTILE + 8, ready);
#pragma unroll
                for (int r = 0; r < 4; ++r) B[0][r] = *(const v4u*)(ub + 2560 + 64 * r);
            }
            asm volatile("" ::: "memory");
            SCHED_FENCE();
            MF1(0, 2, 0, 0)
            fl = lds_ld(lds, SY_INTILE + 12); SCHED_FENCE();
            MF1(0, 2, 0, 1)
            B[1][0] = *(const v4u*)(ub + 2048); SCHED_FENCE();
            MF1(0, 2, 0, 2)
            B[1][1] = *(const v4u*)(ub + 2112); SCHED_FENCE();
            MF1(0, 2, 0, 3)
            B[1][2] = *(const v4u*)(ub + 2176); SCHED_FENCE();
            MF1(0, 3, 1, 0)
            B[1][3] = *(const v4u*)(ub + 2240); SCHED_FENCE();
            MF1(0, 3, 1, 1)
            MF1(0, 3, 1, 2)
            put_partial<2>(vpart, acc[2]); SCHED_FENCE();
            MF1(0, 3, 1, 3)
            MF1(0, 4, 2, 0)
            put_partial_flag<2>(vflag, vready); SCHED_FENCE();
            MF1(0, 4, 2, 1)
            const u32 fls2 = (u32)__builtin_amdgcn_readfirstlane(fl); SCHED_FENCE();
            MF1(0, 4, 2, 2)
            MF1(0, 4, 2, 3)
            MF4(0, 5, 3)
            MF4(0, 6, 4)
            MF4(0, 7, 5)
            if (__builtin_expect(fls2 < ready, 0)) {
                lds_wait_ge(lds, SY_INTILE + 12, ready);
#pragma unroll
                for (int r = 0; r < 4; ++r) B[1][r] = *(const v4u*)(ub + 2048 + 64 * r);
            }
            asm volatile("" ::: "memory");
            SCHED_FENCE();
            MF1(1, 3, 0, 0)
            fl = lds_ld(lds, SY_INTILE + 16); SCHED_FENCE();
            MF1(1, 3, 0, 1)
            B[0][0] = *(const v4u*)(ub + 1536); SCHED_FENCE();
            MF1(1, 3, 0, 2)
            B[0][1] = *(const v4u*)(ub + 1600); SCHED_FENCE();
            MF1(1, 3, 0, 3)
            B[0][2] = *(const v4u*)(ub + 1664); SCHED_FENCE();
            MF1(1, 4, 1, 0)
            B[0][3] = *(const v4u*)(ub + 1728); SCHED_FENCE();
            MF1(1, 4, 1, 1)
            MF1(1, 4, 1, 2)
            put_partial<3>(vpart, acc[3]); SCHED_FENCE();
            MF1(1, 4, 1, 3)
            MF1(1, 5, 2, 0)
            put_partial_flag<3>(vflag, vready); SCHED_FENCE();
            MF1(1, 5, 2, 1)
            const u32 fls3 = (u32)__builtin_amdgcn_readfirstlane(fl); SCHED_FENCE();
            MF1(1, 5, 2, 2)
            MF1(1, 5, 2, 3)
            MF4(1, 6, 3)
            MF4(1, 7, 4)
            if (__builtin_expect(fls3 < ready, 0)) {
                lds_wait_ge(lds, SY_INTILE + 16, ready);
#pragma unroll
                for (int r = 0; r < 4; ++r) B[0][r] = *(const v4u*)(ub + 1536 + 64 * r);
            }
            asm volatile("" ::: "memory");
            SCHED_FENCE();
            MF1(0, 4, 0, 0)
            fl = lds_ld(lds, SY_INTILE + 20); SCHED_FENCE();
            MF1(0, 4, 0, 1)
            B[1][0] = *(const v4u*)(ub + 1024); SCHED_FENCE();
            MF1(0, 4, 0, 2)
            B[1][1] = *(const v4u*)(ub + 1088); SCHED_FENCE();
            MF1(0, 4, 0, 3)
            B[1][2] = *(const v4u*)(ub + 1152); SCHED_FENCE();
            MF1(0, 5, 1, 0)
            B[1][3] = *(const v4u*)(ub + 1216); SCHED_FENCE();
            MF1(0, 5, 1, 1)
            MF1(0, 5, 1, 2)
            put_partial<4>(vpart, acc[4]); SCHED_FENCE();
            MF1(0, 5, 1, 3)
            MF1(0, 6, 2, 0)
            put_partial_flag<4>(vflag, vready); SCHED_FENCE();
            MF1(0, 6, 2, 1)
            const u32 fls4 = (u32)__builtin_amdgcn_readfirstlane(fl); SCHED_FENCE();
            MF1(0, 6, 2, 2)
            MF1(0, 6, 2, 3)
            MF4(0, 7, 3)
            if (__builtin_expect(fls4 < ready, 0)) {
                lds_wait_ge(lds, SY_INTILE + 20, ready);
#pragma unroll
                for (int r = 0; r < 4; ++r) B[1][r] = *(const v4u*)(ub + 1024 + 64 * r);
            }
            asm volatile("" ::: "memory");
            SCHED_FENCE();
            MF1(1, 5, 0, 0)
            fl = lds_ld(lds, SY_INTILE + 24); SCHED_FENCE();
            MF1(1, 5, 0, 1)
            B[0][0] = *(const v4u*)(ub + 512); SCHED_FENCE();
            MF1(1, 5, 0, 2)
            B[0][1] = *(const v4u*)(ub + 576); SCHED_FENCE();
            MF1(1, 5, 0, 3)
            B[0][2] = *(const v4u*)(ub + 640); SCHED_FENCE();
            MF1(1, 6, 1, 0)
            B[0][3] = *(const v4u*)(ub + 704); SCHED_FENCE();
            MF1(1, 6, 1, 1)
            MF1(1, 6, 1, 2)
            put_partial<5>(vpart, acc[5]); SCHED_FENCE();
            MF1(1, 6, 1, 3)
            MF1(1, 7, 2, 0)
            put_partial_flag<5>(vflag, vready); SCHED_FENCE();
            MF1(1, 7, 2, 1)
            const u32 fls5 = (u32)__builtin_amdgcn_readfirstlane(fl); SCHED_FENCE();
            MF1(1, 7, 2, 2)
            MF1(1, 7, 2, 3)
            if (__builtin_expect(fls5 < ready, 0)) {
                lds_wait_ge(lds, SY_INTILE + 24, ready);
#pragma unroll
                for (int r = 0; r < 4; ++r) B[0][r] = *(const v4u*)(ub + 512 + 64 * r);
            }
            asm volatile("" ::: "memory");
            SCHED_FENCE();
            MF1(0, 6, 0, 0)
            fl = lds_ld(lds, SY_INTILE + 28); SCHED_FENCE();
            MF1(0, 6, 0, 1)
            B[1][0] = *(const v4u*)(ub + 0); SCHED_FENCE();
            MF1(0, 6, 0, 2)
            B[1][1] = *(const v4u*)(ub + 64); SCHED_FENCE();
            MF1(0, 6, 0, 3)
            B[1][2] = *(const v4u*)(ub + 128); SCHED_FENCE();
            MF1(0, 7, 1, 0)
            B[1][3] = *(const v4u*)(ub + 192); SCHED_FENCE();
            MF1(0, 7, 1, 1)
            MF1(0, 7, 1, 2)
            put_partial<6>(vpart, acc[6]); SCHED_FENCE();
            MF1(0, 7, 1, 3)
            put_partial_flag<6>(vflag, vready); SCHED_FENCE();
            const u32 fls6 = (u32)__builtin_amdgcn_readfirstlane(fl); SCHED_FENCE();
            if (__builtin_expect(fls6 < ready, 0)) {
                lds_wait_ge(lds, SY_INTILE + 28, ready);
#pragma unroll
                for (int r = 0; r < 4; ++r) B[1][r] = *(const v4u*)(ub + 0 + 64 * r);
            }
            asm volatile("" ::: "memory");
            STAMP(9 + 8 * k); if (k == 0) STAMPC(30);
            SCHED_FENCE();
            MF4(1, 7, 0)
            asm volatile("s_nop 7" ::: "memory");
            put_partial<7>(vpart, acc[7]); put_partial_flag<7>(vflag, vready);
            {
                const u32 pub = __builtin_amdgcn_readfirstlane(lds_ld(lds, SY_PUB)), allf = __builtin_amdgcn_readfirstlane(lds_ld(lds, SY_ALLF));
                const u32 pubt = __builtin_amdgcn_readfirstlane(lds_ld(lds, SY_PUBT));
                if (pub >= ready && allf && now_ticks() - pubt >= C_DELAY) { cg = __builtin_amdgcn_raw_buffer_load_b128(rs, cvoff, csoff, 16); cissued = true; }
                SCHED_FENCE();
            }
#undef MF1
#undef MF4
#undef MFA
            __builtin_amdgcn_s_setprio(0);
            if (k == 0 && mytile >= KLOAD_SPLIT) {
                lds_wait_ge_slow(lds, SY_KREADY, 2);
                load_afrags(A, lds, L_KB, ch, h, l);
            }
        } else if (w >= 6) {
            const int ch = l & 1, tl = 32 * (l >> 3) + 8 * ((l >> 1) & 3);
            const float dco = *(const float*)(lds + L_DCO + (k == 0 ? 0 : 8) + ch * 4);
            __builtin_amdgcn_s_setprio(3);
#pragma unroll 1
            for (int c = w & 1; c < 8; c += 2) {
                if (c >= 6) {
                    const u32 pub = __builtin_amdgcn_readfirstlane(lds_ld(lds, SY_PUB)), allf = __builtin_amdgcn_readfirstlane(lds_ld(lds, SY_ALLF));
                    const u32 pubt = __builtin_amdgcn_readfirstlane(lds_ld(lds, SY_PUBT));
                    if (pub >= (u32)(k + 1) && allf && now_ticks() - pubt >= C_DELAY) { cg = __builtin_amdgcn_raw_buffer_load_b128(rs, cvoff, csoff, 16); cissued = true; }
                }
                const int sb = (l >> 1) & 3, pa = 2 * (l >> 3) + (sb >> 1);
                const unsigned char* pp2 = lds + L_PART + c * PART_TILE + ch * PART_CH + 16 * (32 * (sb & 1) + ((pa + 8 * (sb & 1)) & 15));
                v4f a0, a1, b0, b1;
                v4u u;
                for (u32 it = 0;; ++it) {
                    asm volatile("" ::: "memory");
                    const v4u f = *(const v4u*)(lds + SY_PART + 16 * c);
                    asm volatile("" ::: "memory");
                    a0 = *(const v4f*)pp2; a1 = *(const v4f*)(pp2 + 256);
                    b0 = *(const v4f*)(pp2 + 1024); b1 = *(const v4f*)(pp2 + 1280);
                    u = *(const v4u*)(lds + L_UR + ch * UR_PITCH + 2 * (2040 - (256 * c + tl)));
                    const u32 fa = f.x < f.y ? f.x : f.y, fb = f.z < f.w ? f.z : f.w;
                    if ((u32)__builtin_amdgcn_readfirstlane(fa < fb ? fa : fb) >= (u32)(k + 1)) break;
                    if (spin_give_up(lds, it, LDS_SPIN_CAP)) break;
                }
                v4u o;
                o.x = no_poison(pack_bf(vfma(dco, bf_hi(u.w), vadd(a0.x, b0.x)), vfma(dco, bf_lo(u.w), vadd(a0.y, b0.y))));
                o.y = no_poison(pack_bf(vfma(dco, bf_hi(u.z), vadd(a0.z, b0.z)), vfma(dco, bf_lo(u.z), vadd(a0.w, b0.w))));
                o.z = no_poison(pack_bf(vfma(dco, bf_hi(u.y), vadd(a1.x, b1.x)), vfma(dco, bf_lo(u.y), vadd(a1.y, b1.y))));
                o.w = no_poison(pack_bf(vfma(dco, bf_hi(u.x), vadd(a1.z, b1.z)), vfma(dco, bf_lo(u.x), vadd(a1.w, b1.w))));
                store16(o, rs, (8 * c + (l >> 3)) * C_PITCH + me * 128 + (l & 7) * 16, WS_C + k * C_SIZE);
                if (c == mytile) { lds_flag_set(SY_PUBT, now_ticks()); lds_flag_set(SY_PUB, (u32)(k + 1)); }
                if (c < 2) STAMP(8 + 8 * k);
                if (c >= 6) STAMP(9 + 8 * k);
            }
            __builtin_amdgcn_s_setprio(2);
        }

        asm volatile("" : "+v"(i));
        STAMP(10 + 8 * k);
        float x[8], y[8], dm, rsd;
        u32 n0[8];
        float piv;
        v4u yap = {0, 0, 0, 0};
        if (!cissued) {
            if (k == 0) lds_wait_ge(lds, SY_ALLF, 1u);
            lds_wait_ge(lds, SY_PUB, (u32)(k + 1));
            wait_until(__builtin_amdgcn_readfirstlane(lds_ld(lds, SY_PUBT)), C_DELAY);
            cg = __builtin_amdgcn_raw_buffer_load_b128(rs, cvoff, csoff, 16);
        }
        STAMP(11 + 8 * k);
        SCHED_FENCE();
        if (k == 0) {
            store16(poison4, rs, (i >> 3) * C_PITCH + me * 128 + (i & 7) * 16, WS_C + C_SIZE);
            store16(poison4, rs, myr * U_PITCH + i * 16, WS_U + U_SIZE);
            lds_wait_ge(lds, SY_X0, 2);
#pragma unroll
            for (int t = 0; t < 8; ++t) {
                const u32 xn = *(const u32*)(lds + L_XN + t * 2048 + i * 4);
                x[t] = bf_lo(xn);
                n0[t] = (t & 1) ? (xn & 0xFFFF0000u) : (xn >> 16);
            }
            piv = *(const float*)(lds + L_PIV + (l & 7) * 4);
        } else {
#pragma unroll
            for (int t = 0; t < 8; ++t) n0[t] = (t & 1) ? 0x3F800000u : 0x00003F80u;
            if (k == 1) store16(poison4, rs, (i >> 3) * C_PITCH + me * 128 + (i & 7) * 16, WS_C + 2 * C_SIZE);
            const v4f xa = *(const v4f*)(lds + L_PARK + i * 16), xb = *(const v4f*)(lds + L_PARK + 8192 + i * 16);
            x[0] = xa.x; x[1] = xa.y; x[2] = xa.z; x[3] = xa.w; x[4] = xb.x; x[5] = xb.y; x[6] = xb.z; x[7] = xb.w;
            yap = *(const v4u*)(lds + L_PARK + 16384 + i * 16);
            piv = *(const float*)(lds + L_PARK + 24576 + i * 4);
        }
        v4u sel_lo, sel_hi;
        {
            const u32 one = 0x3F80u << (16 * (l & 1)), dsel = (l & 7) >> 1;
            const v4u pat = {dsel == 0 ? one : 0u, dsel == 1 ? one : 0u, dsel == 2 ? one : 0u, dsel == 3 ? one : 0u};
            const v4u zero = {0, 0, 0, 0};
            sel_lo = (l & 8) ? zero : pat;
            sel_hi = (l & 8) ? pat : zero;
        }
        u32 red_rd0 = L_RED + (l & 15) * 32, red_wr0 = red_rd0 + w * 4, red_rd1 = red_rd0 + 512, red_wr1 = red_wr0 + 512;
        float ya[8] = {bf_lo(yap.x), bf_hi(yap.x), bf_lo(yap.y), bf_hi(yap.y), bf_lo(yap.z), bf_hi(yap.z), bf_lo(yap.w), bf_hi(yap.w)};
        asm volatile("" : "+v"(sel_lo), "+v"(sel_hi), "+v"(red_rd0), "+v"(red_wr0), "+v"(red_rd1), "+v"(red_wr1), "+v"(piv));
        asm volatile("" : "+v"(ya[0]), "+v"(ya[1]), "+v"(ya[2]), "+v"(ya[3]), "+v"(ya[4]), "+v"(ya[5]), "+v"(ya[6]), "+v"(ya[7]));
        asm volatile("" : "+v"(x[0]), "+v"(x[1]), "+v"(x[2]), "+v"(x[3]), "+v"(x[4]), "+v"(x[5]), "+v"(x[6]), "+v"(x[7]));
        asm volatile("" : "+v"(n0[0]), "+v"(n0[1]), "+v"(n0[2]), "+v"(n0[3]), "+v"(n0[4]), "+v"(n0[5]), "+v"(n0[6]), "+v"(n0[7]));
        SCHED_FENCE();
        {
            bool ok = valid16(cg);
            for (u32 it = 0; !__all(ok); ++it) {
                if (spin_give_up(lds, it * 4 + 3, GLB_SPIN_CAP * 4)) break;
                asm volatile("" ::: "memory");
                if (!ok) { cg = __builtin_amdgcn_raw_buffer_load_b128(rs, cvoff, csoff, 16); ok = valid16(cg); }
            }
        }
        STAMP(12 + 8 * k);
        const u32 cgp[4] = {cg.x, cg.y, cg.z, cg.w};
        dot_rows(x, cgp, n0);
        if (k == 2) {
#pragma unroll
            for (int t = 0; t < 8; t += 2) {
                const u32 o = pack_bf(x[t], x[t + 1]);
                __builtin_amdgcn_raw_buffer_store_b16((u16)o, ro, i * 2 + t * (D_ * 2), myr * (8 * D_ * 2), OUT_AUX);
                __builtin_amdgcn_raw_buffer_store_b16((u16)(o >> 16), ro, i * 2 + (t + 1) * (D_ * 2), myr * (8 * D_ * 2), OUT_AUX);
            }
            break;
        }
        if (k == 0) {
            yap = cg;
        } else {
            sub_rows(y, x, piv);
            row_stats(y, sel_lo, sel_hi, lds, red_rd0, red_wr0, dm, rsd, [] {});
            norm_rows(y, rsd, dm * rsd);
            fma_rows(x, y, ya);
            piv += dm;
        }
        sub_rows(y, x, piv);
        WAIT_VM0();
        auto tail = [&](auto npre_c) {
            constexpr int NPRE = decltype(npre_c)::value;
            const u32 gsoff = WS_U + k * U_SIZE;
            const u32 gvoff = (32 * (w & 1) + (l >> 1)) * U_PITCH + (2 * me + (l & 1)) * 16;
            v4u d[4];
            row_stats(y, sel_lo, sel_hi, lds, red_rd1, red_wr1, dm, rsd, [&] {
                if (NPRE >= 1) d[0] = __builtin_amdgcn_raw_buffer_load_b128(rs, gvoff, gsoff, 16);
                SCHED_FENCE();
            });
            norm_rows(y, rsd, dm * rsd);
            v4u uo;
            uo.x = no_poison(pack_bf(y[7], y[6]));
            uo.y = no_poison(pack_bf(y[5], y[4]));
            uo.z = no_poison(pack_bf(y[3], y[2]));
            uo.w = no_poison(pack_bf(y[1], y[0]));
            SCHED_FENCE();
#pragma unroll
            for (int t = 1; t < NPRE; ++t) d[t] = __builtin_amdgcn_raw_buffer_load_b128(rs, gvoff + t * 64 * U_PITCH, gsoff, 16);
            SCHED_FENCE();
            store16(uo, rs, myr * U_PITCH + i * 16, WS_U + k * U_SIZE);
            const u32 ustore_t = NPRE >= 0 ? now_ticks() : 0u;
            piv += dm;
            *(v4f*)(lds + L_PARK + i * 16) = (v4f){x[0], x[1], x[2], x[3]};
            *(v4f*)(lds + L_PARK + 8192 + i * 16) = (v4f){x[4], x[5], x[6], x[7]};
            *(v4u*)(lds + L_PARK + 16384 + i * 16) = yap;
            *(float*)(lds + L_PARK + 24576 + i * 4) = piv;
            STAMP(13 + 8 * k);
            if (NPRE >= 0) {
                __builtin_amdgcn_s_setprio(3);
                unsigned char* dst = lds + L_UR + (l & 1) * UR_PITCH + 2 * (2040 - 8 * (32 * (w & 1) + (l >> 1)));
#pragma unroll
                for (int t = 0; t < 4; ++t) {
                    const int q = 2 * t + (w & 1);
                    if (t == NPRE) {
                        wait_until(ustore_t, U_DELAY);
#pragma unroll
                        for (int u = t; u < 4; ++u) {
                            d[u] = poison4;
                            if (2 * u + (w & 1) <= mytile + GATHER_AHEAD) d[u] = __builtin_amdgcn_raw_buffer_load_b128(rs, gvoff + u * 64 * U_PITCH, gsoff, 16);
                        }
                    }
                    if (__builtin_expect(!__all(valid16(d[t])), 0)) {
                        for (u32 it = 0; !__all(valid16(d[t])); ++it) {
                            if (spin_give_up(lds, it * 4 + 3, GLB_SPIN_CAP * 4)) break;
                            asm volatile("" ::: "memory");
                            if (!valid16(d[t])) d[t] = __builtin_amdgcn_raw_buffer_load_b128(rs, gvoff + t * 64 * U_PITCH, gsoff, 16);
                        }
                    }
                    *(v4u*)(dst - t * 1024) = d[t];
                    lds_flag_set(SY_INTILE + 4 * q, (u32)(k + 2));
                    if (t == 0) STAMP(16 + 8 * k);
                    if (t == 3) STAMP(17 + 8 * k);
                }
                WAIT_VM0();
                __builtin_amdgcn_s_setprio(2);
            }
        };
        if (w == 4 || w == 5) {
            if (mytile >= 6) tail(std::integral_constant<int, 3>());
            else if (mytile >= 4) tail(std::integral_constant<int, 2>());
            else if (mytile >= 2) tail(std::integral_constant<int, 1>());
            else tail(std::integral_constant<int, 0>());
        } else {
            tail(std::integral_constant<int, -1>());
        }
    }
    asm volatile(".Lh3s4_end:");
}

extern "C" void kernel_launch(void* const* d_in, const int* in_sizes, int n_in, void* d_out, int out_size, void* d_ws, size_t ws_size,
                              hipStream_t stream) {
    (void)in_sizes; (void)n_in; (void)out_size; (void)ws_size;
    h3s4_kernel<<<256, 512, 0, stream>>>(
        (const u16*)d_in[0], (const u16*)d_in[1], (const u16*)d_in[2], (const u16*)d_in[3], (const u16*)d_in[4], (const u16*)d_in[5],
        (const u16*)d_in[7], (const u16*)d_in[8], (const u16*)d_in[9], (const u16*)d_in[10], (const u16*)d_in[11], (const u16*)d_in[12],
        (const u16*)d_in[13], (const u16*)d_in[14], (u16*)d_out, (unsigned char*)d_ws);
}
```

```cpp
#include <hip/hip_runtime.h>
#include <stdint.h>
#include <type_traits>

typedef unsigned short u16;
typedef unsigned u32;
typedef u32 v4u __attribute__((ext_vector_type(4)));
typedef float v2f __attribute__((ext_vector_type(2)));
typedef float v4f __attribute__((ext_vector_type(4)));
typedef __bf16 v2bf __attribute__((ext_vector_type(2)));
typedef __bf16 v8bf __attribute__((ext_vector_type(8)));
typedef _Float16 v2h __attribute__((ext_vector_type(2)));
typedef _Float16 v8h __attribute__((ext_vector_type(8)));

constexpr int S_ = 2048, D_ = 512, NS_ = 128;
constexpr u32 POISON = 0xFFFFFFFFu;

constexpr u32 WS_EPOCH = 0;
constexpr u32 WS_FLAGS = 4096;
constexpr u32 KV_PITCH = 4096 + 128;
constexpr u32 WS_FLAGP = WS_FLAGS + 256 * 128;
constexpr u32 WS_KV = WS_FLAGP + 256 * 128 + 4096;
constexpr u32 C_PITCH = 32768 + 128;
constexpr u32 C_SIZE = 64 * C_PITCH;
constexpr u32 WS_C = WS_KV + 512 * KV_PITCH;
constexpr u32 U_PITCH = 8192 + 128;
constexpr u32 U_SIZE = 256 * U_PITCH;
constexpr u32 WS_U = WS_C + 3 * C_SIZE;
constexpr u32 WS_TOTAL = WS_U + 2 * U_SIZE;

constexpr int UR_PITCH = 4608;
constexpr int L_UR = 0;
constexpr int PART_CH = 2048 + 64;
constexpr int PART_TILE = 2 * PART_CH;
constexpr int L_PART = L_UR + 2 * UR_PITCH;
constexpr int KC_COPY = 4160;
constexpr int KC_CH = 2 * KC_COPY;
constexpr int L_KA = L_PART + 8 * PART_TILE;
constexpr int L_KB = L_KA + 2 * KC_CH;
constexpr int KT_CH = 648 * 4;
constexpr int L_KT = L_KB + 2 * KC_CH;
constexpr int TAB_ROW = 528;
constexpr int TAB_CH = 57 * TAB_ROW;
constexpr int L_TAB = L_KT + 2 * KT_CH;
constexpr int L_T = L_TAB + 2 * TAB_CH;
constexpr int L_XN = L_T;
constexpr int L_PARK = L_TAB;
constexpr int L_RED = L_T + 16384;
constexpr int L_SYNC = L_RED + 2 * 512;
constexpr int SY_PART = L_SYNC;
constexpr int SY_INTILE = L_SYNC + 128;
constexpr int SY_KV = L_SYNC + 160;
constexpr int SY_GO = L_SYNC + 164;
constexpr int SY_TAB = L_SYNC + 168;

constexpr int SY_KREADY = L_SYNC + 184;
constexpr int SY_ABORT = L_SYNC + 188;
constexpr int SY_TAG = L_SYNC + 192;
constexpr int SY_ALLF = L_SYNC + 196;
constexpr int SY_PUB = L_SYNC + 200;
constexpr int SY_X0 = L_SYNC + 204;
constexpr int SY_KA = L_SYNC + 176;
constexpr int SY_TAGSET = L_SYNC + 180;
constexpr int SY_POIS = L_SYNC + 164 + 44;
constexpr int SY_PUBT = L_SYNC + 212;
constexpr int L_DCO = L_SYNC + 224;
constexpr int L_PIV = L_DCO + 32;
constexpr int LDS_BYTES = L_PIV + 32;
static_assert(LDS_BYTES > 82 * 1024 && LDS_BYTES <= 160 * 1024, "one workgroup per CU");

#define GATHER_AHEAD 1
#define ALLF_TILE 1
#define KV_DELAY 0
#define KV_BACKOFF 0
#define YA_POISON_AT 1
#define ALLF_DELAY 30
#define C_DELAY 65
#define U_DELAY 55
#define PUB_EXTRA 0
#define GATH_EXTRA 0
#define POLL_BACKOFF 0
#define PUB_SLEEP 0
#define GATH_SLEEP 0
#define KLOAD_SPLIT 5
#define GATHER_LAG 2
#define GATHER_LOOK 0
#define OUT_AUX 0
#define CODE_PREFETCH 1
constexpr u32 LDS_SPIN_CAP = 1u << 19;
constexpr u32 GLB_SPIN_CAP = 1u << 16;

#define STAMP(n) do {} while (0)
#define STAMPC(n) do {} while (0)
#define SCHED_FENCE() __builtin_amdgcn_sched_barrier(0)
#define WAIT_VM0() do { asm volatile("" ::: "memory"); __builtin_amdgcn_s_waitcnt(0x0F70); asm volatile("" ::: "memory"); } while (0)

__device__ __forceinline__ float bf_lo(u32 x) { return __builtin_bit_cast(float, x << 16); }
__device__ __forceinline__ float bf_hi(u32 x) { return __builtin_bit_cast(float, x & 0xFFFF0000u); }
__device__ __forceinline__ float bf2f(u16 x) { return __builtin_bit_cast(float, ((u32)x) << 16); }
__device__ __forceinline__ u32 pack_bf(float lo, float hi) {
    v2f f = {lo, hi};
    return __builtin_bit_cast(u32, __builtin_convertvector(f, v2bf));
}
__device__ __forceinline__ u32 pack_h(v2f f) { return __builtin_bit_cast(u32, __builtin_convertvector(f, v2h)); }
__device__ __forceinline__ u32 no_poison(u32 x) { return x < 0xFFFFFFFEu ? x : 0xFFFFFFFEu; }
template <int CTRL> __device__ __forceinline__ float dppf(float x) {
    return __builtin_bit_cast(float, __builtin_amdgcn_mov_dpp(__builtin_bit_cast(int, x), CTRL, 0xf, 0xf, true));
}
template <int CTRL> __device__ __forceinline__ u32 dppu(u32 x) { return (u32)__builtin_amdgcn_mov_dpp((int)x, CTRL, 0xf, 0xf, true); }
__device__ __forceinline__ float f_of(u32 x) { return __builtin_bit_cast(float, x); }
__device__ __forceinline__ u32 u_of(float x) { return __builtin_bit_cast(u32, x); }

__device__ __forceinline__ u32 lds_ld(unsigned char* lds, int off) {
    return __hip_atomic_load((u32*)(lds + off), __ATOMIC_RELAXED, __HIP_MEMORY_SCOPE_WORKGROUP);
}
__device__ __forceinline__ void lds_st(unsigned char* lds, int off, u32 v) {
    __hip_atomic_store((u32*)(lds + off), v, __ATOMIC_RELAXED, __HIP_MEMORY_SCOPE_WORKGROUP);
}
__device__ __forceinline__ void lds_flag_set(int off, u32 v) {
    asm volatile("s_mov_b64 exec, 1\n\tds_write_b32 %0, %1\n\ts_mov_b64 exec, -1" ::"v"(off), "v"(v) : "memory");
}
template <int Q> __device__ __forceinline__ void put_partial(u32 vpart, const v4f& acc) {
    asm volatile("ds_write_b128 %0, %1 offset:%c2" ::"v"(vpart), "v"(acc), "i"(Q * PART_TILE) : "memory");
}
template <int Q> __device__ __forceinline__ void put_partial_flag(u32 vflag, u32 v) {
    asm volatile("ds_write_b32 %0, %1 offset:%c2" ::"v"(vflag), "v"(v), "i"(Q * 16) : "memory");
}
__device__ __forceinline__ void lds_flag_inc(int off) {
    asm volatile("s_mov_b64 exec, 1\n\tds_add_u32 %0, %1\n\ts_mov_b64 exec, -1" ::"v"(off), "v"(1u) : "memory");
}
__device__ __forceinline__ bool spin_give_up(unsigned char* lds, u32 it, u32 cap) {
    if ((it & 63u) != 63u) return false;
    if (it > cap) { lds_st(lds, SY_ABORT, 1u); return true; }
    return __builtin_amdgcn_readfirstlane(lds_ld(lds, SY_ABORT)) != 0;
}
__device__ __forceinline__ void lds_wait_ge(unsigned char* lds, int off, u32 target) {
    if (__builtin_expect((u32)__builtin_amdgcn_readfirstlane(lds_ld(lds, off)) < target, 0)) {
        for (u32 it = 0; (u32)__builtin_amdgcn_readfirstlane(lds_ld(lds, off)) < target; ++it)
            if (spin_give_up(lds, it, LDS_SPIN_CAP)) break;
    }
    asm volatile("" ::: "memory");
}
__device__ __forceinline__ void lds_wait_ge_slow(unsigned char* lds, int off, u32 target) {
    for (u32 it = 0; (u32)__builtin_amdgcn_readfirstlane(lds_ld(lds, off)) < target; ++it) {
        if (spin_give_up(lds, it, LDS_SPIN_CAP)) break;
        __builtin_amdgcn_s_sleep(1);
    }
    asm volatile("" ::: "memory");
}
__device__ __forceinline__ void lds_wait4_ge(unsigned char* lds, int off, u32 target) {
    for (u32 it = 0;; ++it) {
        asm volatile("" ::: "memory");
        const v4u f = *(const v4u*)(lds + off);
        const u32 a = f.x < f.y ? f.x : f.y, b = f.z < f.w ? f.z : f.w;
        if ((u32)__builtin_amdgcn_readfirstlane(a < b ? a : b) >= target) break;
        if (spin_give_up(lds, it, LDS_SPIN_CAP)) break;
    }
    asm volatile("" ::: "memory");
}
__device__ __forceinline__ void store16(v4u v, __amdgpu_buffer_rsrc_t rs, u32 voff, u32 soff) {
    __builtin_amdgcn_raw_buffer_store_b128(v, rs, voff + soff, 0, 16);
}
__device__ __forceinline__ u32 now_ticks() { return (u32)__builtin_amdgcn_s_memrealtime(); }
__device__ __forceinline__ void wait_until(u32 t0, u32 delay) {
    for (u32 it = 0; now_ticks() - t0 < delay && it < 4096; ++it) __builtin_amdgcn_s_sleep(1);
}
__device__ __forceinline__ bool valid16(v4u r) {
    u32 m = r.x > r.y ? r.x : r.y;
    m = m > r.z ? m : r.z;
    m = m > r.w ? m : r.w;
    return m != POISON;
}
__device__ __forceinline__ v4u poll16(__amdgpu_buffer_rsrc_t rs, u32 voff, u32 soff, unsigned char* lds) {
    v4u r = __builtin_amdgcn_raw_buffer_load_b128(rs, voff, soff, 16);
    bool ok = valid16(r);
    for (u32 it = 0; !__all(ok); ++it) {
        if (spin_give_up(lds, it * 4 + 3, GLB_SPIN_CAP * 4)) break;
        asm volatile("" ::: "memory");
        if (!ok) { r = __builtin_amdgcn_raw_buffer_load_b128(rs, voff, soff, 16); ok = valid16(r); }
    }
    return r;
}

__device__ __forceinline__ float vmul(float a, float b) {
    float r;
    asm("v_mul_f32 %0, %1, %2" : "=v"(r) : "v"(a), "v"(b));
    return r;
}
#define DPPB(t) " row_newbcast:" #t " row_mask:0xf bank_mask:0xf\n\t"
__device__ __forceinline__ void sub_rows(float (&y)[8], const float (&x)[8], float p) {
    asm("s_nop 1\n\t"
        "v_subrev_f32_dpp %0, %16, %8" DPPB(0) "v_subrev_f32_dpp %1, %16, %9" DPPB(1) "v_subrev_f32_dpp %2, %16, %10" DPPB(2)
        "v_subrev_f32_dpp %3, %16, %11" DPPB(3) "v_subrev_f32_dpp %4, %16, %12" DPPB(4) "v_subrev_f32_dpp %5, %16, %13" DPPB(5)
        "v_subrev_f32_dpp %6, %16, %14" DPPB(6) "v_subrev_f32_dpp %7, %16, %15" DPPB(7)
        : "=&v"(y[0]), "=&v"(y[1]), "=&v"(y[2]), "=&v"(y[3]), "=&v"(y[4]), "=&v"(y[5]), "=&v"(y[6]), "=&v"(y[7])
        : "v"(x[0]), "v"(x[1]), "v"(x[2]), "v"(x[3]), "v"(x[4]), "v"(x[5]), "v"(x[6]), "v"(x[7]), "v"(p));
}
__device__ __forceinline__ void norm_rows(float (&y)[8], float r, float m) {
    asm("s_nop 1\n\t"
        "v_mul_f32_dpp %0, %8, %0" DPPB(0) "v_mul_f32_dpp %1, %8, %1" DPPB(1) "v_mul_f32_dpp %2, %8, %2" DPPB(2) "v_mul_f32_dpp %3, %8, %3" DPPB(3)
        "v_mul_f32_dpp %4, %8, %4" DPPB(4) "v_mul_f32_dpp %5, %8, %5" DPPB(5) "v_mul_f32_dpp %6, %8, %6" DPPB(6) "v_mul_f32_dpp %7, %8, %7" DPPB(7)
        "v_subrev_f32_dpp %0, %9, %0" DPPB(0) "v_subrev_f32_dpp %1, %9, %1" DPPB(1) "v_subrev_f32_dpp %2, %9, %2" DPPB(2)
        "v_subrev_f32_dpp %3, %9, %3" DPPB(3) "v_subrev_f32_dpp %4, %9, %4" DPPB(4) "v_subrev_f32_dpp %5, %9, %5" DPPB(5)
        "v_subrev_f32_dpp %6, %9, %6" DPPB(6) "v_subrev_f32_dpp %7, %9, %7" DPPB(7)
        : "+v"(y[0]), "+v"(y[1]), "+v"(y[2]), "+v"(y[3]), "+v"(y[4]), "+v"(y[5]), "+v"(y[6]), "+v"(y[7])
        : "v"(r), "v"(m));
}
#undef DPPB
__device__ __forceinline__ void dot_rows(float (&x)[8], const u32 (&p)[4], const u32 (&m)[8]) {
    asm("v_dot2_f32_bf16 %0, %8, %12, %0\n\tv_dot2_f32_bf16 %1, %8, %13, %1\n\t"
        "v_dot2_f32_bf16 %2, %9, %14, %2\n\tv_dot2_f32_bf16 %3, %9, %15, %3\n\t"
        "v_dot2_f32_bf16 %4, %10, %16, %4\n\tv_dot2_f32_bf16 %5, %10, %17, %5\n\t"
        "v_dot2_f32_bf16 %6, %11, %18, %6\n\tv_dot2_f32_bf16 %7, %11, %19, %7\n\ts_nop 2"
        : "+v"(x[0]), "+v"(x[1]), "+v"(x[2]), "+v"(x[3]), "+v"(x[4]), "+v"(x[5]), "+v"(x[6]), "+v"(x[7])
        : "v"(p[0]), "v"(p[1]), "v"(p[2]), "v"(p[3]), "v"(m[0]), "v"(m[1]), "v"(m[2]), "v"(m[3]), "v"(m[4]), "v"(m[5]), "v"(m[6]), "v"(m[7]));
}
__device__ __forceinline__ void fma_rows(float (&x)[8], const float (&a)[8], const float (&b)[8]) {
    asm("v_fmac_f32 %0, %8, %16\n\tv_fmac_f32 %1, %9, %17\n\tv_fmac_f32 %2, %10, %18\n\tv_fmac_f32 %3, %11, %19\n\t"
        "v_fmac_f32 %4, %12, %20\n\tv_fmac_f32 %5, %13, %21\n\tv_fmac_f32 %6, %14, %22\n\tv_fmac_f32 %7, %15, %23"
        : "+v"(x[0]), "+v"(x[1]), "+v"(x[2]), "+v"(x[3]), "+v"(x[4]), "+v"(x[5]), "+v"(x[6]), "+v"(x[7])
        : "v"(a[0]), "v"(a[1]), "v"(a[2]), "v"(a[3]), "v"(a[4]), "v"(a[5]), "v"(a[6]), "v"(a[7]),
          "v"(b[0]), "v"(b[1]), "v"(b[2]), "v"(b[3]), "v"(b[4]), "v"(b[5]), "v"(b[6]), "v"(b[7]));
}
__device__ __forceinline__ float vfma(float a, float b, float c) {
    float r;
    asm("v_fma_f32 %0, %1, %2, %3" : "=v"(r) : "v"(a), "v"(b), "v"(c));
    return r;
}
__device__ __forceinline__ float vadd(float a, float b) {
    float r;
    asm("v_add_f32 %0, %1, %2" : "=v"(r) : "v"(a), "v"(b));
    return r;
}

template <class Hook>
__device__ __forceinline__ void row_stats(const float (&y)[8], const v4u& sel_lo, const v4u& sel_hi, unsigned char* lds, u32 red_rd, u32 red_wr,
                                          float& dmean, float& rstd, Hook after_barrier) {
    const v4u a = {pack_bf(y[0], y[1]), pack_bf(y[2], y[3]), pack_bf(y[4], y[5]), pack_bf(y[6], y[7])};
    float q[8];
    asm("v_mul_f32 %0, %8, %8\n\tv_mul_f32 %1, %9, %9\n\tv_mul_f32 %2, %10, %10\n\tv_mul_f32 %3, %11, %11\n\t"
        "v_mul_f32 %4, %12, %12\n\tv_mul_f32 %5, %13, %13\n\tv_mul_f32 %6, %14, %14\n\tv_mul_f32 %7, %15, %15"
        : "=&v"(q[0]), "=&v"(q[1]), "=&v"(q[2]), "=&v"(q[3]), "=&v"(q[4]), "=&v"(q[5]), "=&v"(q[6]), "=&v"(q[7])
        : "v"(y[0]), "v"(y[1]), "v"(y[2]), "v"(y[3]), "v"(y[4]), "v"(y[5]), "v"(y[6]), "v"(y[7]));
    const v4u s = {pack_bf(q[0], q[1]), pack_bf(q[2], q[3]), pack_bf(q[4], q[5]), pack_bf(q[6], q[7])};
    v4f d = {0, 0, 0, 0};
    d = __builtin_amdgcn_mfma_f32_16x16x32_bf16(__builtin_bit_cast(v8bf, a), __builtin_bit_cast(v8bf, sel_lo), d, 0, 0, 0);
    d = __builtin_amdgcn_mfma_f32_16x16x32_bf16(__builtin_bit_cast(v8bf, s), __builtin_bit_cast(v8bf, sel_hi), d, 0, 0, 0);
    u32 t0 = u_of((d.x + d.z) + (d.y + d.w)), t1 = t0;
    {
        auto r = __builtin_amdgcn_permlane16_swap(t0, t1, false, false);
        const u32 r0 = r[0], r1 = r[1];
        t0 = u_of(f_of(r0) + f_of(r1)); t1 = t0;
    }
    {
        auto r = __builtin_amdgcn_permlane32_swap(t0, t1, false, false);
        const u32 r0 = r[0], r1 = r[1];
        t0 = u_of(f_of(r0) + f_of(r1));
    }
    *(u32*)(lds + red_wr) = t0;
    __syncthreads();
    after_barrier();
    const v4f p0 = *(const v4f*)(lds + red_rd), p1 = *(const v4f*)(lds + red_rd + 16);
    const v4f pp = p0 + p1;
    const float tot = (pp.x + pp.z) + (pp.y + pp.w);
    const float s2 = dppf<0x128>(tot);
    dmean = tot * (1.0f / D_);
    const float var = fmaxf(s2 * (1.0f / D_) - dmean * dmean, 0.0f);
    rstd = __builtin_amdgcn_rsqf(var + 1e-5f);
}
__device__ __forceinline__ float wave_sum(float v) {
    asm("s_nop 1" : "+v"(v));
    v += dppf<0x128>(v);
    v += dppf<0x141>(v);
    v += dppf<0xB1>(v);
    v += dppf<0x4E>(v);
    u32 a = u_of(v), b = a;
    {
        auto r = __builtin_amdgcn_permlane16_swap(a, b, false, false);
        const u32 r0 = r[0], r1 = r[1];
        a = u_of(f_of(r0) + f_of(r1)); b = a;
    }
    auto r = __builtin_amdgcn_permlane32_swap(a, b, false, false);
    const u32 r0 = r[0], r1 = r[1];
    return f_of(r0) + f_of(r1);
}

__device__ __forceinline__ void load_afrags(v4u (&A)[8][4], unsigned char* lds, int region, int ch, int h, int l) {
    const int b = l & 15, g = l >> 4, par = (1 + b) & 1;
    const unsigned char* base = lds + region + ch * KC_CH + par * KC_COPY + 256 * h + 16 * g + 2 * (1 + b - par);
#pragma unroll
    for (int p = 0; p < 8; ++p)
#pragma unroll
        for (int r = 0; r < 4; ++r) {
            const u32* q = (const u32*)(base + 512 * p + 64 * r);
            A[p][r] = (v4u){q[0], q[1], q[2], q[3]};
        }
}

__device__ __forceinline__ v2f cexp_pow(float al, float be, float m) {
    const float e = __builtin_amdgcn_exp2f(al * m);
    const float ph = __builtin_amdgcn_fractf(be * m);
    return (v2f){e * __builtin_amdgcn_cosf(ph), e * __builtin_amdgcn_sinf(ph)};
}
__device__ __forceinline__ v2f cmul(v2f a, v2f b) {
    v2f t;
    asm("v_pk_mul_f32 %0, %1, %2 op_sel:[0,0] op_sel_hi:[1,0]\n\t"
        "v_pk_fma_f32 %0, %1, %2, %0 op_sel:[1,1,0] op_sel_hi:[0,1,1] neg_lo:[1,0,0]"
        : "=&v"(t) : "v"(a), "v"(b));
    return t;
}
__device__ __forceinline__ v4f mfma_bf(v4u a, v4u b, v4f c) {
    return __builtin_amdgcn_mfma_f32_16x16x32_bf16(__builtin_bit_cast(v8bf, a), __builtin_bit_cast(v8bf, b), c, 0, 0, 0);
}

extern "C" __global__ void __launch_bounds__(512, 2)
h3s4_kernel(const u16* __restrict__ q_, const u16* __restrict__ k_, const u16* __restrict__ v_, const u16* __restrict__ shk_,
            const u16* __restrict__ ssmk_, const u16* __restrict__ ssmD_, const u16* __restrict__ Are_, const u16* __restrict__ Aim_,
            const u16* __restrict__ Bre_, const u16* __restrict__ Bim_, const u16* __restrict__ Cre_, const u16* __restrict__ Cim_,
            const u16* __restrict__ logdt_, const u16* __restrict__ Dskip_, u16* __restrict__ out_, unsigned char* ws_) {
    __shared__ __attribute__((aligned(16))) unsigned char lds[LDS_BYTES];
    asm volatile("" ::"s"(q_), "s"(k_), "s"(v_), "s"(shk_), "s"(ssmk_), "s"(ssmD_), "s"(Are_), "s"(Aim_), "s"(Bre_), "s"(Bim_),
                 "s"(Cre_), "s"(Cim_), "s"(logdt_), "s"(Dskip_), "s"(out_), "s"(ws_));
    const int i = threadIdx.x, l = i & 63;
    const int w = __builtin_amdgcn_readfirstlane(i >> 6);
    u32 code_touch = 0;
    if (w == 0) {
        unsigned long long pc;
        u32 left;
        asm volatile("s_getpc_b64 %0\n.Lh3s4_pc:\n\ts_mov_b32 %1, .Lh3s4_end-.Lh3s4_pc+1536" : "=s"(pc), "=s"(left));
        const u32 nl = (left >> 12) + 1, off = ((blockIdx.x >> 3) * nl + l) * 128;
        if ((u32)l < nl && off + 4 <= left) code_touch = *(const __attribute__((address_space(1))) u32*)((pc & ~3ull) + off);
    }
    const int xcd = blockIdx.x & 7, idx = blockIdx.x >> 3;
    const int me = xcd * 32 + idx;
    const int mytile = idx >> 2;
    const int myr = 32 * mytile + 4 * xcd + (idx & 3);
    const __amdgpu_buffer_rsrc_t rs = __builtin_amdgcn_make_buffer_rsrc(ws_, 0, WS_TOTAL, 0x00020000);
    const v4u poison4 = {POISON, POISON, POISON, POISON};
    STAMP(0);

    const int cp = i & 31, rb = i >> 5;
    const int pcg = (xcd + idx) & 7;
    const int R0 = 64 * idx + 4 * rb, pch = 64 * pcg + 2 * cp;
    u32 kd[7], vd[4];
#pragma unroll
    for (int j = 0; j < 7; ++j) {
        const int Rj = R0 - 3 + j;
        kd[j] = *(const u32*)(k_ + (Rj < 0 ? 0 : Rj) * D_ + pch);
    }
#pragma unroll
    for (int j = 0; j < 4; ++j) vd[j] = *(const u32*)(v_ + (R0 + j) * D_ + pch);
    const v4u wd = *(const v4u*)(shk_ + pch * 4);
    u32 epoch = 0;
    if (w == 0) epoch = __builtin_amdgcn_raw_buffer_load_b32(rs, 0, WS_EPOCH, 16);
    v4u xq[4];

    if (i >= 256 && i < 256 + 64) *(v4u*)(lds + L_UR + ((i >> 5) & 1) * UR_PITCH + 4096 + (i & 31) * 16) = (v4u){0, 0, 0, 0};
    if (i >= 320 && i < 320 + 16) {
        const int e = i - 320;
        *(v4u*)(lds + L_KA + (e >> 1) * KC_COPY + (e & 1) * 16) = (v4u){0, 0, 0, 0};
    }
    if (i >= 384 && i < 384 + 56) *(u32*)(lds + L_SYNC + (i - 384) * 4) = 0;

    STAMP(1);
    {
#pragma unroll
        for (int j = 0; j < 3; ++j) if (R0 - 3 + j < 0) kd[j] = 0;
#pragma unroll
        for (int c = 0; c < 2; ++c) {
            const u32 sel = c ? 0x07060302u : 0x05040100u;
            const u32 w01 = wd[2 * c], w23 = wd[2 * c + 1];
            u32 P[7];
#pragma unroll
            for (int j = 1; j < 7; ++j) P[j] = __builtin_amdgcn_perm(kd[j - 1], kd[j], sel);
            float res[4];
#pragma unroll
            for (int t = 0; t < 4; ++t) {
                float a = __builtin_amdgcn_fdot2_f32_bf16(__builtin_bit_cast(v2bf, P[t + 3]), __builtin_bit_cast(v2bf, w01), 0.0f, false);
                a = __builtin_amdgcn_fdot2_f32_bf16(__builtin_bit_cast(v2bf, P[t + 1]), __builtin_bit_cast(v2bf, w23), a, false);
                res[t] = a * (c ? bf_hi(vd[t]) : bf_lo(vd[t]));
            }
            typedef u32 v2u __attribute__((ext_vector_type(2)));
            const int half = (1 - (rb & 1)) ^ ((cp >> 3) & 1);
            *(v2u*)(lds + L_T + (2 * cp + c) * 128 + (((rb >> 1) ^ (cp & 7)) * 16) + half * 8) = (v2u){pack_bf(res[3], res[2]), pack_bf(res[1], res[0])};
        }
    }
    SCHED_FENCE();
    if (w < 4) {
#pragma unroll
        for (int j = 0; j < 2; ++j) {
            const int e = i + 256 * j;
            store16(poison4, rs, (e >> 3) * C_PITCH + me * 128 + (e & 7) * 16, WS_C);
        }
    }
    u32 pp[11], dA, dB;
    if (w >= 4) {
        const int pc = 2 * me + ((w - 4) >> 1), pi = pc * NS_ + (i & 127), pj = pi ^ 64;
        pp[0] = Are_[pi]; pp[1] = Aim_[pi]; pp[2] = Bre_[pi]; pp[3] = Bim_[pi]; pp[4] = Cre_[pi]; pp[5] = Cim_[pi];
        pp[6] = Bre_[pj]; pp[7] = Bim_[pj]; pp[8] = Cre_[pj]; pp[9] = Cim_[pj];
        pp[10] = logdt_[pc];
        dA = ssmD_[2 * me + (l & 1)]; dB = Dskip_[2 * me + (l & 1)];
        if (w >= 6) {
#pragma unroll
            for (int j = 0; j < 4; ++j) xq[j] = *(const v4u*)(q_ + (8 * myr + 4 * (w & 1) + j) * D_ + l * 8);
        }
    }
    STAMP(2);
    __syncthreads();
    STAMP(3);

    v4u A[8][4];
    if (w < 4) {
#pragma unroll
        for (int j = 0; j < 2; ++j) {
            typedef u32 v2u __attribute__((ext_vector_type(2)));
            const int e = i + 256 * j, ch = e >> 3, chunk = e & 7, sw = (ch >> 4) & 1;
            const unsigned char* src = lds + L_T + ch * 128 + ((chunk ^ ((ch >> 1) & 7)) * 16);
            const v2u d0 = *(const v2u*)(src + 8 * sw), d1 = *(const v2u*)(src + 8 * (1 - sw));
            store16((v4u){d0.x, d0.y, d1.x, d1.y}, rs, (64 * pcg + ch) * KV_PITCH + (8 * idx + chunk) * 16, WS_KV);
        }
        SCHED_FENCE();
        asm volatile("" ::: "memory");
        v4u sk[3];
        u32 sknext[3];
#pragma unroll
        for (int j = 0; j < 3; ++j) {
            const int e = (i - 64) + 192 * j, ec = (w == 0 || e > 511) ? 0 : e;
            const u16* src = ssmk_ + (2 * me + (ec >> 8)) * S_ + (ec & 255) * 8;
            if (w != 0) {
                sk[j] = *(const v4u*)src;
                sknext[j] = *(const u32*)(src + ((ec & 255) < 255 ? 8 : 0));
            }
        }
        SCHED_FENCE();
        if (w == 0) asm volatile("s_waitcnt vmcnt(0)" ::: "memory");
        else asm volatile("s_waitcnt vmcnt(6)" ::: "memory");
        lds_flag_inc(SY_KV);
        STAMP(4);
        u32 tag = 0, tflag = 0;
        if (w == 0) {
            lds_wait_ge(lds, SY_KV, 4);
            tag = __builtin_amdgcn_readfirstlane(epoch) + 1u;
            if (l == 0) __builtin_amdgcn_raw_buffer_store_b32(tag, rs, me * 128, WS_FLAGS, 16);
            tflag = now_ticks();
            lds_flag_set(SY_TAG, tag);
            lds_flag_set(SY_TAGSET, 1u);
        }
        if (w != 0) {
#pragma unroll
            for (int j = 0; j < 3; ++j) {
                const int e = (i - 64) + 192 * j, sm = e & 255;
                if (e < 512) {
                    unsigned char* c0 = lds + L_KA + (e >> 8) * KC_CH;
                    const u32 nx = sm < 255 ? sknext[j] : 0u;
                    *(v4u*)(c0 + 32 + sm * 16) = sk[j];
                    *(v4u*)(c0 + KC_COPY + 32 + sm * 16) =
                        (v4u){__builtin_amdgcn_alignbit(sk[j].y, sk[j].x, 16), __builtin_amdgcn_alignbit(sk[j].z, sk[j].y, 16),
                              __builtin_amdgcn_alignbit(sk[j].w, sk[j].z, 16), __builtin_amdgcn_alignbit(nx, sk[j].w, 16)};
                    if (sm == 0) *(u32*)(c0 + KC_COPY + 28) = sk[j].x << 16;
                }
            }
            lds_flag_inc(SY_KA);
        }
        if (w == 0) {
            const u32 foff = (((xcd - (l & 31)) & 7) * 32 + (l & 31)) * 128;
            wait_until(tflag, KV_DELAY);
            for (u32 it = 0;; ++it) {
                asm volatile("" ::: "memory");
                if (__all(__builtin_amdgcn_raw_buffer_load_b32(rs, foff, WS_FLAGS, 16) == tag)) break;
                if (spin_give_up(lds, it * 4 + 3, GLB_SPIN_CAP * 4)) break;
            }
            lds_flag_set(SY_GO, 1u);
        } else {
            lds_wait_ge(lds, SY_KA, 3);
            load_afrags(A, lds, L_KA, w >> 1, w & 1, l);
            lds_wait_ge(lds, SY_GO, 1u);
        }
        STAMP(5);
        v4u g0, g1;
        {
            const u32 off = (2 * me + (l >> 5)) * KV_PITCH + (64 * w + (l & 31)) * 16;
            g0 = __builtin_amdgcn_raw_buffer_load_b128(rs, off, WS_KV, 16);
            g1 = __builtin_amdgcn_raw_buffer_load_b128(rs, off + 512, WS_KV, 16);
        }
        SCHED_FENCE();
        if (w == 0) {
            lds_wait_ge(lds, SY_KA, 3);
            load_afrags(A, lds, L_KA, 0, 0, l);
        }
        STAMP(6);
        unsigned char* dst = lds + L_UR + (l >> 5) * UR_PITCH + 2 * (2040 - 8 * (64 * w + (l & 31)));
        *(v4u*)dst = g0;
        *(v4u*)(dst - 512) = g1;
        lds_flag_set(SY_INTILE + 8 * w, 1u);
        lds_flag_set(SY_INTILE + 8 * w + 4, 1u);
        WAIT_VM0();

    } else {
        __builtin_amdgcn_s_setprio(2);
        const int wa = w - 4, ch = wa >> 1, n = (i - 256) & 127;
#pragma unroll
        for (int j = 0; j < 11; ++j) asm volatile("" : "+v"(pp[j]));
        asm volatile("" : "+v"(dA), "+v"(dB));
        const float ex = expf(f_of(pp[10] << 16));
        const float dt = bf_lo(pack_bf(ex, ex));
        const float al = dt * f_of(pp[0] << 16) * 1.44269504088896341f, be = dt * f_of(pp[1] << 16) * 0.15915494309189535f;
        v2f W;
        float inv;
        {
            const float br = dt * f_of(pp[2] << 16), bi = dt * f_of(pp[3] << 16), cr = f_of(pp[4] << 16), ci = f_of(pp[5] << 16);
            const float br2 = dt * f_of(pp[6] << 16), bi2 = dt * f_of(pp[7] << 16), cr2 = f_of(pp[8] << 16), ci2 = f_of(pp[9] << 16);
            W = (v2f){cr * br - ci * bi, cr * bi + ci * br};
            float mx = fmaxf(fmaxf(fabsf(W.x), fabsf(W.y)), fmaxf(fabsf(cr2 * br2 - ci2 * bi2), fabsf(cr2 * bi2 + ci2 * br2)));
            mx = fmaxf(mx, dppf<0x128>(mx));
            mx = fmaxf(mx, dppf<0x141>(mx));
            mx = fmaxf(mx, dppf<0xB1>(mx));
            mx = fmaxf(mx, dppf<0x4E>(mx));
            float m0 = f_of((u32)__builtin_amdgcn_readlane((int)u_of(mx), 0)), m1 = f_of((u32)__builtin_amdgcn_readlane((int)u_of(mx), 16));
            float m2 = f_of((u32)__builtin_amdgcn_readlane((int)u_of(mx), 32)), m3 = f_of((u32)__builtin_amdgcn_readlane((int)u_of(mx), 48));
            const int e = __builtin_amdgcn_frexp_expf(fmaxf(fmaxf(m0, m1), fmaxf(m2, m3)));
            W = (v2f){__builtin_amdgcn_ldexpf(W.x, 14 - e), __builtin_amdgcn_ldexpf(W.y, 14 - e)};
            inv = __builtin_amdgcn_ldexpf(1.0f, e - 14);
        }
        unsigned char* tab = lds + L_TAB + ch * TAB_CH + n * 4;
#define TAB_PUT(row, val) (*(u32*)(tab + (row) * TAB_ROW) = pack_h(val))
        const v2f E1 = cexp_pow(al, be, 1.0f), E8 = cexp_pow(al, be, 8.0f), E128 = cexp_pow(al, be, 128.0f);
        const v2f E256 = cmul(E128, E128), E384 = cmul(E256, E128), E512 = cmul(E256, E256), E1024 = cmul(E512, E512);
        {
            const v2f E8c = {E8.x, -E8.y};
            v2f B = {W.x, -W.y}, P = {1.0f, 0.0f}, Q = E512, Rr = E1024, P4 = cmul(cmul(E1, E1), cmul(E1, E1));
#pragma unroll
            for (int r = 0; r < 8; ++r) {
                TAB_PUT(41 + 2 * r, B);
                B = cmul(B, E8c);
                TAB_PUT(42 + 2 * r, B);
                if (r < 7) B = cmul(B, E8c);
                TAB_PUT(r, P);
                TAB_PUT(8 + r, r ? cmul(P, E128) : E128);
                if ((r & 1) == 0) {
                    TAB_PUT(16 + (r >> 1), r ? cmul(P, E256) : E256);
                    TAB_PUT(20 + (r >> 1), r ? cmul(P, E384) : E384);
                }
                if (r < 7) P = r ? cmul(P, E1) : E1;
                TAB_PUT(32 + r, Rr);
                if (r < 7) Rr = cmul(Rr, E128);
                if (r < 4) {
                    TAB_PUT(24 + 2 * r, Q);
                    TAB_PUT(25 + 2 * r, cmul(Q, P4));
                    if (r < 3) Q = cmul(Q, E128);
                }
            }
            TAB_PUT(40, cmul(E1024, E1024));
        }
#undef TAB_PUT
        if (l == 0) *(float*)(lds + L_DCO + 16 + ch * 4) = inv;
        lds_flag_inc(SY_TAB + 4 * ch);
        if (l < 2) { *(u32*)(lds + L_DCO + l * 4) = dA << 16; *(u32*)(lds + L_DCO + 8 + l * 4) = dB << 16; }
        STAMP(4);
        if (w < 6) {
            const int kc = w & 1;
            lds_wait_ge(lds, SY_TAB + 4 * kc, 2);
            {
                const int row = l & 15, g = l >> 4;
                const unsigned char* tb = lds + L_TAB + kc * TAB_CH + row * TAB_ROW + g * 16;
                float* kt = (float*)(lds + L_KT + kc * KT_CH);
                const float scale = *(const float*)(lds + L_DCO + 16 + kc * 4);
                v4u fb[8], fa[8];
                v4f acc[3];
#pragma unroll
                for (int s2 = 0; s2 < 8; ++s2) fb[s2] = *(const v4u*)(tb + 41 * TAB_ROW + s2 * 64);
#pragma unroll
                for (int tile = 0; tile < 3; ++tile) {
#pragma unroll
                    for (int s2 = 0; s2 < 8; ++s2) fa[s2] = *(const v4u*)(tb + tile * 16 * TAB_ROW + s2 * 64);
                    acc[tile] = (v4f){0, 0, 0, 0};
#pragma unroll
                    for (int s2 = 0; s2 < 8; ++s2)
                        acc[tile] = __builtin_amdgcn_mfma_f32_16x16x32_f16(__builtin_bit_cast(v8h, fa[s2]), __builtin_bit_cast(v8h, fb[s2]), acc[tile], 0, 0, 0);
                }
                asm volatile("s_nop 7\n\ts_nop 1" : "+v"(acc[0]), "+v"(acc[1]), "+v"(acc[2]));
#pragma unroll
                for (int r = 0; r < 4; ++r) {
                    const int rw = 4 * g + r;
                    kt[128 * (rw >> 3) + (rw & 7) + 8 * row] = vmul(acc[0][r], scale);
                    kt[rw < 8 ? 256 + 64 * (rw >> 2) + (rw & 3) + 4 * row : 384 + 32 * ((rw - 8) >> 1) + (rw & 1) + 2 * row] = vmul(acc[1][r], scale);
                    kt[rw < 8 ? 512 + 16 * rw + row : ((rw == 8 && row == 0) ? 640 : 644 + (l & 3))] = vmul(acc[2][r], scale);
                }
            }
            asm volatile("s_waitcnt lgkmcnt(0)" ::: "memory");
            {
                const float* t = (const float*)(lds + L_KT + kc * KT_CH);
                unsigned char* c0 = lds + L_KB + kc * KC_CH;
                const float a2 = t[512 + l], b2 = t[513 + l], a3 = t[576 + l], b3 = t[577 + l];
                const float v0 = t[384 + 2 * l], v1 = t[385 + 2 * l], v2 = t[386 + 2 * l];
                float kvv[9];
#pragma unroll
                for (int j = 0; j < 9; ++j) {
                    const int lag = 8 * l + j;
                    const u32 bb = pack_bf((float)lag, 0.0f) & 0xFFFFu;
                    kvv[j] = t[l < 32 ? lag : (int)(bb - 0x4280u)];
                }
                {
                    const u32 e0 = pack_bf(kvv[0], kvv[1]);
                    *(v4u*)(c0 + 32 + l * 16) = (v4u){e0, pack_bf(kvv[2], kvv[3]), pack_bf(kvv[4], kvv[5]), pack_bf(kvv[6], kvv[7])};
                    *(v4u*)(c0 + KC_COPY + 32 + l * 16) = (v4u){pack_bf(kvv[1], kvv[2]), pack_bf(kvv[3], kvv[4]), pack_bf(kvv[5], kvv[6]), pack_bf(kvv[7], kvv[8])};
                    if (l == 0) *(u32*)(c0 + KC_COPY + 28) = e0 << 16;
                }
                {
                    const u32 p00 = pack_bf(v0, v0), p01 = pack_bf(v0, v1), p11 = pack_bf(v1, v1), p12 = pack_bf(v1, v2), p22 = pack_bf(v2, v2);
                    *(v4u*)(c0 + 32 + (64 + l) * 16) = (v4u){p00, p01, p11, p22};
                    *(v4u*)(c0 + KC_COPY + 32 + (64 + l) * 16) = (v4u){p00, p11, p12, p22};
                }
#pragma unroll
                for (int it = 0; it < 2; ++it) {
                    const float a = it ? a3 : a2, b = it ? b3 : b2, c4 = (l & 1) ? b : a;
                    const u32 paa = pack_bf(a, a), pbb = pack_bf(b, b);
                    *(v4u*)(c0 + 32 + (128 + 64 * it + l) * 16) = (v4u){paa, paa, pack_bf(c4, b), pbb};
                    *(v4u*)(c0 + KC_COPY + 32 + (128 + 64 * it + l) * 16) = (v4u){paa, pack_bf(a, c4), pbb, pbb};
                }
            }
            lds_flag_inc(SY_KREADY);
            STAMP(5);
            if (w == 4) {
                lds_wait_ge(lds, SY_TAGSET, 1u);
                const u32 tag = __builtin_amdgcn_readfirstlane(lds_ld(lds, SY_TAG));
                lds_wait_ge_slow(lds, SY_GO, 1u);
                wait_until(now_ticks(), ALLF_DELAY);
                for (u32 it = 0;; ++it) {
                    asm volatile("" ::: "memory");
                    if (it) __builtin_amdgcn_s_sleep(8);
                    const u32 f0 = __builtin_amdgcn_raw_buffer_load_b32(rs, l * 128, WS_FLAGS, 16);
                    const u32 f1 = __builtin_amdgcn_raw_buffer_load_b32(rs, (l + 64) * 128, WS_FLAGS, 16);
                    const u32 f2 = __builtin_amdgcn_raw_buffer_load_b32(rs, (l + 128) * 128, WS_FLAGS, 16);
                    const u32 f3 = __builtin_amdgcn_raw_buffer_load_b32(rs, (l + 192) * 128, WS_FLAGS, 16);
                    if (__all(f0 == tag && f1 == tag && f2 == tag && f3 == tag)) break;
                    if (spin_give_up(lds, it * 4 + 3, GLB_SPIN_CAP * 4)) break;
                }
                if (me == 0 && l == 0) __builtin_amdgcn_raw_buffer_store_b32(tag, rs, 0, WS_EPOCH, 16);
                lds_flag_set(SY_ALLF, 1u);
            }
            WAIT_VM0();
        } else {
            const int kr = w & 1;
            lds_wait_ge(lds, SY_KV, 4);
#pragma unroll
            for (int j = 0; j < 4; ++j) store16(poison4, rs, myr * U_PITCH + ((i & 127) + 128 * j) * 16, WS_U);
            STAMP(5);
#pragma unroll
            for (int j = 0; j < 4; ++j) asm volatile("" : "+v"(xq[j]));
            {
                float f[4][8], mean[4], rstd[4];
#pragma unroll
                for (int rr = 0; rr < 4; ++rr) {
                    const v4u xr = xq[rr];
                    f[rr][0] = bf_lo(xr.x); f[rr][1] = bf_hi(xr.x); f[rr][2] = bf_lo(xr.y); f[rr][3] = bf_hi(xr.y);
                    f[rr][4] = bf_lo(xr.z); f[rr][5] = bf_hi(xr.z); f[rr][6] = bf_lo(xr.w); f[rr][7] = bf_hi(xr.w);
                    mean[rr] = vadd(vadd(vadd(f[rr][0], f[rr][1]), vadd(f[rr][2], f[rr][3])), vadd(vadd(f[rr][4], f[rr][5]), vadd(f[rr][6], f[rr][7])));
                }
#pragma unroll
                for (int rr = 0; rr < 4; ++rr) mean[rr] = vmul(wave_sum(mean[rr]), -1.0f / D_);
#pragma unroll
                for (int rr = 0; rr < 4; ++rr) {
#pragma unroll
                    for (int j = 0; j < 8; ++j) f[rr][j] = vadd(f[rr][j], mean[rr]);
                    float q2 = vmul(f[rr][0], f[rr][0]);
#pragma unroll
                    for (int j = 1; j < 8; ++j) q2 = vfma(f[rr][j], f[rr][j], q2);
                    rstd[rr] = q2;
                }
#pragma unroll
                for (int rr = 0; rr < 4; ++rr) rstd[rr] = __builtin_amdgcn_rsqf(vfma(wave_sum(rstd[rr]), 1.0f / D_, 1e-5f));
                asm volatile("s_nop 1" : "+v"(rstd[0]), "+v"(rstd[1]), "+v"(rstd[2]), "+v"(rstd[3]));
#pragma unroll
                for (int rr = 0; rr < 4; ++rr) {
                    const v4u xr = xq[rr];
                    u32 o[8];
#pragma unroll
                    for (int j = 0; j < 4; ++j) {
                        const u32 np = pack_bf(vmul(f[rr][2 * j], rstd[rr]), vmul(f[rr][2 * j + 1], rstd[rr]));
                        o[2 * j] = __builtin_amdgcn_perm(np, xr[j], 0x05040100u);
                        o[2 * j + 1] = __builtin_amdgcn_perm(np, xr[j], 0x07060302u);
                    }
                    unsigned char* dstx = lds + L_XN + (4 * kr + rr) * 2048 + l * 32;
                    *(v4u*)dstx = (v4u){o[0], o[1], o[2], o[3]};
                    *(v4u*)(dstx + 16) = (v4u){o[4], o[5], o[6], o[7]};
                    if (l == 0) *(float*)(lds + L_PIV + (4 * kr + rr) * 4) = -mean[rr];
                }
            }
            lds_flag_inc(SY_X0);
            WAIT_VM0();
            lds_flag_inc(SY_POIS);
            lds_wait_ge(lds, SY_POIS, 2);
            STAMP(6);
        }
    }

    asm volatile("" ::"v"(code_touch));
    WAIT_VM0();
    const int i_ = i;
    const __amdgpu_buffer_rsrc_t ro = __builtin_amdgcn_make_buffer_rsrc(out_, 0, S_ * D_ * 2, 0x00020000);

#pragma unroll 1
    for (int k = 0; k < 3; ++k) {
        int i = i_;
        asm volatile("" : "+v"(i));
        const int l = i & 63;
        STAMP(7 + 8 * k);
        const u32 cvoff = (myr >> 2) * C_PITCH + (i >> 1) * 128 + (myr & 3) * 32 + (i & 1) * 16, csoff = WS_C + k * C_SIZE;
        v4u cg = poison4;
        bool cissued = false;
        if (w < 4) {
            const int ch = w >> 1, h = w & 1, a = l & 15, g = l >> 4;
            const unsigned char* ub = lds + L_UR + ch * UR_PITCH + 2 * (2032 + 128 * h - 16 * a + 8 * g) - 3584;
            const u32 ready = (u32)(k + 1);
            if (k == 1 && mytile < KLOAD_SPLIT) load_afrags(A, lds, L_KB, ch, h, l);
            v4f acc[8];
#pragma unroll
            for (int c = 0; c < 8; ++c) acc[c] = (v4f){0, 0, 0, 0};
#pragma unroll
            for (int c = 0; c < 8; ++c) asm volatile("" : "+v"(acc[c]));
            v4u B[2][4];
            __builtin_amdgcn_s_setprio(1);
            for (u32 it = 0;; ++it) {
                asm volatile("" ::: "memory");
                const u32 f0 = lds_ld(lds, SY_INTILE);
                asm volatile("" ::: "memory");
#pragma unroll
                for (int r = 0; r < 4; ++r) B[0][r] = *(const v4u*)(ub + 512 * 7 + 64 * r);
                if (__builtin_expect((u32)__builtin_amdgcn_readfirstlane(f0) >= ready, 1)) break;
                if (spin_give_up(lds, it, LDS_SPIN_CAP)) break;
            }
            STAMP(8 + 8 * k);
#define MFA "v_mfma_f32_16x16x32_bf16 "
#define MF1(cur, c, p, r)                                                                                     \
    asm volatile(MFA "%0, %1, %2, %0" : "+v"(acc[c]) : "v"(A[p][r]), "v"(B[cur][r]));                          \
    SCHED_FENCE();
#define MF4(cur, c, p)                                                                                        \
    asm volatile(MFA "%0, %1, %5, %0\n\t" MFA "%0, %2, %6, %0\n\t" MFA "%0, %3, %7, %0\n\t" MFA "%0, %4, %8, %0" \
                 : "+v"(acc[c]) : "v"(A[p][0]), "v"(A[p][1]), "v"(A[p][2]), "v"(A[p][3]),                      \
                   "v"(B[cur][0]), "v"(B[cur][1]), "v"(B[cur][2]), "v"(B[cur][3]));                             \
    SCHED_FENCE();
            u32 vflag = SY_PART + 4 * w, vready = ready, vpart = L_PART + ch * PART_CH + h * 1024 + 16 * (16 * g + ((a + 8 * (g >> 1)) & 15));
            asm volatile("" : "+v"(vflag), "+v"(vready), "+v"(vpart));
            u32 fl = 0;
            MF1(0, 0, 0, 0)
            fl = lds_ld(lds, SY_INTILE + 4); SCHED_FENCE();
            MF1(0, 0, 0, 1)
            B[1][0] = *(const v4u*)(ub + 3072); SCHED_FENCE();
            MF1(0, 0, 0, 2)
            B[1][1] = *(const v4u*)(ub + 3136); SCHED_FENCE();
            MF1(0, 0, 0, 3)
            B[1][2] = *(const v4u*)(ub + 3200); SCHED_FENCE();
            MF1(0, 1, 1, 0)
            B[1][3] = *(const v4u*)(ub + 3264); SCHED_FENCE();
            MF1(0, 1, 1, 1)
            MF1(0, 1, 1, 2)
            put_partial<0>(vpart, acc[0]); SCHED_FENCE();
            MF1(0, 1, 1, 3)
            MF1(0, 2, 2, 0)
            put_partial_flag<0>(vflag, vready); SCHED_FENCE();
            MF1(0, 2, 2, 1)
            const u32 fls0 = (u32)__builtin_amdgcn_readfirstlane(fl); SCHED_FENCE();
            MF1(0, 2, 2, 2)
            MF1(0, 2, 2, 3)
            MF4(0, 3, 3)
            MF4(0, 4, 4)
            MF4(0, 5, 5)
            MF4(0, 6, 6)
            MF4(0, 7, 7)
            if (__builtin_expect(fls0 < ready, 0)) {
                lds_wait_ge(lds, SY_INTILE + 4, ready);
#pragma unroll
                for (int r = 0; r < 4; ++r) B[1][r] = *(const v4u*)(ub + 3072 + 64 * r);
            }
            asm volatile("" ::: "memory");
            SCHED_FENCE();
            MF1(1, 1, 0, 0)
            fl = lds_ld(lds, SY_INTILE + 8); SCHED_FENCE();
            MF1(1, 1, 0, 1)
            B[0][0] = *(const v4u*)(ub + 2560); SCHED_FENCE();
            MF1(1, 1, 0, 2)
            B[0][1] = *(const v4u*)(ub + 2624); SCHED_FENCE();
            MF1(1, 1, 0, 3)
            B[0][2] = *(const v4u*)(ub + 2688); SCHED_FENCE();
            MF1(1, 2, 1, 0)
            B[0][3] = *(const v4u*)(ub + 2752); SCHED_FENCE();
            MF1(1, 2, 1, 1)
            MF1(1, 2, 1, 2)
            put_partial<1>(vpart, acc[1]); SCHED_FENCE();
            MF1(1, 2, 1, 3)
            MF1(1, 3, 2, 0)
            put_partial_flag<1>(vflag, vready); SCHED_FENCE();
            MF1(1, 3, 2, 1)
            const u32 fls1 = (u32)__builtin_amdgcn_readfirstlane(fl); SCHED_FENCE();
            MF1(1, 3, 2, 2)
            MF1(1, 3, 2, 3)
            MF4(1, 4, 3)
            MF4(1, 5, 4)
            MF4(1, 6, 5)
            MF4(1, 7, 6)
            if (__builtin_expect(fls1 < ready, 0)) {
                lds_wait_ge(lds, SY_INTILE + 8, ready);
#pragma unroll
                for (int r = 0; r < 4; ++r) B[0][r] = *(const v4u*)(ub + 2560 + 64 * r);
            }
            asm volatile("" ::: "memory");
            SCHED_FENCE();
            MF1(0, 2, 0, 0)
            fl = lds_ld(lds, SY_INTILE + 12); SCHED_FENCE();
            MF1(0, 2, 0, 1)
            B[1][0] = *(const v4u*)(ub + 2048); SCHED_FENCE();
            MF1(0, 2, 0, 2)
            B[1][1] = *(const v4u*)(ub + 2112); SCHED_FENCE();
            MF1(0, 2, 0, 3)
            B[1][2] = *(const v4u*)(ub + 2176); SCHED_FENCE();
            MF1(0, 3, 1, 0)
            B[1][3] = *(const v4u*)(ub + 2240); SCHED_FENCE();
            MF1(0, 3, 1, 1)
            MF1(0, 3, 1, 2)
            put_partial<2>(vpart, acc[2]); SCHED_FENCE();
            MF1(0, 3, 1, 3)
            MF1(0, 4, 2, 0)
            put_partial_flag<2>(vflag, vready); SCHED_FENCE();
            MF1(0, 4, 2, 1)
            const u32 fls2 = (u32)__builtin_amdgcn_readfirstlane(fl); SCHED_FENCE();
            MF1(0, 4, 2, 2)
            MF1(0, 4, 2, 3)
            MF4(0, 5, 3)
            MF4(0, 6, 4)
            MF4(0, 7, 5)
            if (__builtin_expect(fls2 < ready, 0)) {
                lds_wait_ge(lds, SY_INTILE + 12, ready);
#pragma unroll
                for (int r = 0; r < 4; ++r) B[1][r] = *(const v4u*)(ub + 2048 + 64 * r);
            }
            asm volatile("" ::: "memory");
            SCHED_FENCE();
            MF1(1, 3, 0, 0)
            fl = lds_ld(lds, SY_INTILE + 16); SCHED_FENCE();
            MF1(1, 3, 0, 1)
            B[0][0] = *(const v4u*)(ub + 1536); SCHED_FENCE();
            MF1(1, 3, 0, 2)
            B[0][1] = *(const v4u*)(ub + 1600); SCHED_FENCE();
            MF1(1, 3, 0, 3)
            B[0][2] = *(const v4u*)(ub + 1664); SCHED_FENCE();
            MF1(1, 4, 1, 0)
            B[0][3] = *(const v4u*)(ub + 1728); SCHED_FENCE();
            MF1(1, 4, 1, 1)
            MF1(1, 4, 1, 2)
            put_partial<3>(vpart, acc[3]); SCHED_FENCE();
            MF1(1, 4, 1, 3)
            MF1(1, 5, 2, 0)
            put_partial_flag<3>(vflag, vready); SCHED_FENCE();
            MF1(1, 5, 2, 1)
            const u32 fls3 = (u32)__builtin_amdgcn_readfirstlane(fl); SCHED_FENCE();
            MF1(1, 5, 2, 2)
            MF1(1, 5, 2, 3)
            MF4(1, 6, 3)
            MF4(1, 7, 4)
            if (__builtin_expect(fls3 < ready, 0)) {
                lds_wait_ge(lds, SY_INTILE + 16, ready);
#pragma unroll
                for (int r = 0; r < 4; ++r) B[0][r] = *(const v4u*)(ub + 1536 + 64 * r);
            }
            asm volatile("" ::: "memory");
            SCHED_FENCE();
            MF1(0, 4, 0, 0)
            fl = lds_ld(lds, SY_INTILE + 20); SCHED_FENCE();
            MF1(0, 4, 0, 1)
            B[1][0] = *(const v4u*)(ub + 1024); SCHED_FENCE();
            MF1(0, 4, 0, 2)
            B[1][1] = *(const v4u*)(ub + 1088); SCHED_FENCE();
            MF1(0, 4, 0, 3)
            B[1][2] = *(const v4u*)(ub + 1152); SCHED_FENCE();
            MF1(0, 5, 1, 0)
            B[1][3] = *(const v4u*)(ub + 1216); SCHED_FENCE();
            MF1(0, 5, 1, 1)
            MF1(0, 5, 1, 2)
            put_partial<4>(vpart, acc[4]); SCHED_FENCE();
            MF1(0, 5, 1, 3)
            MF1(0, 6, 2, 0)
            put_partial_flag<4>(vflag, vready); SCHED_FENCE();
            MF1(0, 6, 2, 1)
            const u32 fls4 = (u32)__builtin_amdgcn_readfirstlane(fl); SCHED_FENCE();
            MF1(0, 6, 2, 2)
            MF1(0, 6, 2, 3)
            MF4(0, 7, 3)
            if (__builtin_expect(fls4 < ready, 0)) {
                lds_wait_ge(lds, SY_INTILE + 20, ready);
#pragma unroll
                for (int r = 0; r < 4; ++r) B[1][r] = *(const v4u*)(ub + 1024 + 64 * r);
            }
            asm volatile("" ::: "memory");
            SCHED_FENCE();
            MF1(1, 5, 0, 0)
            fl = lds_ld(lds, SY_INTILE + 24); SCHED_FENCE();
            MF1(1, 5, 0, 1)
            B[0][0] = *(const v4u*)(ub + 512); SCHED_FENCE();
            MF1(1, 5, 0, 2)
            B[0][1] = *(const v4u*)(ub + 576); SCHED_FENCE();
            MF1(1, 5, 0, 3)
            B[0][2] = *(const v4u*)(ub + 640); SCHED_FENCE();
            MF1(1, 6, 1, 0)
            B[0][3] = *(const v4u*)(ub + 704); SCHED_FENCE();
            MF1(1, 6, 1, 1)
            MF1(1, 6, 1, 2)
            put_partial<5>(vpart, acc[5]); SCHED_FENCE();
            MF1(1, 6, 1, 3)
            MF1(1, 7, 2, 0)
            put_partial_flag<5>(vflag, vready); SCHED_FENCE();
            MF1(1, 7, 2, 1)
            const u32 fls5 = (u32)__builtin_amdgcn_readfirstlane(fl); SCHED_FENCE();
            MF1(1, 7, 2, 2)
            MF1(1, 7, 2, 3)
            if (__builtin_expect(fls5 < ready, 0)) {
                lds_wait_ge(lds, SY_INTILE + 24, ready);
#pragma unroll
                for (int r = 0; r < 4; ++r) B[0][r] = *(const v4u*)(ub + 512 + 64 * r);
            }
            asm volatile("" ::: "memory");
            SCHED_FENCE();
            MF1(0, 6, 0, 0)
            fl = lds_ld(lds, SY_INTILE + 28); SCHED_FENCE();
            MF1(0, 6, 0, 1)
            B[1][0] = *(const v4u*)(ub + 0); SCHED_FENCE();
            MF1(0, 6, 0, 2)
            B[1][1] = *(const v4u*)(ub + 64); SCHED_FENCE();
            MF1(0, 6, 0, 3)
            B[1][2] = *(const v4u*)(ub + 128); SCHED_FENCE();
            MF1(0, 7, 1, 0)
            B[1][3] = *(const v4u*)(ub + 192); SCHED_FENCE();
            MF1(0, 7, 1, 1)
            MF1(0, 7, 1, 2)
            put_partial<6>(vpart, acc[6]); SCHED_FENCE();
            MF1(0, 7, 1, 3)
            put_partial_flag<6>(vflag, vready); SCHED_FENCE();
            const u32 fls6 = (u32)__builtin_amdgcn_readfirstlane(fl); SCHED_FENCE();
            if (__builtin_expect(fls6 < ready, 0)) {
                lds_wait_ge(lds, SY_INTILE + 28, ready);
#pragma unroll
                for (int r = 0; r < 4; ++r) B[1][r] = *(const v4u*)(ub + 0 + 64 * r);
            }
            asm volatile("" ::: "memory");
            STAMP(9 + 8 * k); if (k == 0) STAMPC(30);
            SCHED_FENCE();
            MF4(1, 7, 0)
            asm volatile("s_nop 7" ::: "memory");
            put_partial<7>(vpart, acc[7]); put_partial_flag<7>(vflag, vready);
            {
                const u32 pub = __builtin_amdgcn_readfirstlane(lds_ld(lds, SY_PUB)), allf = __builtin_amdgcn_readfirstlane(lds_ld(lds, SY_ALLF));
                const u32 pubt = __builtin_amdgcn_readfirstlane(lds_ld(lds, SY_PUBT));
                if (pub >= ready && allf && now_ticks() - pubt >= C_DELAY) { cg = __builtin_amdgcn_raw_buffer_load_b128(rs, cvoff, csoff, 16); cissued = true; }
                SCHED_FENCE();
            }
#undef MF1
#undef MF4
#undef MFA
            __builtin_amdgcn_s_setprio(0);
            if (k == 0 && mytile >= KLOAD_SPLIT) {
                lds_wait_ge_slow(lds, SY_KREADY, 2);
                load_afrags(A, lds, L_KB, ch, h, l);
            }
        } else if (w >= 6) {
            const int ch = l & 1, tl = 32 * (l >> 3) + 8 * ((l >> 1) & 3);
            const float dco = *(const float*)(lds + L_DCO + (k == 0 ? 0 : 8) + ch * 4);
            __builtin_amdgcn_s_setprio(3);
#pragma unroll 1
            for (int c = w & 1; c < 8; c += 2) {
                if (c >= 6) {
                    const u32 pub = __builtin_amdgcn_readfirstlane(lds_ld(lds, SY_PUB)), allf = __builtin_amdgcn_readfirstlane(lds_ld(lds, SY_ALLF));
                    const u32 pubt = __builtin_amdgcn_readfirstlane(lds_ld(lds, SY_PUBT));
                    const u32 in6 = __builtin_amdgcn_readfirstlane(lds_ld(lds, SY_INTILE + 24)), in7 = __builtin_amdgcn_readfirstlane(lds_ld(lds, SY_INTILE + 28));
                    if (pub >= (u32)(k + 1) && allf && in6 >= (u32)(k + 1) && in7 >= (u32)(k + 1) && now_ticks() - pubt >= C_DELAY) { cg = __builtin_amdgcn_raw_buffer_load_b128(rs, cvoff, csoff, 16); cissued = true; }
                }
                const int sb = (l >> 1) & 3, pa = 2 * (l >> 3) + (sb >> 1);
                const unsigned char* pp2 = lds + L_PART + c * PART_TILE + ch * PART_CH + 16 * (32 * (sb & 1) + ((pa + 8 * (sb & 1)) & 15));
                v4f a0, a1, b0, b1;
                v4u u;
                for (u32 it = 0;; ++it) {
                    asm volatile("" ::: "memory");
                    const v4u f = *(const v4u*)(lds + SY_PART + 16 * c);
                    asm volatile("" ::: "memory");
                    a0 = *(const v4f*)pp2; a1 = *(const v4f*)(pp2 + 256);
                    b0 = *(const v4f*)(pp2 + 1024); b1 = *(const v4f*)(pp2 + 1280);
                    u = *(const v4u*)(lds + L_UR + ch * UR_PITCH + 2 * (2040 - (256 * c + tl)));
                    const u32 fa = f.x < f.y ? f.x : f.y, fb = f.z < f.w ? f.z : f.w;
                    if ((u32)__builtin_amdgcn_readfirstlane(fa < fb ? fa : fb) >= (u32)(k + 1)) break;
                    if (spin_give_up(lds, it, LDS_SPIN_CAP)) break;
                }
                v4u o;
                o.x = no_poison(pack_bf(vfma(dco, bf_hi(u.w), vadd(a0.x, b0.x)), vfma(dco, bf_lo(u.w), vadd(a0.y, b0.y))));
                o.y = no_poison(pack_bf(vfma(dco, bf_hi(u.z), vadd(a0.z, b0.z)), vfma(dco, bf_lo(u.z), vadd(a0.w, b0.w))));
                o.z = no_poison(pack_bf(vfma(dco, bf_hi(u.y), vadd(a1.x, b1.x)), vfma(dco, bf_lo(u.y), vadd(a1.y, b1.y))));
                o.w = no_poison(pack_bf(vfma(dco, bf_hi(u.x), vadd(a1.z, b1.z)), vfma(dco, bf_lo(u.x), vadd(a1.w, b1.w))));
                store16(o, rs, (8 * c + (l >> 3)) * C_PITCH + me * 128 + (l & 7) * 16, WS_C + k * C_SIZE);
                if (c == mytile) { lds_flag_set(SY_PUBT, now_ticks()); lds_flag_set(SY_PUB, (u32)(k + 1)); }
                if (c < 2) STAMP(8 + 8 * k);
                if (c >= 6) STAMP(9 + 8 * k);
            }
            __builtin_amdgcn_s_setprio(2);
        }

        asm volatile("" : "+v"(i));
        STAMP(10 + 8 * k);
        float x[8], y[8], dm, rsd;
        u32 n0[8];
        float piv;
        v4u yap = {0, 0, 0, 0};
        if (!cissued) {
            if (k == 0) lds_wait_ge(lds, SY_ALLF, 1u);
            lds_wait_ge(lds, SY_INTILE + 24, (u32)(k + 1));
            lds_wait_ge(lds, SY_INTILE + 28, (u32)(k + 1));
            lds_wait_ge(lds, SY_PUB, (u32)(k + 1));
            wait_until(__builtin_amdgcn_readfirstlane(lds_ld(lds, SY_PUBT)), C_DELAY);
            cg = __builtin_amdgcn_raw_buffer_load_b128(rs, cvoff, csoff, 16);
        }
        STAMP(11 + 8 * k);
        SCHED_FENCE();
        if (k == 0) {
            store16(poison4, rs, (i >> 3) * C_PITCH + me * 128 + (i & 7) * 16, WS_C + C_SIZE);
            store16(poison4, rs, myr * U_PITCH + i * 16, WS_U + U_SIZE);
            lds_wait_ge(lds, SY_X0, 2);
#pragma unroll
            for (int t = 0; t < 8; ++t) {
                const u32 xn = *(const u32*)(lds + L_XN + t * 2048 + i * 4);
                x[t] = bf_lo(xn);
                n0[t] = (t & 1) ? (xn & 0xFFFF0000u) : (xn >> 16);
            }
            piv = *(const float*)(lds + L_PIV + (l & 7) * 4);
        } else {
#pragma unroll
            for (int t = 0; t < 8; ++t) n0[t] = (t & 1) ? 0x3F800000u : 0x00003F80u;
            if (k == 1) store16(poison4, rs, (i >> 3) * C_PITCH + me * 128 + (i & 7) * 16, WS_C + 2 * C_SIZE);
            const v4f xa = *(const v4f*)(lds + L_PARK + i * 16), xb = *(const v4f*)(lds + L_PARK + 8192 + i * 16);
            x[0] = xa.x; x[1] = xa.y; x[2] = xa.z; x[3] = xa.w; x[4] = xb.x; x[5] = xb.y; x[6] = xb.z; x[7] = xb.w;
            yap = *(const v4u*)(lds + L_PARK + 16384 + i * 16);
            piv = *(const float*)(lds + L_PARK + 24576 + i * 4);
        }
        v4u sel_lo, sel_hi;
        {
            const u32 one = 0x3F80u << (16 * (l & 1)), dsel = (l & 7) >> 1;
            const v4u pat = {dsel == 0 ? one : 0u, dsel == 1 ? one : 0u, dsel == 2 ? one : 0u, dsel == 3 ? one : 0u};
            const v4u zero = {0, 0, 0, 0};
            sel_lo = (l & 8) ? zero : pat;
            sel_hi = (l & 8) ? pat : zero;
        }
        u32 red_rd0 = L_RED + (l & 15) * 32, red_wr0 = red_rd0 + w * 4, red_rd1 = red_rd0 + 512, red_wr1 = red_wr0 + 512;
        float ya[8] = {bf_lo(yap.x), bf_hi(yap.x), bf_lo(yap.y), bf_hi(yap.y), bf_lo(yap.z), bf_hi(yap.z), bf_lo(yap.w), bf_hi(yap.w)};
        asm volatile("" : "+v"(sel_lo), "+v"(sel_hi), "+v"(red_rd0), "+v"(red_wr0), "+v"(red_rd1), "+v"(red_wr1), "+v"(piv));
        asm volatile("" : "+v"(ya[0]), "+v"(ya[1]), "+v"(ya[2]), "+v"(ya[3]), "+v"(ya[4]), "+v"(ya[5]), "+v"(ya[6]), "+v"(ya[7]));
        asm volatile("" : "+v"(x[0]), "+v"(x[1]), "+v"(x[2]), "+v"(x[3]), "+v"(x[4]), "+v"(x[5]), "+v"(x[6]), "+v"(x[7]));
        asm volatile("" : "+v"(n0[0]), "+v"(n0[1]), "+v"(n0[2]), "+v"(n0[3]), "+v"(n0[4]), "+v"(n0[5]), "+v"(n0[6]), "+v"(n0[7]));
        SCHED_FENCE();
        {
            bool ok = valid16(cg);
            for (u32 it = 0; !__all(ok); ++it) {
                if (spin_give_up(lds, it * 4 + 3, GLB_SPIN_CAP * 4)) break;
                asm volatile("" ::: "memory");
                if (!ok) { cg = __builtin_amdgcn_raw_buffer_load_b128(rs, cvoff, csoff, 16); ok = valid16(cg); }
            }
        }
        STAMP(12 + 8 * k);
        const u32 cgp[4] = {cg.x, cg.y, cg.z, cg.w};
        dot_rows(x, cgp, n0);
        if (k == 2) {
#pragma unroll
            for (int t = 0; t < 8; t += 2) {
                const u32 o = pack_bf(x[t], x[t + 1]);
                __builtin_amdgcn_raw_buffer_store_b16((u16)o, ro, i * 2 + t * (D_ * 2), myr * (8 * D_ * 2), OUT_AUX);
                __builtin_amdgcn_raw_buffer_store_b16((u16)(o >> 16), ro, i * 2 + (t + 1) * (D_ * 2), myr * (8 * D_ * 2), OUT_AUX);
            }
            break;
        }
        if (k == 0) {
            yap = cg;
        } else {
            sub_rows(y, x, piv);
            row_stats(y, sel_lo, sel_hi, lds, red_rd0, red_wr0, dm, rsd, [] {});
            norm_rows(y, rsd, dm * rsd);
            fma_rows(x, y, ya);
            piv += dm;
        }
        sub_rows(y, x, piv);
        WAIT_VM0();
        auto tail = [&](auto npre_c) {
            constexpr int NPRE = decltype(npre_c)::value;
            const u32 gsoff = WS_U + k * U_SIZE;
            const u32 gvoff = (32 * (w & 1) + (l >> 1)) * U_PITCH + (2 * me + (l & 1)) * 16;
            v4u d[4];
            row_stats(y, sel_lo, sel_hi, lds, red_rd1, red_wr1, dm, rsd, [&] {
                if (NPRE >= 1) d[0] = __builtin_amdgcn_raw_buffer_load_b128(rs, gvoff, gsoff, 16);
                SCHED_FENCE();
            });
            norm_rows(y, rsd, dm * rsd);
            v4u uo;
            uo.x = no_poison(pack_bf(y[7], y[6]));
            uo.y = no_poison(pack_bf(y[5], y[4]));
            uo.z = no_poison(pack_bf(y[3], y[2]));
            uo.w = no_poison(pack_bf(y[1], y[0]));
            SCHED_FENCE();
#pragma unroll
            for (int t = 1; t < NPRE; ++t) d[t] = __builtin_amdgcn_raw_buffer_load_b128(rs, gvoff + t * 64 * U_PITCH, gsoff, 16);
            SCHED_FENCE();
            store16(uo, rs, myr * U_PITCH + i * 16, WS_U + k * U_SIZE);
            const u32 ustore_t = NPRE >= 0 ? now_ticks() : 0u;
            piv += dm;
            *(v4f*)(lds + L_PARK + i * 16) = (v4f){x[0], x[1], x[2], x[3]};
            *(v4f*)(lds + L_PARK + 8192 + i * 16) = (v4f){x[4], x[5], x[6], x[7]};
            *(v4u*)(lds + L_PARK + 16384 + i * 16) = yap;
            *(float*)(lds + L_PARK + 24576 + i * 4) = piv;
            STAMP(13 + 8 * k);
            if (NPRE >= 0) {
                __builtin_amdgcn_s_setprio(3);
                unsigned char* dst = lds + L_UR + (l & 1) * UR_PITCH + 2 * (2040 - 8 * (32 * (w & 1) + (l >> 1)));
#pragma unroll
                for (int t = 0; t < 4; ++t) {
                    const int q = 2 * t + (w & 1);
                    if (t == NPRE) {
                        wait_until(ustore_t, U_DELAY);
#pragma unroll
                        for (int u = t; u < 4; ++u) {
                            d[u] = poison4;
                            if (2 * u + (w & 1) <= mytile + GATHER_AHEAD) d[u] = __builtin_amdgcn_raw_buffer_load_b128(rs, gvoff + u * 64 * U_PITCH, gsoff, 16);
                        }
                    }
                    if (__builtin_expect(!__all(valid16(d[t])), 0)) {
                        for (u32 it = 0; !__all(valid16(d[t])); ++it) {
                            if (spin_give_up(lds, it * 4 + 3, GLB_SPIN_CAP * 4)) break;
                            asm volatile("" ::: "memory");
                            if (!valid16(d[t])) d[t] = __builtin_amdgcn_raw_buffer_load_b128(rs, gvoff + t * 64 * U_PITCH, gsoff, 16);
                        }
                    }
                    *(v4u*)(dst - t * 1024) = d[t];
                    lds_flag_set(SY_INTILE + 4 * q, (u32)(k + 2));
                    if (t == 0) STAMP(16 + 8 * k);
                    if (t == 3) STAMP(17 + 8 * k);
                }
                WAIT_VM0();
                __builtin_amdgcn_s_setprio(2);
            }
        };
        if (w == 4 || w == 5) {
            if (mytile >= 6) tail(std::integral_constant<int, 3>());
            else if (mytile >= 4) tail(std::integral_constant<int, 2>());
            else if (mytile >= 2) tail(std::integral_constant<int, 1>());
            else tail(std::integral_constant<int, 0>());
        } else {
            tail(std::integral_constant<int, -1>());
        }
    }
    asm volatile(".Lh3s4_end:");
}

extern "C" void kernel_launch(void* const* d_in, const int* in_sizes, int n_in, void* d_out, int out_size, void* d_ws, size_t ws_size,
                              hipStream_t stream) {
    (void)in_sizes; (void)n_in; (void)out_size; (void)ws_size;
    h3s4_kernel<<<256, 512, 0, stream>>>(
        (const u16*)d_in[0], (const u16*)d_in[1], (const u16*)d_in[2], (const u16*)d_in[3], (const u16*)d_in[4], (const u16*)d_in[5],
        (const u16*)d_in[7], (const u16*)d_in[8], (const u16*)d_in[9], (const u16*)d_in[10], (const u16*)d_in[11], (const u16*)d_in[12],
        (const u16*)d_in[13], (const u16*)d_in[14], (u16*)d_out, (unsigned char*)d_ws);
}
```

```cpp
#include <hip/hip_runtime.h>
#include <stdint.h>
#include <type_traits>

typedef unsigned short u16;
typedef unsigned u32;
typedef u32 v4u __attribute__((ext_vector_type(4)));
typedef float v2f __attribute__((ext_vector_type(2)));
typedef float v4f __attribute__((ext_vector_type(4)));
typedef __bf16 v2bf __attribute__((ext_vector_type(2)));
typedef __bf16 v8bf __attribute__((ext_vector_type(8)));
typedef _Float16 v2h __attribute__((ext_vector_type(2)));
typedef _Float16 v8h __attribute__((ext_vector_type(8)));

constexpr int S_ = 2048, D_ = 512, NS_ = 128;
constexpr u32 POISON = 0xFFFFFFFFu;

constexpr u32 WS_EPOCH = 0;
constexpr u32 WS_FLAGS = 4096;
constexpr u32 KV_PITCH = 4096 + 128;
constexpr u32 WS_FLAGP = WS_FLAGS + 256 * 128;
constexpr u32 WS_KV = WS_FLAGP + 256 * 128 + 4096;
constexpr u32 C_PITCH = 32768 + 128;
constexpr u32 C_SIZE = 64 * C_PITCH;
constexpr u32 WS_C = WS_KV + 512 * KV_PITCH;
constexpr u32 U_PITCH = 8192 + 128;
constexpr u32 U_SIZE = 256 * U_PITCH;
constexpr u32 WS_U = WS_C + 3 * C_SIZE;
constexpr u32 WS_TOTAL = WS_U + 2 * U_SIZE;

constexpr int UR_PITCH = 4608;
constexpr int L_UR = 0;
constexpr int PART_CH = 2048 + 64;
constexpr int PART_TILE = 2 * PART_CH;
constexpr int L_PART = L_UR + 2 * UR_PITCH;
constexpr int KC_COPY = 4160;
constexpr int KC_CH = 2 * KC_COPY;
constexpr int L_KA = L_PART + 8 * PART_TILE;
constexpr int L_KB = L_KA + 2 * KC_CH;
constexpr int KT_CH = 648 * 4;
constexpr int L_KT = L_KB + 2 * KC_CH;
constexpr int TAB_ROW = 528;
constexpr int TAB_CH = 57 * TAB_ROW;
constexpr int L_TAB = L_KT + 2 * KT_CH;
constexpr int L_T = L_TAB + 2 * TAB_CH;
constexpr int L_XN = L_T;
constexpr int L_PARK = L_TAB;
constexpr int L_RED = L_T + 16384;
constexpr int L_SYNC = L_RED + 2 * 512;
constexpr int SY_PART = L_SYNC;
constexpr int SY_INTILE = L_SYNC + 128;
constexpr int SY_KV = L_SYNC + 160;
constexpr int SY_GO = L_SYNC + 164;
constexpr int SY_TAB = L_SYNC + 168;

constexpr int SY_KREADY = L_SYNC + 184;
constexpr int SY_ABORT = L_SYNC + 188;
constexpr int SY_TAG = L_SYNC + 192;
constexpr int SY_ALLF = L_SYNC + 196;
constexpr int SY_PUB = L_SYNC + 200;
constexpr int SY_X0 = L_SYNC + 204;
constexpr int SY_KA = L_SYNC + 176;
constexpr int SY_TAGSET = L_SYNC + 180;
constexpr int SY_POIS = L_SYNC + 164 + 44;
constexpr int SY_PUBT = L_SYNC + 212;
constexpr int L_DCO = L_SYNC + 224;
constexpr int L_PIV = L_DCO + 32;
constexpr int LDS_BYTES = L_PIV + 32;
static_assert(LDS_BYTES > 82 * 1024 && LDS_BYTES <= 160 * 1024, "one workgroup per CU");

#define GATHER_AHEAD 1
#define ALLF_TILE 1
#define KV_DELAY 0
#define KV_BACKOFF 0
#define YA_POISON_AT 1
#define ALLF_DELAY 30
#define C_DELAY 65
#define U_DELAY 55
#define PUB_EXTRA 0
#define GATH_EXTRA 0
#define POLL_BACKOFF 0
#define PUB_SLEEP 0
#define GATH_SLEEP 0
#define KLOAD_SPLIT 5
#define GATHER_LAG 2
#define GATHER_LOOK 0
#define OUT_AUX 16
#define CODE_PREFETCH 1
constexpr u32 LDS_SPIN_CAP = 1u << 19;
constexpr u32 GLB_SPIN_CAP = 1u << 16;

#define STAMP(n) do {} while (0)
#define STAMPC(n) do {} while (0)
#define SCHED_FENCE() __builtin_amdgcn_sched_barrier(0)
#define WAIT_VM0() do { asm volatile("" ::: "memory"); __builtin_amdgcn_s_waitcnt(0x0F70); asm volatile("" ::: "memory"); } while (0)

__device__ __forceinline__ float bf_lo(u32 x) { return __builtin_bit_cast(float, x << 16); }
__device__ __forceinline__ float bf_hi(u32 x) { return __builtin_bit_cast(float, x & 0xFFFF0000u); }
__device__ __forceinline__ float bf2f(u16 x) { return __builtin_bit_cast(float, ((u32)x) << 16); }
__device__ __forceinline__ u32 pack_bf(float lo, float hi) {
    v2f f = {lo, hi};
    return __builtin_bit_cast(u32, __builtin_convertvector(f, v2bf));
}
__device__ __forceinline__ u32 pack_h(v2f f) { return __builtin_bit_cast(u32, __builtin_convertvector(f, v2h)); }
__device__ __forceinline__ u32 no_poison(u32 x) { return x < 0xFFFFFFFEu ? x : 0xFFFFFFFEu; }
template <int CTRL> __device__ __forceinline__ float dppf(float x) {
    return __builtin_bit_cast(float, __builtin_amdgcn_mov_dpp(__builtin_bit_cast(int, x), CTRL, 0xf, 0xf, true));
}
template <int CTRL> __device__ __forceinline__ u32 dppu(u32 x) { return (u32)__builtin_amdgcn_mov_dpp((int)x, CTRL, 0xf, 0xf, true); }
__device__ __forceinline__ float f_of(u32 x) { return __builtin_bit_cast(float, x); }
__device__ __forceinline__ u32 u_of(float x) { return __builtin_bit_cast(u32, x); }

__device__ __forceinline__ u32 lds_ld(unsigned char* lds, int off) {
    return __hip_atomic_load((u32*)(lds + off), __ATOMIC_RELAXED, __HIP_MEMORY_SCOPE_WORKGROUP);
}
__device__ __forceinline__ void lds_st(unsigned char* lds, int off, u32 v) {
    __hip_atomic_store((u32*)(lds + off), v, __ATOMIC_RELAXED, __HIP_MEMORY_SCOPE_WORKGROUP);
}
__device__ __forceinline__ void lds_flag_set(int off, u32 v) {
    asm volatile("s_mov_b64 exec, 1\n\tds_write_b32 %0, %1\n\ts_mov_b64 exec, -1" ::"v"(off), "v"(v) : "memory");
}
template <int Q> __device__ __forceinline__ void put_partial(u32 vpart, const v4f& acc) {
    asm volatile("ds_write_b128 %0, %1 offset:%c2" ::"v"(vpart), "v"(acc), "i"(Q * PART_TILE) : "memory");
}
template <int Q> __device__ __forceinline__ void put_partial_flag(u32 vflag, u32 v) {
    asm volatile("ds_write_b32 %0, %1 offset:%c2" ::"v"(vflag), "v"(v), "i"(Q * 16) : "memory");
}
__device__ __forceinline__ void lds_flag_inc(int off) {
    asm volatile("s_mov_b64 exec, 1\n\tds_add_u32 %0, %1\n\ts_mov_b64 exec, -1" ::"v"(off), "v"(1u) : "memory");
}
__device__ __forceinline__ bool spin_give_up(unsigned char* lds, u32 it, u32 cap) {
    if ((it & 63u) != 63u) return false;
    if (it > cap) { lds_st(lds, SY_ABORT, 1u); return true; }
    return __builtin_amdgcn_readfirstlane(lds_ld(lds, SY_ABORT)) != 0;
}
__device__ __forceinline__ void lds_wait_ge(unsigned char* lds, int off, u32 target) {
    if (__builtin_expect((u32)__builtin_amdgcn_readfirstlane(lds_ld(lds, off)) < target, 0)) {
        for (u32 it = 0; (u32)__builtin_amdgcn_readfirstlane(lds_ld(lds, off)) < target; ++it)
            if (spin_give_up(lds, it, LDS_SPIN_CAP)) break;
    }
    asm volatile("" ::: "memory");
}
__device__ __forceinline__ void lds_wait_ge_slow(unsigned char* lds, int off, u32 target) {
    for (u32 it = 0; (u32)__builtin_amdgcn_readfirstlane(lds_ld(lds, off)) < target; ++it) {
        if (spin_give_up(lds, it, LDS_SPIN_CAP)) break;
        __builtin_amdgcn_s_sleep(1);
    }
    asm volatile("" ::: "memory");
}
__device__ __forceinline__ void lds_wait4_ge(unsigned char* lds, int off, u32 target) {
    for (u32 it = 0;; ++it) {
        asm volatile("" ::: "memory");
        const v4u f = *(const v4u*)(lds + off);
        const u32 a = f.x < f.y ? f.x : f.y, b = f.z < f.w ? f.z : f.w;
        if ((u32)__builtin_amdgcn_readfirstlane(a < b ? a : b) >= target) break;
        if (spin_give_up(lds, it, LDS_SPIN_CAP)) break;
    }
    asm volatile("" ::: "memory");
}
__device__ __forceinline__ void store16(v4u v, __amdgpu_buffer_rsrc_t rs, u32 voff, u32 soff) {
    __builtin_amdgcn_raw_buffer_store_b128(v, rs, voff + soff, 0, 16);
}
__device__ __forceinline__ u32 now_ticks() { return (u32)__builtin_amdgcn_s_memrealtime(); }
__device__ __forceinline__ void wait_until(u32 t0, u32 delay) {
    for (u32 it = 0; now_ticks() - t0 < delay && it < 4096; ++it) __builtin_amdgcn_s_sleep(1);
}
__device__ __forceinline__ bool valid16(v4u r) {
    u32 m = r.x > r.y ? r.x : r.y;
    m = m > r.z ? m : r.z;
    m = m > r.w ? m : r.w;
    return m != POISON;
}
__device__ __forceinline__ v4u poll16(__amdgpu_buffer_rsrc_t rs, u32 voff, u32 soff, unsigned char* lds) {
    v4u r = __builtin_amdgcn_raw_buffer_load_b128(rs, voff, soff, 16);
    bool ok = valid16(r);
    for (u32 it = 0; !__all(ok); ++it) {
        if (spin_give_up(lds, it * 4 + 3, GLB_SPIN_CAP * 4)) break;
        asm volatile("" ::: "memory");
        if (!ok) { r = __builtin_amdgcn_raw_buffer_load_b128(rs, voff, soff, 16); ok = valid16(r); }
    }
    return r;
}

__device__ __forceinline__ float vmul(float a, float b) {
    float r;
    asm("v_mul_f32 %0, %1, %2" : "=v"(r) : "v"(a), "v"(b));
    return r;
}
#define DPPB(t) " row_newbcast:" #t " row_mask:0xf bank_mask:0xf\n\t"
__device__ __forceinline__ void sub_rows(float (&y)[8], const float (&x)[8], float p) {
    asm("s_nop 1\n\t"
        "v_subrev_f32_dpp %0, %16, %8" DPPB(0) "v_subrev_f32_dpp %1, %16, %9" DPPB(1) "v_subrev_f32_dpp %2, %16, %10" DPPB(2)
        "v_subrev_f32_dpp %3, %16, %11" DPPB(3) "v_subrev_f32_dpp %4, %16, %12" DPPB(4) "v_subrev_f32_dpp %5, %16, %13" DPPB(5)
        "v_subrev_f32_dpp %6, %16, %14" DPPB(6) "v_subrev_f32_dpp %7, %16, %15" DPPB(7)
        : "=&v"(y[0]), "=&v"(y[1]), "=&v"(y[2]), "=&v"(y[3]), "=&v"(y[4]), "=&v"(y[5]), "=&v"(y[6]), "=&v"(y[7])
        : "v"(x[0]), "v"(x[1]), "v"(x[2]), "v"(x[3]), "v"(x[4]), "v"(x[5]), "v"(x[6]), "v"(x[7]), "v"(p));
}
__device__ __forceinline__ void norm_rows(float (&y)[8], float r, float m) {
    asm("s_nop 1\n\t"
        "v_mul_f32_dpp %0, %8, %0" DPPB(0) "v_mul_f32_dpp %1, %8, %1" DPPB(1) "v_mul_f32_dpp %2, %8, %2" DPPB(2) "v_mul_f32_dpp %3, %8, %3" DPPB(3)
        "v_mul_f32_dpp %4, %8, %4" DPPB(4) "v_mul_f32_dpp %5, %8, %5" DPPB(5) "v_mul_f32_dpp %6, %8, %6" DPPB(6) "v_mul_f32_dpp %7, %8, %7" DPPB(7)
        "v_subrev_f32_dpp %0, %9, %0" DPPB(0) "v_subrev_f32_dpp %1, %9, %1" DPPB(1) "v_subrev_f32_dpp %2, %9, %2" DPPB(2)
        "v_subrev_f32_dpp %3, %9, %3" DPPB(3) "v_subrev_f32_dpp %4, %9, %4" DPPB(4) "v_subrev_f32_dpp %5, %9, %5" DPPB(5)
        "v_subrev_f32_dpp %6, %9, %6" DPPB(6) "v_subrev_f32_dpp %7, %9, %7" DPPB(7)
        : "+v"(y[0]), "+v"(y[1]), "+v"(y[2]), "+v"(y[3]), "+v"(y[4]), "+v"(y[5]), "+v"(y[6]), "+v"(y[7])
        : "v"(r), "v"(m));
}
#undef DPPB
__device__ __forceinline__ void dot_rows(float (&x)[8], const u32 (&p)[4], const u32 (&m)[8]) {
    asm("v_dot2_f32_bf16 %0, %8, %12, %0\n\tv_dot2_f32_bf16 %1, %8, %13, %1\n\t"
        "v_dot2_f32_bf16 %2, %9, %14, %2\n\tv_dot2_f32_bf16 %3, %9, %15, %3\n\t"
        "v_dot2_f32_bf16 %4, %10, %16, %4\n\tv_dot2_f32_bf16 %5, %10, %17, %5\n\t"
        "v_dot2_f32_bf16 %6, %11, %18, %6\n\tv_dot2_f32_bf16 %7, %11, %19, %7\n\ts_nop 2"
        : "+v"(x[0]), "+v"(x[1]), "+v"(x[2]), "+v"(x[3]), "+v"(x[4]), "+v"(x[5]), "+v"(x[6]), "+v"(x[7])
        : "v"(p[0]), "v"(p[1]), "v"(p[2]), "v"(p[3]), "v"(m[0]), "v"(m[1]), "v"(m[2]), "v"(m[3]), "v"(m[4]), "v"(m[5]), "v"(m[6]), "v"(m[7]));
}
__device__ __forceinline__ void fma_rows(float (&x)[8], const float (&a)[8], const float (&b)[8]) {
    asm("v_fmac_f32 %0, %8, %16\n\tv_fmac_f32 %1, %9, %17\n\tv_fmac_f32 %2, %10, %18\n\tv_fmac_f32 %3, %11, %19\n\t"
        "v_fmac_f32 %4, %12, %20\n\tv_fmac_f32 %5, %13, %21\n\tv_fmac_f32 %6, %14, %22\n\tv_fmac_f32 %7, %15, %23"
        : "+v"(x[0]), "+v"(x[1]), "+v"(x[2]), "+v"(x[3]), "+v"(x[4]), "+v"(x[5]), "+v"(x[6]), "+v"(x[7])
        : "v"(a[0]), "v"(a[1]), "v"(a[2]), "v"(a[3]), "v"(a[4]), "v"(a[5]), "v"(a[6]), "v"(a[7]),
          "v"(b[0]), "v"(b[1]), "v"(b[2]), "v"(b[3]), "v"(b[4]), "v"(b[5]), "v"(b[6]), "v"(b[7]));
}
__device__ __forceinline__ float vfma(float a, float b, float c) {
    float r;
    asm("v_fma_f32 %0, %1, %2, %3" : "=v"(r) : "v"(a), "v"(b), "v"(c));
    return r;
}
__device__ __forceinline__ float vadd(float a, float b) {
    float r;
    asm("v_add_f32 %0, %1, %2" : "=v"(r) : "v"(a), "v"(b));
    return r;
}

template <class Hook>
__device__ __forceinline__ void row_stats(const float (&y)[8], const v4u& sel_lo, const v4u& sel_hi, unsigned char* lds, u32 red_rd, u32 red_wr,
                                          float& dmean, float& rstd, Hook after_barrier) {
    const v4u a = {pack_bf(y[0], y[1]), pack_bf(y[2], y[3]), pack_bf(y[4], y[5]), pack_bf(y[6], y[7])};
    float q[8];
    asm("v_mul_f32 %0, %8, %8\n\tv_mul_f32 %1, %9, %9\n\tv_mul_f32 %2, %10, %10\n\tv_mul_f32 %3, %11, %11\n\t"
        "v_mul_f32 %4, %12, %12\n\tv_mul_f32 %5, %13, %13\n\tv_mul_f32 %6, %14, %14\n\tv_mul_f32 %7, %15, %15"
        : "=&v"(q[0]), "=&v"(q[1]), "=&v"(q[2]), "=&v"(q[3]), "=&v"(q[4]), "=&v"(q[5]), "=&v"(q[6]), "=&v"(q[7])
        : "v"(y[0]), "v"(y[1]), "v"(y[2]), "v"(y[3]), "v"(y[4]), "v"(y[5]), "v"(y[6]), "v"(y[7]));
    const v4u s = {pack_bf(q[0], q[1]), pack_bf(q[2], q[3]), pack_bf(q[4], q[5]), pack_bf(q[6], q[7])};
    v4f d = {0, 0, 0, 0};
    d = __builtin_amdgcn_mfma_f32_16x16x32_bf16(__builtin_bit_cast(v8bf, a), __builtin_bit_cast(v8bf, sel_lo), d, 0, 0, 0);
    d = __builtin_amdgcn_mfma_f32_16x16x32_bf16(__builtin_bit_cast(v8bf, s), __builtin_bit_cast(v8bf, sel_hi), d, 0, 0, 0);
    u32 t0 = u_of((d.x + d.z) + (d.y + d.w)), t1 = t0;
    {
        auto r = __builtin_amdgcn_permlane16_swap(t0, t1, false, false);
        const u32 r0 = r[0], r1 = r[1];
        t0 = u_of(f_of(r0) + f_of(r1)); t1 = t0;
    }
    {
        auto r = __builtin_amdgcn_permlane32_swap(t0, t1, false, false);
        const u32 r0 = r[0], r1 = r[1];
        t0 = u_of(f_of(r0) + f_of(r1));
    }
    *(u32*)(lds + red_wr) = t0;
    __syncthreads();
    after_barrier();
    const v4f p0 = *(const v4f*)(lds + red_rd), p1 = *(const v4f*)(lds + red_rd + 16);
    const v4f pp = p0 + p1;
    const float tot = (pp.x + pp.z) + (pp.y + pp.w);
    const float s2 = dppf<0x128>(tot);
    dmean = tot * (1.0f / D_);
    const float var = fmaxf(s2 * (1.0f / D_) - dmean * dmean, 0.0f);
    rstd = __builtin_amdgcn_rsqf(var + 1e-5f);
}
__device__ __forceinline__ float wave_sum(float v) {
    asm("s_nop 1" : "+v"(v));
    v += dppf<0x128>(v);
    v += dppf<0x141>(v);
    v += dppf<0xB1>(v);
    v += dppf<0x4E>(v);
    u32 a = u_of(v), b = a;
    {
        auto r = __builtin_amdgcn_permlane16_swap(a, b, false, false);
        const u32 r0 = r[0], r1 = r[1];
        a = u_of(f_of(r0) + f_of(r1)); b = a;
    }
    auto r = __builtin_amdgcn_permlane32_swap(a, b, false, false);
    const u32 r0 = r[0], r1 = r[1];
    return f_of(r0) + f_of(r1);
}

__device__ __forceinline__ void load_afrags(v4u (&A)[8][4], unsigned char* lds, int region, int ch, int h, int l) {
    const int b = l & 15, g = l >> 4, par = (1 + b) & 1;
    const unsigned char* base = lds + region + ch * KC_CH + par * KC_COPY + 256 * h + 16 * g + 2 * (1 + b - par);
#pragma unroll
    for (int p = 0; p < 8; ++p)
#pragma unroll
        for (int r = 0; r < 4; ++r) {
            const u32* q = (const u32*)(base + 512 * p + 64 * r);
            A[p][r] = (v4u){q[0], q[1], q[2], q[3]};
        }
}

__device__ __forceinline__ v2f cexp_pow(float al, float be, float m) {
    const float e = __builtin_amdgcn_exp2f(al * m);
    const float ph = __builtin_amdgcn_fractf(be * m);
    return (v2f){e * __builtin_amdgcn_cosf(ph), e * __builtin_amdgcn_sinf(ph)};
}
__device__ __forceinline__ v2f cmul(v2f a, v2f b) {
    v2f t;
    asm("v_pk_mul_f32 %0, %1, %2 op_sel:[0,0] op_sel_hi:[1,0]\n\t"
        "v_pk_fma_f32 %0, %1, %2, %0 op_sel:[1,1,0] op_sel_hi:[0,1,1] neg_lo:[1,0,0]"
        : "=&v"(t) : "v"(a), "v"(b));
    return t;
}
__device__ __forceinline__ v4f mfma_bf(v4u a, v4u b, v4f c) {
    return __builtin_amdgcn_mfma_f32_16x16x32_bf16(__builtin_bit_cast(v8bf, a), __builtin_bit_cast(v8bf, b), c, 0, 0, 0);
}

extern "C" __global__ void __launch_bounds__(512, 2)
h3s4_kernel(const u16* __restrict__ q_, const u16* __restrict__ k_, const u16* __restrict__ v_, const u16* __restrict__ shk_,
            const u16* __restrict__ ssmk_, const u16* __restrict__ ssmD_, const u16* __restrict__ Are_, const u16* __restrict__ Aim_,
            const u16* __restrict__ Bre_, const u16* __restrict__ Bim_, const u16* __restrict__ Cre_, const u16* __restrict__ Cim_,
            const u16* __restrict__ logdt_, const u16* __restrict__ Dskip_, u16* __restrict__ out_, unsigned char* ws_) {
    __shared__ __attribute__((aligned(16))) unsigned char lds[LDS_BYTES];
    asm volatile("" ::"s"(q_), "s"(k_), "s"(v_), "s"(shk_), "s"(ssmk_), "s"(ssmD_), "s"(Are_), "s"(Aim_), "s"(Bre_), "s"(Bim_),
                 "s"(Cre_), "s"(Cim_), "s"(logdt_), "s"(Dskip_), "s"(out_), "s"(ws_));
    const int i = threadIdx.x, l = i & 63;
    const int w = __builtin_amdgcn_readfirstlane(i >> 6);
    u32 code_touch = 0;
    if (w == 0) {
        unsigned long long pc;
        u32 left;
        asm volatile("s_getpc_b64 %0\n.Lh3s4_pc:\n\ts_mov_b32 %1, .Lh3s4_end-.Lh3s4_pc+1536" : "=s"(pc), "=s"(left));
        const u32 nl = (left >> 12) + 1, off = ((blockIdx.x >> 3) * nl + l) * 128;
        if ((u32)l < nl && off + 4 <= left) code_touch = *(const __attribute__((address_space(1))) u32*)((pc & ~3ull) + off);
    }
    const int xcd = blockIdx.x & 7, idx = blockIdx.x >> 3;
    const int me = xcd * 32 + idx;
    const int mytile = idx >> 2;
    const int myr = 32 * mytile + 4 * xcd + (idx & 3);
    const __amdgpu_buffer_rsrc_t rs = __builtin_amdgcn_make_buffer_rsrc(ws_, 0, WS_TOTAL, 0x00020000);
    const v4u poison4 = {POISON, POISON, POISON, POISON};
    STAMP(0);

    const int cp = i & 31, rb = i >> 5;
    const int pcg = (xcd + idx) & 7;
    const int R0 = 64 * idx + 4 * rb, pch = 64 * pcg + 2 * cp;
    u32 kd[7], vd[4];
#pragma unroll
    for (int j = 0; j < 7; ++j) {
        const int Rj = R0 - 3 + j;
        kd[j] = *(const u32*)(k_ + (Rj < 0 ? 0 : Rj) * D_ + pch);
    }
#pragma unroll
    for (int j = 0; j < 4; ++j) vd[j] = *(const u32*)(v_ + (R0 + j) * D_ + pch);
    const v4u wd = *(const v4u*)(shk_ + pch * 4);
    u32 epoch = 0;
    if (w == 0) epoch = __builtin_amdgcn_raw_buffer_load_b32(rs, 0, WS_EPOCH, 16);
    u32 warm = 0;
    if (w == 7) {
        const unsigned char* p = ws_ + WS_KV - 128 + (size_t)(l & 7) * (2u << 20);
        if (l == 7) p = (const unsigned char*)out_ + myr * 8192;
        if (l == 8) p = (const unsigned char*)(ssmk_ + 2 * me * S_);
        if (l == 9) p = (const unsigned char*)(q_ + 8 * myr * D_);
        if (l == 10) p = (const unsigned char*)(Are_ + 2 * me * NS_);
        if (l == 11) p = (const unsigned char*)(Aim_ + 2 * me * NS_);
        if (l == 12) p = (const unsigned char*)(Bre_ + 2 * me * NS_);
        if (l == 13) p = (const unsigned char*)(Bim_ + 2 * me * NS_);
        if (l == 14) p = (const unsigned char*)(Cre_ + 2 * me * NS_);
        if (l == 15) p = (const unsigned char*)(Cim_ + 2 * me * NS_);
        if (l == 16) p = (const unsigned char*)(logdt_ + 2 * me);
        if (l == 17) p = (const unsigned char*)(ssmD_ + 2 * me);
        if (l == 18) p = (const unsigned char*)(Dskip_ + 2 * me);
        if (l < 19) warm = *(const u32*)p;
    }
    v4u xq[4];

    if (i >= 256 && i < 256 + 64) *(v4u*)(lds + L_UR + ((i >> 5) & 1) * UR_PITCH + 4096 + (i & 31) * 16) = (v4u){0, 0, 0, 0};
    if (i >= 320 && i < 320 + 16) {
        const int e = i - 320;
        *(v4u*)(lds + L_KA + (e >> 1) * KC_COPY + (e & 1) * 16) = (v4u){0, 0, 0, 0};
    }
    if (i >= 384 && i < 384 + 56) *(u32*)(lds + L_SYNC + (i - 384) * 4) = 0;

    STAMP(1);
    {
#pragma unroll
        for (int j = 0; j < 3; ++j) if (R0 - 3 + j < 0) kd[j] = 0;
#pragma unroll
        for (int c = 0; c < 2; ++c) {
            const u32 sel = c ? 0x07060302u : 0x05040100u;
            const u32 w01 = wd[2 * c], w23 = wd[2 * c + 1];
            u32 P[7];
#pragma unroll
            for (int j = 1; j < 7; ++j) P[j] = __builtin_amdgcn_perm(kd[j - 1], kd[j], sel);
            float res[4];
#pragma unroll
            for (int t = 0; t < 4; ++t) {
                float a = __builtin_amdgcn_fdot2_f32_bf16(__builtin_bit_cast(v2bf, P[t + 3]), __builtin_bit_cast(v2bf, w01), 0.0f, false);
                a = __builtin_amdgcn_fdot2_f32_bf16(__builtin_bit_cast(v2bf, P[t + 1]), __builtin_bit_cast(v2bf, w23), a, false);
                res[t] = a * (c ? bf_hi(vd[t]) : bf_lo(vd[t]));
            }
            typedef u32 v2u __attribute__((ext_vector_type(2)));
            const int half = (1 - (rb & 1)) ^ ((cp >> 3) & 1);
            *(v2u*)(lds + L_T + (2 * cp + c) * 128 + (((rb >> 1) ^ (cp & 7)) * 16) + half * 8) = (v2u){pack_bf(res[3], res[2]), pack_bf(res[1], res[0])};
        }
    }
    SCHED_FENCE();
    if (w < 4) {
#pragma unroll
        for (int j = 0; j < 2; ++j) {
            const int e = i + 256 * j;
            store16(poison4, rs, (e >> 3) * C_PITCH + me * 128 + (e & 7) * 16, WS_C);
        }
    }
    u32 pp[11], dA, dB;
    if (w >= 4) {
        const int pc = 2 * me + ((w - 4) >> 1), pi = pc * NS_ + (i & 127), pj = pi ^ 64;
        pp[0] = Are_[pi]; pp[1] = Aim_[pi]; pp[2] = Bre_[pi]; pp[3] = Bim_[pi]; pp[4] = Cre_[pi]; pp[5] = Cim_[pi];
        pp[6] = Bre_[pj]; pp[7] = Bim_[pj]; pp[8] = Cre_[pj]; pp[9] = Cim_[pj];
        pp[10] = logdt_[pc];
        dA = ssmD_[2 * me + (l & 1)]; dB = Dskip_[2 * me + (l & 1)];
        if (w >= 6) {
#pragma unroll
            for (int j = 0; j < 4; ++j) xq[j] = *(const v4u*)(q_ + (8 * myr + 4 * (w & 1) + j) * D_ + l * 8);
        }
    }
    STAMP(2);
    __syncthreads();
    STAMP(3);

    v4u A[8][4];
    if (w < 4) {
#pragma unroll
        for (int j = 0; j < 2; ++j) {
            typedef u32 v2u __attribute__((ext_vector_type(2)));
            const int e = i + 256 * j, ch = e >> 3, chunk = e & 7, sw = (ch >> 4) & 1;
            const unsigned char* src = lds + L_T + ch * 128 + ((chunk ^ ((ch >> 1) & 7)) * 16);
            const v2u d0 = *(const v2u*)(src + 8 * sw), d1 = *(const v2u*)(src + 8 * (1 - sw));
            store16((v4u){d0.x, d0.y, d1.x, d1.y}, rs, (64 * pcg + ch) * KV_PITCH + (8 * idx + chunk) * 16, WS_KV);
        }
        SCHED_FENCE();
        asm volatile("" ::: "memory");
        v4u sk[3];
        u32 sknext[3];
#pragma unroll
        for (int j = 0; j < 3; ++j) {
            const int e = (i - 64) + 192 * j, ec = (w == 0 || e > 511) ? 0 : e;
            const u16* src = ssmk_ + (2 * me + (ec >> 8)) * S_ + (ec & 255) * 8;
            if (w != 0) {
                sk[j] = *(const v4u*)src;
                sknext[j] = *(const u32*)(src + ((ec & 255) < 255 ? 8 : 0));
            }
        }
        SCHED_FENCE();
        if (w == 0) asm volatile("s_waitcnt vmcnt(0)" ::: "memory");
        else asm volatile("s_waitcnt vmcnt(6)" ::: "memory");
        lds_flag_inc(SY_KV);
        STAMP(4);
        u32 tag = 0, tflag = 0;
        if (w == 0) {
            lds_wait_ge(lds, SY_KV, 4);
            tag = __builtin_amdgcn_readfirstlane(epoch) + 1u;
            if (l == 0) __builtin_amdgcn_raw_buffer_store_b32(tag, rs, me * 128, WS_FLAGS, 16);
            tflag = now_ticks();
            lds_flag_set(SY_TAG, tag);
            lds_flag_set(SY_TAGSET, 1u);
        }
        if (w != 0) {
#pragma unroll
            for (int j = 0; j < 3; ++j) {
                const int e = (i - 64) + 192 * j, sm = e & 255;
                if (e < 512) {
                    unsigned char* c0 = lds + L_KA + (e >> 8) * KC_CH;
                    const u32 nx = sm < 255 ? sknext[j] : 0u;
                    *(v4u*)(c0 + 32 + sm * 16) = sk[j];
                    *(v4u*)(c0 + KC_COPY + 32 + sm * 16) =
                        (v4u){__builtin_amdgcn_alignbit(sk[j].y, sk[j].x, 16), __builtin_amdgcn_alignbit(sk[j].z, sk[j].y, 16),
                              __builtin_amdgcn_alignbit(sk[j].w, sk[j].z, 16), __builtin_amdgcn_alignbit(nx, sk[j].w, 16)};
                    if (sm == 0) *(u32*)(c0 + KC_COPY + 28) = sk[j].x << 16;
                }
            }
            lds_flag_inc(SY_KA);
        }
        if (w == 0) {
            const u32 foff = (((xcd - (l & 31)) & 7) * 32 + (l & 31)) * 128;
            wait_until(tflag, KV_DELAY);
            for (u32 it = 0;; ++it) {
                asm volatile("" ::: "memory");
                if (__all(__builtin_amdgcn_raw_buffer_load_b32(rs, foff, WS_FLAGS, 16) == tag)) break;
                if (spin_give_up(lds, it * 4 + 3, GLB_SPIN_CAP * 4)) break;
            }
            lds_flag_set(SY_GO, 1u);
        } else {
            lds_wait_ge(lds, SY_KA, 3);
            load_afrags(A, lds, L_KA, w >> 1, w & 1, l);
            lds_wait_ge(lds, SY_GO, 1u);
        }
        STAMP(5);
        v4u g0, g1;
        {
            const u32 off = (2 * me + (l >> 5)) * KV_PITCH + (64 * w + (l & 31)) * 16;
            g0 = __builtin_amdgcn_raw_buffer_load_b128(rs, off, WS_KV, 16);
            g1 = __builtin_amdgcn_raw_buffer_load_b128(rs, off + 512, WS_KV, 16);
        }
        SCHED_FENCE();
        if (w == 0) {
            lds_wait_ge(lds, SY_KA, 3);
            load_afrags(A, lds, L_KA, 0, 0, l);
        }
        STAMP(6);
        unsigned char* dst = lds + L_UR + (l >> 5) * UR_PITCH + 2 * (2040 - 8 * (64 * w + (l & 31)));
        *(v4u*)dst = g0;
        *(v4u*)(dst - 512) = g1;
        lds_flag_set(SY_INTILE + 8 * w, 1u);
        lds_flag_set(SY_INTILE + 8 * w + 4, 1u);
        WAIT_VM0();

    } else {
        __builtin_amdgcn_s_setprio(2);
        const int wa = w - 4, ch = wa >> 1, n = (i - 256) & 127;
#pragma unroll
        for (int j = 0; j < 11; ++j) asm volatile("" : "+v"(pp[j]));
        asm volatile("" : "+v"(dA), "+v"(dB));
        const float ex = expf(f_of(pp[10] << 16));
        const float dt = bf_lo(pack_bf(ex, ex));
        const float al = dt * f_of(pp[0] << 16) * 1.44269504088896341f, be = dt * f_of(pp[1] << 16) * 0.15915494309189535f;
        v2f W;
        float inv;
        {
            const float br = dt * f_of(pp[2] << 16), bi = dt * f_of(pp[3] << 16), cr = f_of(pp[4] << 16), ci = f_of(pp[5] << 16);
            const float br2 = dt * f_of(pp[6] << 16), bi2 = dt * f_of(pp[7] << 16), cr2 = f_of(pp[8] << 16), ci2 = f_of(pp[9] << 16);
            W = (v2f){cr * br - ci * bi, cr * bi + ci * br};
            float mx = fmaxf(fmaxf(fabsf(W.x), fabsf(W.y)), fmaxf(fabsf(cr2 * br2 - ci2 * bi2), fabsf(cr2 * bi2 + ci2 * br2)));
            mx = fmaxf(mx, dppf<0x128>(mx));
            mx = fmaxf(mx, dppf<0x141>(mx));
            mx = fmaxf(mx, dppf<0xB1>(mx));
            mx = fmaxf(mx, dppf<0x4E>(mx));
            float m0 = f_of((u32)__builtin_amdgcn_readlane((int)u_of(mx), 0)), m1 = f_of((u32)__builtin_amdgcn_readlane((int)u_of(mx), 16));
            float m2 = f_of((u32)__builtin_amdgcn_readlane((int)u_of(mx), 32)), m3 = f_of((u32)__builtin_amdgcn_readlane((int)u_of(mx), 48));
            const int e = __builtin_amdgcn_frexp_expf(fmaxf(fmaxf(m0, m1), fmaxf(m2, m3)));
            W = (v2f){__builtin_amdgcn_ldexpf(W.x, 14 - e), __builtin_amdgcn_ldexpf(W.y, 14 - e)};
            inv = __builtin_amdgcn_ldexpf(1.0f, e - 14);
        }
        unsigned char* tab = lds + L_TAB + ch * TAB_CH + n * 4;
#define TAB_PUT(row, val) (*(u32*)(tab + (row) * TAB_ROW) = pack_h(val))
        const v2f E1 = cexp_pow(al, be, 1.0f), E8 = cexp_pow(al, be, 8.0f), E128 = cexp_pow(al, be, 128.0f);
        const v2f E256 = cmul(E128, E128), E384 = cmul(E256, E128), E512 = cmul(E256, E256), E1024 = cmul(E512, E512);
        {
            const v2f E8c = {E8.x, -E8.y};
            v2f B = {W.x, -W.y}, P = {1.0f, 0.0f}, Q = E512, Rr = E1024, P4 = cmul(cmul(E1, E1), cmul(E1, E1));
#pragma unroll
            for (int r = 0; r < 8; ++r) {
                TAB_PUT(41 + 2 * r, B);
                B = cmul(B, E8c);
                TAB_PUT(42 + 2 * r, B);
                if (r < 7) B = cmul(B, E8c);
                TAB_PUT(r, P);
                TAB_PUT(8 + r, r ? cmul(P, E128) : E128);
                if ((r & 1) == 0) {
                    TAB_PUT(16 + (r >> 1), r ? cmul(P, E256) : E256);
                    TAB_PUT(20 + (r >> 1), r ? cmul(P, E384) : E384);
                }
                if (r < 7) P = r ? cmul(P, E1) : E1;
                TAB_PUT(32 + r, Rr);
                if (r < 7) Rr = cmul(Rr, E128);
                if (r < 4) {
                    TAB_PUT(24 + 2 * r, Q);
                    TAB_PUT(25 + 2 * r, cmul(Q, P4));
                    if (r < 3) Q = cmul(Q, E128);
                }
            }
            TAB_PUT(40, cmul(E1024, E1024));
        }
#undef TAB_PUT
        if (l == 0) *(float*)(lds + L_DCO + 16 + ch * 4) = inv;
        lds_flag_inc(SY_TAB + 4 * ch);
        if (l < 2) { *(u32*)(lds + L_DCO + l * 4) = dA << 16; *(u32*)(lds + L_DCO + 8 + l * 4) = dB << 16; }
        STAMP(4);
        if (w < 6) {
            const int kc = w & 1;
            lds_wait_ge(lds, SY_TAB + 4 * kc, 2);
            {
                const int row = l & 15, g = l >> 4;
                const unsigned char* tb = lds + L_TAB + kc * TAB_CH + row * TAB_ROW + g * 16;
                float* kt = (float*)(lds + L_KT + kc * KT_CH);
                const float scale = *(const float*)(lds + L_DCO + 16 + kc * 4);
                v4u fb[8], fa[8];
                v4f acc[3];
#pragma unroll
                for (int s2 = 0; s2 < 8; ++s2) fb[s2] = *(const v4u*)(tb + 41 * TAB_ROW + s2 * 64);
#pragma unroll
                for (int tile = 0; tile < 3; ++tile) {
#pragma unroll
                    for (int s2 = 0; s2 < 8; ++s2) fa[s2] = *(const v4u*)(tb + tile * 16 * TAB_ROW + s2 * 64);
                    acc[tile] = (v4f){0, 0, 0, 0};
#pragma unroll
                    for (int s2 = 0; s2 < 8; ++s2)
                        acc[tile] = __builtin_amdgcn_mfma_f32_16x16x32_f16(__builtin_bit_cast(v8h, fa[s2]), __builtin_bit_cast(v8h, fb[s2]), acc[tile], 0, 0, 0);
                }
                asm volatile("s_nop 7\n\ts_nop 1" : "+v"(acc[0]), "+v"(acc[1]), "+v"(acc[2]));
#pragma unroll
                for (int r = 0; r < 4; ++r) {
                    const int rw = 4 * g + r;
                    kt[128 * (rw >> 3) + (rw & 7) + 8 * row] = vmul(acc[0][r], scale);
                    kt[rw < 8 ? 256 + 64 * (rw >> 2) + (rw & 3) + 4 * row : 384 + 32 * ((rw - 8) >> 1) + (rw & 1) + 2 * row] = vmul(acc[1][r], scale);
                    kt[rw < 8 ? 512 + 16 * rw + row : ((rw == 8 && row == 0) ? 640 : 644 + (l & 3))] = vmul(acc[2][r], scale);
                }
            }
            asm volatile("s_waitcnt lgkmcnt(0)" ::: "memory");
            {
                const float* t = (const float*)(lds + L_KT + kc * KT_CH);
                unsigned char* c0 = lds + L_KB + kc * KC_CH;
                const float a2 = t[512 + l], b2 = t[513 + l], a3 = t[576 + l], b3 = t[577 + l];
                const float v0 = t[384 + 2 * l], v1 = t[385 + 2 * l], v2 = t[386 + 2 * l];
                float kvv[9];
#pragma unroll
                for (int j = 0; j < 9; ++j) {
                    const int lag = 8 * l + j;
                    const u32 bb = pack_bf((float)lag, 0.0f) & 0xFFFFu;
                    kvv[j] = t[l < 32 ? lag : (int)(bb - 0x4280u)];
                }
                {
                    const u32 e0 = pack_bf(kvv[0], kvv[1]);
                    *(v4u*)(c0 + 32 + l * 16) = (v4u){e0, pack_bf(kvv[2], kvv[3]), pack_bf(kvv[4], kvv[5]), pack_bf(kvv[6], kvv[7])};
                    *(v4u*)(c0 + KC_COPY + 32 + l * 16) = (v4u){pack_bf(kvv[1], kvv[2]), pack_bf(kvv[3], kvv[4]), pack_bf(kvv[5], kvv[6]), pack_bf(kvv[7], kvv[8])};
                    if (l == 0) *(u32*)(c0 + KC_COPY + 28) = e0 << 16;
                }
                {
                    const u32 p00 = pack_bf(v0, v0), p01 = pack_bf(v0, v1), p11 = pack_bf(v1, v1), p12 = pack_bf(v1, v2), p22 = pack_bf(v2, v2);
                    *(v4u*)(c0 + 32 + (64 + l) * 16) = (v4u){p00, p01, p11, p22};
                    *(v4u*)(c0 + KC_COPY + 32 + (64 + l) * 16) = (v4u){p00, p11, p12, p22};
                }
#pragma unroll
                for (int it = 0; it < 2; ++it) {
                    const float a = it ? a3 : a2, b = it ? b3 : b2, c4 = (l & 1) ? b : a;
                    const u32 paa = pack_bf(a, a), pbb = pack_bf(b, b);
                    *(v4u*)(c0 + 32 + (128 + 64 * it + l) * 16) = (v4u){paa, paa, pack_bf(c4, b), pbb};
                    *(v4u*)(c0 + KC_COPY + 32 + (128 + 64 * it + l) * 16) = (v4u){paa, pack_bf(a, c4), pbb, pbb};
                }
            }
            lds_flag_inc(SY_KREADY);
            STAMP(5);
            if (w == 4) {
                lds_wait_ge(lds, SY_TAGSET, 1u);
                const u32 tag = __builtin_amdgcn_readfirstlane(lds_ld(lds, SY_TAG));
                lds_wait_ge_slow(lds, SY_GO, 1u);
                wait_until(now_ticks(), ALLF_DELAY);
                for (u32 it = 0;; ++it) {
                    asm volatile("" ::: "memory");
                    if (it) __builtin_amdgcn_s_sleep(8);
                    const u32 f0 = __builtin_amdgcn_raw_buffer_load_b32(rs, l * 128, WS_FLAGS, 16);
                    const u32 f1 = __builtin_amdgcn_raw_buffer_load_b32(rs, (l + 64) * 128, WS_FLAGS, 16);
                    const u32 f2 = __builtin_amdgcn_raw_buffer_load_b32(rs, (l + 128) * 128, WS_FLAGS, 16);
                    const u32 f3 = __builtin_amdgcn_raw_buffer_load_b32(rs, (l + 192) * 128, WS_FLAGS, 16);
                    if (__all(f0 == tag && f1 == tag && f2 == tag && f3 == tag)) break;
                    if (spin_give_up(lds, it * 4 + 3, GLB_SPIN_CAP * 4)) break;
                }
                if (me == 0 && l == 0) __builtin_amdgcn_raw_buffer_store_b32(tag, rs, 0, WS_EPOCH, 16);
                lds_flag_set(SY_ALLF, 1u);
            }
            WAIT_VM0();
        } else {
            const int kr = w & 1;
            lds_wait_ge(lds, SY_KV, 4);
#pragma unroll
            for (int j = 0; j < 4; ++j) store16(poison4, rs, myr * U_PITCH + ((i & 127) + 128 * j) * 16, WS_U);
            STAMP(5);
#pragma unroll
            for (int j = 0; j < 4; ++j) asm volatile("" : "+v"(xq[j]));
            {
                float f[4][8], mean[4], rstd[4];
#pragma unroll
                for (int rr = 0; rr < 4; ++rr) {
                    const v4u xr = xq[rr];
                    f[rr][0] = bf_lo(xr.x); f[rr][1] = bf_hi(xr.x); f[rr][2] = bf_lo(xr.y); f[rr][3] = bf_hi(xr.y);
                    f[rr][4] = bf_lo(xr.z); f[rr][5] = bf_hi(xr.z); f[rr][6] = bf_lo(xr.w); f[rr][7] = bf_hi(xr.w);
                    mean[rr] = vadd(vadd(vadd(f[rr][0], f[rr][1]), vadd(f[rr][2], f[rr][3])), vadd(vadd(f[rr][4], f[rr][5]), vadd(f[rr][6], f[rr][7])));
                }
#pragma unroll
                for (int rr = 0; rr < 4; ++rr) mean[rr] = vmul(wave_sum(mean[rr]), -1.0f / D_);
#pragma unroll
                for (int rr = 0; rr < 4; ++rr) {
#pragma unroll
                    for (int j = 0; j < 8; ++j) f[rr][j] = vadd(f[rr][j], mean[rr]);
                    float q2 = vmul(f[rr][0], f[rr][0]);
#pragma unroll
                    for (int j = 1; j < 8; ++j) q2 = vfma(f[rr][j], f[rr][j], q2);
                    rstd[rr] = q2;
                }
#pragma unroll
                for (int rr = 0; rr < 4; ++rr) rstd[rr] = __builtin_amdgcn_rsqf(vfma(wave_sum(rstd[rr]), 1.0f / D_, 1e-5f));
                asm volatile("s_nop 1" : "+v"(rstd[0]), "+v"(rstd[1]), "+v"(rstd[2]), "+v"(rstd[3]));
#pragma unroll
                for (int rr = 0; rr < 4; ++rr) {
                    const v4u xr = xq[rr];
                    u32 o[8];
#pragma unroll
                    for (int j = 0; j < 4; ++j) {
                        const u32 np = pack_bf(vmul(f[rr][2 * j], rstd[rr]), vmul(f[rr][2 * j + 1], rstd[rr]));
                        o[2 * j] = __builtin_amdgcn_perm(np, xr[j], 0x05040100u);
                        o[2 * j + 1] = __builtin_amdgcn_perm(np, xr[j], 0x07060302u);
                    }
                    unsigned char* dstx = lds + L_XN + (4 * kr + rr) * 2048 + l * 32;
                    *(v4u*)dstx = (v4u){o[0], o[1], o[2], o[3]};
                    *(v4u*)(dstx + 16) = (v4u){o[4], o[5], o[6], o[7]};
                    if (l == 0) *(float*)(lds + L_PIV + (4 * kr + rr) * 4) = -mean[rr];
                }
            }
            lds_flag_inc(SY_X0);
            WAIT_VM0();
            lds_flag_inc(SY_POIS);
            lds_wait_ge(lds, SY_POIS, 2);
            STAMP(6);
        }
    }

    asm volatile("" ::"v"(code_touch), "v"(warm));
    WAIT_VM0();
    const int i_ = i;
    const __amdgpu_buffer_rsrc_t ro = __builtin_amdgcn_make_buffer_rsrc(out_, 0, S_ * D_ * 2, 0x00020000);

#pragma unroll 1
    for (int k = 0; k < 3; ++k) {
        int i = i_;
        asm volatile("" : "+v"(i));
        const int l = i & 63;
        STAMP(7 + 8 * k);
        const u32 cvoff = (myr >> 2) * C_PITCH + (i >> 1) * 128 + (myr & 3) * 32 + (i & 1) * 16, csoff = WS_C + k * C_SIZE;
        v4u cg = poison4;
        bool cissued = false;
        if (w < 4) {
            const int ch = w >> 1, h = w & 1, a = l & 15, g = l >> 4;
            const unsigned char* ub = lds + L_UR + ch * UR_PITCH + 2 * (2032 + 128 * h - 16 * a + 8 * g) - 3584;
            const u32 ready = (u32)(k + 1);
            if (k == 1 && mytile < KLOAD_SPLIT) load_afrags(A, lds, L_KB, ch, h, l);
            v4f acc[8];
#pragma unroll
            for (int c = 0; c < 8; ++c) acc[c] = (v4f){0, 0, 0, 0};
#pragma unroll
            for (int c = 0; c < 8; ++c) asm volatile("" : "+v"(acc[c]));
            v4u B[2][4];
            __builtin_amdgcn_s_setprio(1);
            for (u32 it = 0;; ++it) {
                asm volatile("" ::: "memory");
                const u32 f0 = lds_ld(lds, SY_INTILE);
                asm volatile("" ::: "memory");
#pragma unroll
                for (int r = 0; r < 4; ++r) B[0][r] = *(const v4u*)(ub + 512 * 7 + 64 * r);
                if (__builtin_expect((u32)__builtin_amdgcn_readfirstlane(f0) >= ready, 1)) break;
                if (spin_give_up(lds, it, LDS_SPIN_CAP)) break;
            }
            STAMP(8 + 8 * k);
#define MFA "v_mfma_f32_16x16x32_bf16 "
#define MF1(cur, c, p, r)                                                                                     \
    asm volatile(MFA "%0, %1, %2, %0" : "+v"(acc[c]) : "v"(A[p][r]), "v"(B[cur][r]));                          \
    SCHED_FENCE();
#define MF4(cur, c, p)                                                                                        \
    asm volatile(MFA "%0, %1, %5, %0\n\t" MFA "%0, %2, %6, %0\n\t" MFA "%0, %3, %7, %0\n\t" MFA "%0, %4, %8, %0" \
                 : "+v"(acc[c]) : "v"(A[p][0]), "v"(A[p][1]), "v"(A[p][2]), "v"(A[p][3]),                      \
                   "v"(B[cur][0]), "v"(B[cur][1]), "v"(B[cur][2]), "v"(B[cur][3]));                             \
    SCHED_FENCE();
            u32 vflag = SY_PART + 4 * w, vready = ready, vpart = L_PART + ch * PART_CH + h * 1024 + 16 * (16 * g + ((a + 8 * (g >> 1)) & 15));
            asm volatile("" : "+v"(vflag), "+v"(vready), "+v"(vpart));
            u32 fl = 0;
            MF1(0, 0, 0, 0)
            fl = lds_ld(lds, SY_INTILE + 4); SCHED_FENCE();
            MF1(0, 0, 0, 1)
            B[1][0] = *(const v4u*)(ub + 3072); SCHED_FENCE();
            MF1(0, 0, 0, 2)
            B[1][1] = *(const v4u*)(ub + 3136); SCHED_FENCE();
            MF1(0, 0, 0, 3)
            B[1][2] = *(const v4u*)(ub + 3200); SCHED_FENCE();
            MF1(0, 1, 1, 0)
            B[1][3] = *(const v4u*)(ub + 3264); SCHED_FENCE();
            MF1(0, 1, 1, 1)
            MF1(0, 1, 1, 2)
            put_partial<0>(vpart, acc[0]); SCHED_FENCE();
            MF1(0, 1, 1, 3)
            MF1(0, 2, 2, 0)
            put_partial_flag<0>(vflag, vready); SCHED_FENCE();
            MF1(0, 2, 2, 1)
            const u32 fls0 = (u32)__builtin_amdgcn_readfirstlane(fl); SCHED_FENCE();
            MF1(0, 2, 2, 2)
            MF1(0, 2, 2, 3)
            MF4(0, 3, 3)
            MF4(0, 4, 4)
            MF4(0, 5, 5)
            MF4(0, 6, 6)
            MF4(0, 7, 7)
            if (__builtin_expect(fls0 < ready, 0)) {
                lds_wait_ge(lds, SY_INTILE + 4, ready);
#pragma unroll
                for (int r = 0; r < 4; ++r) B[1][r] = *(const v4u*)(ub + 3072 + 64 * r);
            }
            asm volatile("" ::: "memory");
            SCHED_FENCE();
            MF1(1, 1, 0, 0)
            fl = lds_ld(lds, SY_INTILE + 8); SCHED_FENCE();
            MF1(1, 1, 0, 1)
            B[0][0] = *(const v4u*)(ub + 2560); SCHED_FENCE();
            MF1(1, 1, 0, 2)
            B[0][1] = *(const v4u*)(ub + 2624); SCHED_FENCE();
            MF1(1, 1, 0, 3)
            B[0][2] = *(const v4u*)(ub + 2688); SCHED_FENCE();
            MF1(1, 2, 1, 0)
            B[0][3] = *(const v4u*)(ub + 2752); SCHED_FENCE();
            MF1(1, 2, 1, 1)
            MF1(1, 2, 1, 2)
            put_partial<1>(vpart, acc[1]); SCHED_FENCE();
            MF1(1, 2, 1, 3)
            MF1(1, 3, 2, 0)
            put_partial_flag<1>(vflag, vready); SCHED_FENCE();
            MF1(1, 3, 2, 1)
            const u32 fls1 = (u32)__builtin_amdgcn_readfirstlane(fl); SCHED_FENCE();
            MF1(1, 3, 2, 2)
            MF1(1, 3, 2, 3)
            MF4(1, 4, 3)
            MF4(1, 5, 4)
            MF4(1, 6, 5)
            MF4(1, 7, 6)
            if (__builtin_expect(fls1 < ready, 0)) {
                lds_wait_ge(lds, SY_INTILE + 8, ready);
#pragma unroll
                for (int r = 0; r < 4; ++r) B[0][r] = *(const v4u*)(ub + 2560 + 64 * r);
            }
            asm volatile("" ::: "memory");
            SCHED_FENCE();
            MF1(0, 2, 0, 0)
            fl = lds_ld(lds, SY_INTILE + 12); SCHED_FENCE();
            MF1(0, 2, 0, 1)
            B[1][0] = *(const v4u*)(ub + 2048); SCHED_FENCE();
            MF1(0, 2, 0, 2)
            B[1][1] = *(const v4u*)(ub + 2112); SCHED_FENCE();
            MF1(0, 2, 0, 3)
            B[1][2] = *(const v4u*)(ub + 2176); SCHED_FENCE();
            MF1(0, 3, 1, 0)
            B[1][3] = *(const v4u*)(ub + 2240); SCHED_FENCE();
            MF1(0, 3, 1, 1)
            MF1(0, 3, 1, 2)
            put_partial<2>(vpart, acc[2]); SCHED_FENCE();
            MF1(0, 3, 1, 3)
            MF1(0, 4, 2, 0)
            put_partial_flag<2>(vflag, vready); SCHED_FENCE();
            MF1(0, 4, 2, 1)
            const u32 fls2 = (u32)__builtin_amdgcn_readfirstlane(fl); SCHED_FENCE();
            MF1(0, 4, 2, 2)
            MF1(0, 4, 2, 3)
            MF4(0, 5, 3)
            MF4(0, 6, 4)
            MF4(0, 7, 5)
            if (__builtin_expect(fls2 < ready, 0)) {
                lds_wait_ge(lds, SY_INTILE + 12, ready);
#pragma unroll
                for (int r = 0; r < 4; ++r) B[1][r] = *(const v4u*)(ub + 2048 + 64 * r);
            }
            asm volatile("" ::: "memory");
            SCHED_FENCE();
            MF1(1, 3, 0, 0)
            fl = lds_ld(lds, SY_INTILE + 16); SCHED_FENCE();
            MF1(1, 3, 0, 1)
            B[0][0] = *(const v4u*)(ub + 1536); SCHED_FENCE();
            MF1(1, 3, 0, 2)
            B[0][1] = *(const v4u*)(ub + 1600); SCHED_FENCE();
            MF1(1, 3, 0, 3)
            B[0][2] = *(const v4u*)(ub + 1664); SCHED_FENCE();
            MF1(1, 4, 1, 0)
            B[0][3] = *(const v4u*)(ub + 1728); SCHED_FENCE();
            MF1(1, 4, 1, 1)
            MF1(1, 4, 1, 2)
            put_partial<3>(vpart, acc[3]); SCHED_FENCE();
            MF1(1, 4, 1, 3)
            MF1(1, 5, 2, 0)
            put_partial_flag<3>(vflag, vready); SCHED_FENCE();
            MF1(1, 5, 2, 1)
            const u32 fls3 = (u32)__builtin_amdgcn_readfirstlane(fl); SCHED_FENCE();
            MF1(1, 5, 2, 2)
            MF1(1, 5, 2, 3)
            MF4(1, 6, 3)
            MF4(1, 7, 4)
            if (__builtin_expect(fls3 < ready, 0)) {
                lds_wait_ge(lds, SY_INTILE + 16, ready);
#pragma unroll
                for (int r = 0; r < 4; ++r) B[0][r] = *(const v4u*)(ub + 1536 + 64 * r);
            }
            asm volatile("" ::: "memory");
            SCHED_FENCE();
            MF1(0, 4, 0, 0)
            fl = lds_ld(lds, SY_INTILE + 20); SCHED_FENCE();
            MF1(0, 4, 0, 1)
            B[1][0] = *(const v4u*)(ub + 1024); SCHED_FENCE();
            MF1(0, 4, 0, 2)
            B[1][1] = *(const v4u*)(ub + 1088); SCHED_FENCE();
            MF1(0, 4, 0, 3)
            B[1][2] = *(const v4u*)(ub + 1152); SCHED_FENCE();
            MF1(0, 5, 1, 0)
            B[1][3] = *(const v4u*)(ub + 1216); SCHED_FENCE();
            MF1(0, 5, 1, 1)
            MF1(0, 5, 1, 2)
            put_partial<4>(vpart, acc[4]); SCHED_FENCE();
            MF1(0, 5, 1, 3)
            MF1(0, 6, 2, 0)
            put_partial_flag<4>(vflag, vready); SCHED_FENCE();
            MF1(0, 6, 2, 1)
            const u32 fls4 = (u32)__builtin_amdgcn_readfirstlane(fl); SCHED_FENCE();
            MF1(0, 6, 2, 2)
            MF1(0, 6, 2, 3)
            MF4(0, 7, 3)
            if (__builtin_expect(fls4 < ready, 0)) {
                lds_wait_ge(lds, SY_INTILE + 20, ready);
#pragma unroll
                for (int r = 0; r < 4; ++r) B[1][r] = *(const v4u*)(ub + 1024 + 64 * r);
            }
            asm volatile("" ::: "memory");
            SCHED_FENCE();
            MF1(1, 5, 0, 0)
            fl = lds_ld(lds, SY_INTILE + 24); SCHED_FENCE();
            MF1(1, 5, 0, 1)
            B[0][0] = *(const v4u*)(ub + 512); SCHED_FENCE();
            MF1(1, 5, 0, 2)
            B[0][1] = *(const v4u*)(ub + 576); SCHED_FENCE();
            MF1(1, 5, 0, 3)
            B[0][2] = *(const v4u*)(ub + 640); SCHED_FENCE();
            MF1(1, 6, 1, 0)
            B[0][3] = *(const v4u*)(ub + 704); SCHED_FENCE();
            MF1(1, 6, 1, 1)
            MF1(1, 6, 1, 2)
            put_partial<5>(vpart, acc[5]); SCHED_FENCE();
            MF1(1, 6, 1, 3)
            MF1(1, 7, 2, 0)
            put_partial_flag<5>(vflag, vready); SCHED_FENCE();
            MF1(1, 7, 2, 1)
            const u32 fls5 = (u32)__builtin_amdgcn_readfirstlane(fl); SCHED_FENCE();
            MF1(1, 7, 2, 2)
            MF1(1, 7, 2, 3)
            if (__builtin_expect(fls5 < ready, 0)) {
                lds_wait_ge(lds, SY_INTILE + 24, ready);
#pragma unroll
                for (int r = 0; r < 4; ++r) B[0][r] = *(const v4u*)(ub + 512 + 64 * r);
            }
            asm volatile("" ::: "memory");
            SCHED_FENCE();
            MF1(0, 6, 0, 0)
            fl = lds_ld(lds, SY_INTILE + 28); SCHED_FENCE();
            MF1(0, 6, 0, 1)
            B[1][0] = *(const v4u*)(ub + 0); SCHED_FENCE();
            MF1(0, 6, 0, 2)
            B[1][1] = *(const v4u*)(ub + 64); SCHED_FENCE();
            MF1(0, 6, 0, 3)
            B[1][2] = *(const v4u*)(ub + 128); SCHED_FENCE();
            MF1(0, 7, 1, 0)
            B[1][3] = *(const v4u*)(ub + 192); SCHED_FENCE();
            MF1(0, 7, 1, 1)
            MF1(0, 7, 1, 2)
            put_partial<6>(vpart, acc[6]); SCHED_FENCE();
            MF1(0, 7, 1, 3)
            put_partial_flag<6>(vflag, vready); SCHED_FENCE();
            const u32 fls6 = (u32)__builtin_amdgcn_readfirstlane(fl); SCHED_FENCE();
            if (__builtin_expect(fls6 < ready, 0)) {
                lds_wait_ge(lds, SY_INTILE + 28, ready);
#pragma unroll
                for (int r = 0; r < 4; ++r) B[1][r] = *(const v4u*)(ub + 0 + 64 * r);
            }
            asm volatile("" ::: "memory");
            STAMP(9 + 8 * k); if (k == 0) STAMPC(30);
            SCHED_FENCE();
            MF4(1, 7, 0)
            asm volatile("s_nop 7" ::: "memory");
            put_partial<7>(vpart, acc[7]); put_partial_flag<7>(vflag, vready);
            {
                const u32 pub = __builtin_amdgcn_readfirstlane(lds_ld(lds, SY_PUB)), allf = __builtin_amdgcn_readfirstlane(lds_ld(lds, SY_ALLF));
                const u32 pubt = __builtin_amdgcn_readfirstlane(lds_ld(lds, SY_PUBT));
                if (pub >= ready && allf && now_ticks() - pubt >= C_DELAY) { cg = __builtin_amdgcn_raw_buffer_load_b128(rs, cvoff, csoff, 16); cissued = true; }
                SCHED_FENCE();
            }
#undef MF1
#undef MF4
#undef MFA
            __builtin_amdgcn_s_setprio(0);
            if (k == 0 && mytile >= KLOAD_SPLIT) {
                lds_wait_ge_slow(lds, SY_KREADY, 2);
                load_afrags(A, lds, L_KB, ch, h, l);
            }
        } else if (w >= 6) {
            const int ch = l & 1, tl = 32 * (l >> 3) + 8 * ((l >> 1) & 3);
            const float dco = *(const float*)(lds + L_DCO + (k == 0 ? 0 : 8) + ch * 4);
            __builtin_amdgcn_s_setprio(3);
#pragma unroll 1
            for (int c = w & 1; c < 8; c += 2) {
                if (c >= 6) {
                    const u32 pub = __builtin_amdgcn_readfirstlane(lds_ld(lds, SY_PUB)), allf = __builtin_amdgcn_readfirstlane(lds_ld(lds, SY_ALLF));
                    const u32 pubt = __builtin_amdgcn_readfirstlane(lds_ld(lds, SY_PUBT));
                    const u32 in6 = __builtin_amdgcn_readfirstlane(lds_ld(lds, SY_INTILE + 24)), in7 = __builtin_amdgcn_readfirstlane(lds_ld(lds, SY_INTILE + 28));
                    if (pub >= (u32)(k + 1) && allf && in6 >= (u32)(k + 1) && in7 >= (u32)(k + 1) && now_ticks() - pubt >= C_DELAY) { cg = __builtin_amdgcn_raw_buffer_load_b128(rs, cvoff, csoff, 16); cissued = true; }
                }
                const int sb = (l >> 1) & 3, pa = 2 * (l >> 3) + (sb >> 1);
                const unsigned char* pp2 = lds + L_PART + c * PART_TILE + ch * PART_CH + 16 * (32 * (sb & 1) + ((pa + 8 * (sb & 1)) & 15));
                v4f a0, a1, b0, b1;
                v4u u;
                for (u32 it = 0;; ++it) {
                    asm volatile("" ::: "memory");
                    const v4u f = *(const v4u*)(lds + SY_PART + 16 * c);
                    asm volatile("" ::: "memory");
                    a0 = *(const v4f*)pp2; a1 = *(const v4f*)(pp2 + 256);
                    b0 = *(const v4f*)(pp2 + 1024); b1 = *(const v4f*)(pp2 + 1280);
                    u = *(const v4u*)(lds + L_UR + ch * UR_PITCH + 2 * (2040 - (256 * c + tl)));
                    const u32 fa = f.x < f.y ? f.x : f.y, fb = f.z < f.w ? f.z : f.w;
                    if ((u32)__builtin_amdgcn_readfirstlane(fa < fb ? fa : fb) >= (u32)(k + 1)) break;
                    if (spin_give_up(lds, it, LDS_SPIN_CAP)) break;
                }
                v4u o;
                o.x = no_poison(pack_bf(vfma(dco, bf_hi(u.w), vadd(a0.x, b0.x)), vfma(dco, bf_lo(u.w), vadd(a0.y, b0.y))));
                o.y = no_poison(pack_bf(vfma(dco, bf_hi(u.z), vadd(a0.z, b0.z)), vfma(dco, bf_lo(u.z), vadd(a0.w, b0.w))));
                o.z = no_poison(pack_bf(vfma(dco, bf_hi(u.y), vadd(a1.x, b1.x)), vfma(dco, bf_lo(u.y), vadd(a1.y, b1.y))));
                o.w = no_poison(pack_bf(vfma(dco, bf_hi(u.x), vadd(a1.z, b1.z)), vfma(dco, bf_lo(u.x), vadd(a1.w, b1.w))));
                store16(o, rs, (8 * c + (l >> 3)) * C_PITCH + me * 128 + (l & 7) * 16, WS_C + k * C_SIZE);
                if (c == mytile) { lds_flag_set(SY_PUBT, now_ticks()); lds_flag_set(SY_PUB, (u32)(k + 1)); }
                if (c < 2) STAMP(8 + 8 * k);
                if (c >= 6) STAMP(9 + 8 * k);
            }
            __builtin_amdgcn_s_setprio(2);
        }

        asm volatile("" : "+v"(i));
        STAMP(10 + 8 * k);
        float x[8], y[8], dm, rsd;
        u32 n0[8];
        float piv;
        v4u yap = {0, 0, 0, 0};
        if (!cissued) {
            if (k == 0) lds_wait_ge(lds, SY_ALLF, 1u);
            lds_wait_ge(lds, SY_INTILE + 24, (u32)(k + 1));
            lds_wait_ge(lds, SY_INTILE + 28, (u32)(k + 1));
            lds_wait_ge(lds, SY_PUB, (u32)(k + 1));
            wait_until(__builtin_amdgcn_readfirstlane(lds_ld(lds, SY_PUBT)), C_DELAY);
            cg = __builtin_amdgcn_raw_buffer_load_b128(rs, cvoff, csoff, 16);
        }
        STAMP(11 + 8 * k);
        SCHED_FENCE();
        if (k == 0) {
            store16(poison4, rs, (i >> 3) * C_PITCH + me * 128 + (i & 7) * 16, WS_C + C_SIZE);
            store16(poison4, rs, myr * U_PITCH + i * 16, WS_U + U_SIZE);
            lds_wait_ge(lds, SY_X0, 2);
#pragma unroll
            for (int t = 0; t < 8; ++t) {
                const u32 xn = *(const u32*)(lds + L_XN + t * 2048 + i * 4);
                x[t] = bf_lo(xn);
                n0[t] = (t & 1) ? (xn & 0xFFFF0000u) : (xn >> 16);
            }
            piv = *(const float*)(lds + L_PIV + (l & 7) * 4);
        } else {
#pragma unroll
            for (int t = 0; t < 8; ++t) n0[t] = (t & 1) ? 0x3F800000u : 0x00003F80u;
            if (k == 1) store16(poison4, rs, (i >> 3) * C_PITCH + me * 128 + (i & 7) * 16, WS_C + 2 * C_SIZE);
            const v4f xa = *(const v4f*)(lds + L_PARK + i * 16), xb = *(const v4f*)(lds + L_PARK + 8192 + i * 16);
            x[0] = xa.x; x[1] = xa.y; x[2] = xa.z; x[3] = xa.w; x[4] = xb.x; x[5] = xb.y; x[6] = xb.z; x[7] = xb.w;
            yap = *(const v4u*)(lds + L_PARK + 16384 + i * 16);
            piv = *(const float*)(lds + L_PARK + 24576 + i * 4);
        }
        v4u sel_lo, sel_hi;
        {
            const u32 one = 0x3F80u << (16 * (l & 1)), dsel = (l & 7) >> 1;
            const v4u pat = {dsel == 0 ? one : 0u, dsel == 1 ? one : 0u, dsel == 2 ? one : 0u, dsel == 3 ? one : 0u};
            const v4u zero = {0, 0, 0, 0};
            sel_lo = (l & 8) ? zero : pat;
            sel_hi = (l & 8) ? pat : zero;
        }
        u32 red_rd0 = L_RED + (l & 15) * 32, red_wr0 = red_rd0 + w * 4, red_rd1 = red_rd0 + 512, red_wr1 = red_wr0 + 512;
        float ya[8] = {bf_lo(yap.x), bf_hi(yap.x), bf_lo(yap.y), bf_hi(yap.y), bf_lo(yap.z), bf_hi(yap.z), bf_lo(yap.w), bf_hi(yap.w)};
        asm volatile("" : "+v"(sel_lo), "+v"(sel_hi), "+v"(red_rd0), "+v"(red_wr0), "+v"(red_rd1), "+v"(red_wr1), "+v"(piv));
        asm volatile("" : "+v"(ya[0]), "+v"(ya[1]), "+v"(ya[2]), "+v"(ya[3]), "+v"(ya[4]), "+v"(ya[5]), "+v"(ya[6]), "+v"(ya[7]));
        asm volatile("" : "+v"(x[0]), "+v"(x[1]), "+v"(x[2]), "+v"(x[3]), "+v"(x[4]), "+v"(x[5]), "+v"(x[6]), "+v"(x[7]));
        asm volatile("" : "+v"(n0[0]), "+v"(n0[1]), "+v"(n0[2]), "+v"(n0[3]), "+v"(n0[4]), "+v"(n0[5]), "+v"(n0[6]), "+v"(n0[7]));
        SCHED_FENCE();
        {
            bool ok = valid16(cg);
            for (u32 it = 0; !__all(ok); ++it) {
                if (spin_give_up(lds, it * 4 + 3, GLB_SPIN_CAP * 4)) break;
                asm volatile("" ::: "memory");
                if (!ok) { cg = __builtin_amdgcn_raw_buffer_load_b128(rs, cvoff, csoff, 16); ok = valid16(cg); }
            }
        }
        STAMP(12 + 8 * k);
        const u32 cgp[4] = {cg.x, cg.y, cg.z, cg.w};
        dot_rows(x, cgp, n0);
        if (k == 2) {
#pragma unroll
            for (int t = 0; t < 8; t += 2) {
                const u32 o = pack_bf(x[t], x[t + 1]);
                __builtin_amdgcn_raw_buffer_store_b16((u16)o, ro, i * 2 + t * (D_ * 2), myr * (8 * D_ * 2), OUT_AUX);
                __builtin_amdgcn_raw_buffer_store_b16((u16)(o >> 16), ro, i * 2 + (t + 1) * (D_ * 2), myr * (8 * D_ * 2), OUT_AUX);
            }
            break;
        }
        if (k == 0) {
            yap = cg;
        } else {
            sub_rows(y, x, piv);
            row_stats(y, sel_lo, sel_hi, lds, red_rd0, red_wr0, dm, rsd, [] {});
            norm_rows(y, rsd, dm * rsd);
            fma_rows(x, y, ya);
            piv += dm;
        }
        sub_rows(y, x, piv);
        WAIT_VM0();
        auto tail = [&](auto npre_c) {
            constexpr int NPRE = decltype(npre_c)::value;
            const u32 gsoff = WS_U + k * U_SIZE;
            const u32 gvoff = (32 * (w & 1) + (l >> 1)) * U_PITCH + (2 * me + (l & 1)) * 16;
            v4u d[4];
            row_stats(y, sel_lo, sel_hi, lds, red_rd1, red_wr1, dm, rsd, [&] {
                if (NPRE >= 1) d[0] = __builtin_amdgcn_raw_buffer_load_b128(rs, gvoff, gsoff, 16);
                SCHED_FENCE();
            });
            norm_rows(y, rsd, dm * rsd);
            v4u uo;
            uo.x = no_poison(pack_bf(y[7], y[6]));
            uo.y = no_poison(pack_bf(y[5], y[4]));
            uo.z = no_poison(pack_bf(y[3], y[2]));
            uo.w = no_poison(pack_bf(y[1], y[0]));
            SCHED_FENCE();
#pragma unroll
            for (int t = 1; t < NPRE; ++t) d[t] = __builtin_amdgcn_raw_buffer_load_b128(rs, gvoff + t * 64 * U_PITCH, gsoff, 16);
            SCHED_FENCE();
            store16(uo, rs, myr * U_PITCH + i * 16, WS_U + k * U_SIZE);
            const u32 ustore_t = NPRE >= 0 ? now_ticks() : 0u;
            piv += dm;
            *(v4f*)(lds + L_PARK + i * 16) = (v4f){x[0], x[1], x[2], x[3]};
            *(v4f*)(lds + L_PARK + 8192 + i * 16) = (v4f){x[4], x[5], x[6], x[7]};
            *(v4u*)(lds + L_PARK + 16384 + i * 16) = yap;
            *(float*)(lds + L_PARK + 24576 + i * 4) = piv;
            STAMP(13 + 8 * k);
            if (NPRE >= 0) {
                __builtin_amdgcn_s_setprio(3);
                unsigned char* dst = lds + L_UR + (l & 1) * UR_PITCH + 2 * (2040 - 8 * (32 * (w & 1) + (l >> 1)));
#pragma unroll
                for (int t = 0; t < 4; ++t) {
                    const int q = 2 * t + (w & 1);
                    if (t == NPRE) {
                        wait_until(ustore_t, U_DELAY);
#pragma unroll
                        for (int u = t; u < 4; ++u) {
                            d[u] = poison4;
                            if (2 * u + (w & 1) <= mytile + GATHER_AHEAD) d[u] = __builtin_amdgcn_raw_buffer_load_b128(rs, gvoff + u * 64 * U_PITCH, gsoff, 16);
                        }
                    }
                    if (__builtin_expect(!__all(valid16(d[t])), 0)) {
                        for (u32 it = 0; !__all(valid16(d[t])); ++it) {
                            if (spin_give_up(lds, it * 4 + 3, GLB_SPIN_CAP * 4)) break;
                            asm volatile("" ::: "memory");
                            if (!valid16(d[t])) d[t] = __builtin_amdgcn_raw_buffer_load_b128(rs, gvoff + t * 64 * U_PITCH, gsoff, 16);
                        }
                    }
                    *(v4u*)(dst - t * 1024) = d[t];
                    lds_flag_set(SY_INTILE + 4 * q, (u32)(k + 2));
                    if (t == 0) STAMP(16 + 8 * k);
                    if (t == 3) STAMP(17 + 8 * k);
                }
                WAIT_VM0();
                __builtin_amdgcn_s_setprio(2);
            }
        };
        if (w == 4 || w == 5) {
            if (mytile >= 6) tail(std::integral_constant<int, 3>());
            else if (mytile >= 4) tail(std::integral_constant<int, 2>());
            else if (mytile >= 2) tail(std::integral_constant<int, 1>());
            else tail(std::integral_constant<int, 0>());
        } else {
            tail(std::integral_constant<int, -1>());
        }
    }
    asm volatile(".Lh3s4_end:");
}

extern "C" void kernel_launch(void* const* d_in, const int* in_sizes, int n_in, void* d_out, int out_size, void* d_ws, size_t ws_size,
                              hipStream_t stream) {
    (void)in_sizes; (void)n_in; (void)out_size; (void)ws_size;
    h3s4_kernel<<<256, 512, 0, stream>>>(
        (const u16*)d_in[0], (const u16*)d_in[1], (const u16*)d_in[2], (const u16*)d_in[3], (const u16*)d_in[4], (const u16*)d_in[5],
        (const u16*)d_in[7], (const u16*)d_in[8], (const u16*)d_in[9], (const u16*)d_in[10], (const u16*)d_in[11], (const u16*)d_in[12],
        (const u16*)d_in[13], (const u16*)d_in[14], (u16*)d_out, (unsigned char*)d_ws);
}
```

```cpp
#include <hip/hip_runtime.h>
#include <stdint.h>
#include <type_traits>

typedef unsigned short u16;
typedef unsigned u32;
typedef u32 v4u __attribute__((ext_vector_type(4)));
typedef float v2f __attribute__((ext_vector_type(2)));
typedef float v4f __attribute__((ext_vector_type(4)));
typedef __bf16 v2bf __attribute__((ext_vector_type(2)));
typedef __bf16 v8bf __attribute__((ext_vector_type(8)));
typedef _Float16 v2h __attribute__((ext_vector_type(2)));
typedef _Float16 v8h __attribute__((ext_vector_type(8)));

constexpr int S_ = 2048, D_ = 512, NS_ = 128;
constexpr u32 POISON = 0xFFFFFFFFu;

constexpr u32 WS_EPOCH = 0;
constexpr u32 WS_FLAGS = 4096;
constexpr u32 KV_PITCH = 4096 + 128;
constexpr u32 WS_FLAGP = WS_FLAGS + 256 * 128;
constexpr u32 WS_KV = WS_FLAGP + 256 * 128 + 4096;
constexpr u32 C_PITCH = 32768 + 128;
constexpr u32 C_SIZE = 64 * C_PITCH;
constexpr u32 WS_C = WS_KV + 512 * KV_PITCH;
constexpr u32 U_PITCH = 8192 + 128;
constexpr u32 U_SIZE = 256 * U_PITCH;
constexpr u32 WS_U = WS_C + 3 * C_SIZE;
constexpr u32 WS_TOTAL = WS_U + 2 * U_SIZE;

constexpr int UR_PITCH = 4608;
constexpr int L_UR = 0;
constexpr int PART_CH = 2048 + 64;
constexpr int PART_TILE = 2 * PART_CH;
constexpr int L_PART = L_UR + 2 * UR_PITCH;
constexpr int KC_COPY = 4160;
constexpr int KC_CH = 2 * KC_COPY;
constexpr int L_KA = L_PART + 8 * PART_TILE;
constexpr int L_KB = L_KA + 2 * KC_CH;
constexpr int KT_CH = 648 * 4;
constexpr int L_KT = L_KB + 2 * KC_CH;
constexpr int TAB_ROW = 528;
constexpr int TAB_CH = 57 * TAB_ROW;
constexpr int L_TAB = L_KT + 2 * KT_CH;
constexpr int L_T = L_TAB + 2 * TAB_CH;
constexpr int L_XN = L_T;
constexpr int L_PARK = L_TAB;
constexpr int L_RED = L_T + 16384;
constexpr int L_SYNC = L_RED + 2 * 512;
constexpr int SY_PART = L_SYNC;
constexpr int SY_INTILE = L_SYNC + 128;
constexpr int SY_KV = L_SYNC + 160;
constexpr int SY_GO = L_SYNC + 164;
constexpr int SY_TAB = L_SYNC + 168;

constexpr int SY_KREADY = L_SYNC + 184;
constexpr int SY_ABORT = L_SYNC + 188;
constexpr int SY_TAG = L_SYNC + 192;
constexpr int SY_ALLF = L_SYNC + 196;
constexpr int SY_PUB = L_SYNC + 200;
constexpr int SY_X0 = L_SYNC + 204;
constexpr int SY_KA = L_SYNC + 176;
constexpr int SY_TAGSET = L_SYNC + 180;
constexpr int SY_POIS = L_SYNC + 164 + 44;
constexpr int SY_PUBT = L_SYNC + 212;
constexpr int L_DCO = L_SYNC + 224;
constexpr int L_PIV = L_DCO + 32;
constexpr int LDS_BYTES = L_PIV + 32;
static_assert(LDS_BYTES > 82 * 1024 && LDS_BYTES <= 160 * 1024, "one workgroup per CU");

#define GATHER_AHEAD 1
#define ALLF_TILE 1
#define KV_DELAY 0
#define KV_BACKOFF 0
#define YA_POISON_AT 1
#define ALLF_DELAY 30
#define C_DELAY 65
#define U_DELAY 55
#define PUB_EXTRA 0
#define GATH_EXTRA 0
#define POLL_BACKOFF 0
#define PUB_SLEEP 0
#define GATH_SLEEP 0
#define KLOAD_SPLIT 5
#define GATHER_LAG 2
#define GATHER_LOOK 0
#define OUT_AUX 16
#define CODE_PREFETCH 1
constexpr u32 LDS_SPIN_CAP = 1u << 19;
constexpr u32 GLB_SPIN_CAP = 1u << 16;

#define STAMP(n) do {} while (0)
#define STAMPC(n) do {} while (0)
#define SCHED_FENCE() __builtin_amdgcn_sched_barrier(0)
#define WAIT_VM0() do { asm volatile("" ::: "memory"); __builtin_amdgcn_s_waitcnt(0x0F70); asm volatile("" ::: "memory"); } while (0)

__device__ __forceinline__ float bf_lo(u32 x) { return __builtin_bit_cast(float, x << 16); }
__device__ __forceinline__ float bf_hi(u32 x) { return __builtin_bit_cast(float, x & 0xFFFF0000u); }
__device__ __forceinline__ float bf2f(u16 x) { return __builtin_bit_cast(float, ((u32)x) << 16); }
__device__ __forceinline__ u32 pack_bf(float lo, float hi) {
    v2f f = {lo, hi};
    return __builtin_bit_cast(u32, __builtin_convertvector(f, v2bf));
}
__device__ __forceinline__ u32 pack_h(v2f f) { return __builtin_bit_cast(u32, __builtin_convertvector(f, v2h)); }
__device__ __forceinline__ u32 no_poison(u32 x) { return x < 0xFFFFFFFEu ? x : 0xFFFFFFFEu; }
template <int CTRL> __device__ __forceinline__ float dppf(float x) {
    return __builtin_bit_cast(float, __builtin_amdgcn_mov_dpp(__builtin_bit_cast(int, x), CTRL, 0xf, 0xf, true));
}
template <int CTRL> __device__ __forceinline__ u32 dppu(u32 x) { return (u32)__builtin_amdgcn_mov_dpp((int)x, CTRL, 0xf, 0xf, true); }
__device__ __forceinline__ float f_of(u32 x) { return __builtin_bit_cast(float, x); }
__device__ __forceinline__ u32 u_of(float x) { return __builtin_bit_cast(u32, x); }

__device__ __forceinline__ u32 lds_ld(unsigned char* lds, int off) {
    return __hip_atomic_load((u32*)(lds + off), __ATOMIC_RELAXED, __HIP_MEMORY_SCOPE_WORKGROUP);
}
__device__ __forceinline__ void lds_st(unsigned char* lds, int off, u32 v) {
    __hip_atomic_store((u32*)(lds + off), v, __ATOMIC_RELAXED, __HIP_MEMORY_SCOPE_WORKGROUP);
}
__device__ __forceinline__ void lds_flag_set(int off, u32 v) {
    asm volatile("s_mov_b64 exec, 1\n\tds_write_b32 %0, %1\n\ts_mov_b64 exec, -1" ::"v"(off), "v"(v) : "memory");
}
template <int Q> __device__ __forceinline__ void put_partial(u32 vpart, const v4f& acc) {
    asm volatile("ds_write_b128 %0, %1 offset:%c2" ::"v"(vpart), "v"(acc), "i"(Q * PART_TILE) : "memory");
}
template <int Q> __device__ __forceinline__ void put_partial_flag(u32 vflag, u32 v) {
    asm volatile("ds_write_b32 %0, %1 offset:%c2" ::"v"(vflag), "v"(v), "i"(Q * 16) : "memory");
}
__device__ __forceinline__ void lds_flag_inc(int off) {
    asm volatile("s_mov_b64 exec, 1\n\tds_add_u32 %0, %1\n\ts_mov_b64 exec, -1" ::"v"(off), "v"(1u) : "memory");
}
__device__ __forceinline__ bool spin_give_up(unsigned char* lds, u32 it, u32 cap) {
    if ((it & 63u) != 63u) return false;
    if (it > cap) { lds_st(lds, SY_ABORT, 1u); return true; }
    return __builtin_amdgcn_readfirstlane(lds_ld(lds, SY_ABORT)) != 0;
}
__device__ __forceinline__ void lds_wait_ge(unsigned char* lds, int off, u32 target) {
    if (__builtin_expect((u32)__builtin_amdgcn_readfirstlane(lds_ld(lds, off)) < target, 0)) {
        for (u32 it = 0; (u32)__builtin_amdgcn_readfirstlane(lds_ld(lds, off)) < target; ++it)
            if (spin_give_up(lds, it, LDS_SPIN_CAP)) break;
    }
    asm volatile("" ::: "memory");
}
__device__ __forceinline__ void lds_wait_ge_slow(unsigned char* lds, int off, u32 target) {
    for (u32 it = 0; (u32)__builtin_amdgcn_readfirstlane(lds_ld(lds, off)) < target; ++it) {
        if (spin_give_up(lds, it, LDS_SPIN_CAP)) break;
        __builtin_amdgcn_s_sleep(1);
    }
    asm volatile("" ::: "memory");
}
__device__ __forceinline__ void lds_wait4_ge(unsigned char* lds, int off, u32 target) {
    for (u32 it = 0;; ++it) {
        asm volatile("" ::: "memory");
        const v4u f = *(const v4u*)(lds + off);
        const u32 a = f.x < f.y ? f.x : f.y, b = f.z < f.w ? f.z : f.w;
        if ((u32)__builtin_amdgcn_readfirstlane(a < b ? a : b) >= target) break;
        if (spin_give_up(lds, it, LDS_SPIN_CAP)) break;
    }
    asm volatile("" ::: "memory");
}
__device__ __forceinline__ void store16(v4u v, __amdgpu_buffer_rsrc_t rs, u32 voff, u32 soff) {
    __builtin_amdgcn_raw_buffer_store_b128(v, rs, voff + soff, 0, 16);
}
__device__ __forceinline__ u32 now_ticks() { return (u32)__builtin_amdgcn_s_memrealtime(); }
__device__ __forceinline__ void wait_until(u32 t0, u32 delay) {
    for (u32 it = 0; now_ticks() - t0 < delay && it < 4096; ++it) __builtin_amdgcn_s_sleep(1);
}
__device__ __forceinline__ bool valid16(v4u r) {
    u32 m = r.x > r.y ? r.x : r.y;
    m = m > r.z ? m : r.z;
    m = m > r.w ? m : r.w;
    return m != POISON;
}
__device__ __forceinline__ v4u poll16(__amdgpu_buffer_rsrc_t rs, u32 voff, u32 soff, unsigned char* lds) {
    v4u r = __builtin_amdgcn_raw_buffer_load_b128(rs, voff, soff, 16);
    bool ok = valid16(r);
    for (u32 it = 0; !__all(ok); ++it) {
        if (spin_give_up(lds, it * 4 + 3, GLB_SPIN_CAP * 4)) break;
        asm volatile("" ::: "memory");
        if (!ok) { r = __builtin_amdgcn_raw_buffer_load_b128(rs, voff, soff, 16); ok = valid16(r); }
    }
    return r;
}

__device__ __forceinline__ float vmul(float a, float b) {
    float r;
    asm("v_mul_f32 %0, %1, %2" : "=v"(r) : "v"(a), "v"(b));
    return r;
}
#define DPPB(t) " row_newbcast:" #t " row_mask:0xf bank_mask:0xf\n\t"
__device__ __forceinline__ void sub_rows(float (&y)[8], const float (&x)[8], float p) {
    asm("s_nop 1\n\t"
        "v_subrev_f32_dpp %0, %16, %8" DPPB(0) "v_subrev_f32_dpp %1, %16, %9" DPPB(1) "v_subrev_f32_dpp %2, %16, %10" DPPB(2)
        "v_subrev_f32_dpp %3, %16, %11" DPPB(3) "v_subrev_f32_dpp %4, %16, %12" DPPB(4) "v_subrev_f32_dpp %5, %16, %13" DPPB(5)
        "v_subrev_f32_dpp %6, %16, %14" DPPB(6) "v_subrev_f32_dpp %7, %16, %15" DPPB(7)
        : "=&v"(y[0]), "=&v"(y[1]), "=&v"(y[2]), "=&v"(y[3]), "=&v"(y[4]), "=&v"(y[5]), "=&v"(y[6]), "=&v"(y[7])
        : "v"(x[0]), "v"(x[1]), "v"(x[2]), "v"(x[3]), "v"(x[4]), "v"(x[5]), "v"(x[6]), "v"(x[7]), "v"(p));
}
__device__ __forceinline__ void norm_rows(float (&y)[8], float r, float m) {
    asm("s_nop 1\n\t"
        "v_mul_f32_dpp %0, %8, %0" DPPB(0) "v_mul_f32_dpp %1, %8, %1" DPPB(1) "v_mul_f32_dpp %2, %8, %2" DPPB(2) "v_mul_f32_dpp %3, %8, %3" DPPB(3)
        "v_mul_f32_dpp %4, %8, %4" DPPB(4) "v_mul_f32_dpp %5, %8, %5" DPPB(5) "v_mul_f32_dpp %6, %8, %6" DPPB(6) "v_mul_f32_dpp %7, %8, %7" DPPB(7)
        "v_subrev_f32_dpp %0, %9, %0" DPPB(0) "v_subrev_f32_dpp %1, %9, %1" DPPB(1) "v_subrev_f32_dpp %2, %9, %2" DPPB(2)
        "v_subrev_f32_dpp %3, %9, %3" DPPB(3) "v_subrev_f32_dpp %4, %9, %4" DPPB(4) "v_subrev_f32_dpp %5, %9, %5" DPPB(5)
        "v_subrev_f32_dpp %6, %9, %6" DPPB(6) "v_subrev_f32_dpp %7, %9, %7" DPPB(7)
        : "+v"(y[0]), "+v"(y[1]), "+v"(y[2]), "+v"(y[3]), "+v"(y[4]), "+v"(y[5]), "+v"(y[6]), "+v"(y[7])
        : "v"(r), "v"(m));
}
#undef DPPB
__device__ __forceinline__ void dot_rows(float (&x)[8], const u32 (&p)[4], const u32 (&m)[8]) {
    asm("v_dot2_f32_bf16 %0, %8, %12, %0\n\tv_dot2_f32_bf16 %1, %8, %13, %1\n\t"
        "v_dot2_f32_bf16 %2, %9, %14, %2\n\tv_dot2_f32_bf16 %3, %9, %15, %3\n\t"
        "v_dot2_f32_bf16 %4, %10, %16, %4\n\tv_dot2_f32_bf16 %5, %10, %17, %5\n\t"
        "v_dot2_f32_bf16 %6, %11, %18, %6\n\tv_dot2_f32_bf16 %7, %11, %19, %7\n\ts_nop 2"
        : "+v"(x[0]), "+v"(x[1]), "+v"(x[2]), "+v"(x[3]), "+v"(x[4]), "+v"(x[5]), "+v"(x[6]), "+v"(x[7])
        : "v"(p[0]), "v"(p[1]), "v"(p[2]), "v"(p[3]), "v"(m[0]), "v"(m[1]), "v"(m[2]), "v"(m[3]), "v"(m[4]), "v"(m[5]), "v"(m[6]), "v"(m[7]));
}
__device__ __forceinline__ void fma_rows(float (&x)[8], const float (&a)[8], const float (&b)[8]) {
    asm("v_fmac_f32 %0, %8, %16\n\tv_fmac_f32 %1, %9, %17\n\tv_fmac_f32 %2, %10, %18\n\tv_fmac_f32 %3, %11, %19\n\t"
        "v_fmac_f32 %4, %12, %20\n\tv_fmac_f32 %5, %13, %21\n\tv_fmac_f32 %6, %14, %22\n\tv_fmac_f32 %7, %15, %23"
        : "+v"(x[0]), "+v"(x[1]), "+v"(x[2]), "+v"(x[3]), "+v"(x[4]), "+v"(x[5]), "+v"(x[6]), "+v"(x[7])
        : "v"(a[0]), "v"(a[1]), "v"(a[2]), "v"(a[3]), "v"(a[4]), "v"(a[5]), "v"(a[6]), "v"(a[7]),
          "v"(b[0]), "v"(b[1]), "v"(b[2]), "v"(b[3]), "v"(b[4]), "v"(b[5]), "v"(b[6]), "v"(b[7]));
}
__device__ __forceinline__ float vfma(float a, float b, float c) {
    float r;
    asm("v_fma_f32 %0, %1, %2, %3" : "=v"(r) : "v"(a), "v"(b), "v"(c));
    return r;
}
__device__ __forceinline__ float vadd(float a, float b) {
    float r;
    asm("v_add_f32 %0, %1, %2" : "=v"(r) : "v"(a), "v"(b));
    return r;
}

template <class Hook>
__device__ __forceinline__ void row_stats(const float (&y)[8], const v4u& sel_lo, const v4u& sel_hi, unsigned char* lds, u32 red_rd, u32 red_wr,
                                          float& dmean, float& rstd, Hook after_barrier) {
    const v4u a = {pack_bf(y[0], y[1]), pack_bf(y[2], y[3]), pack_bf(y[4], y[5]), pack_bf(y[6], y[7])};
    float q[8];
    asm("v_mul_f32 %0, %8, %8\n\tv_mul_f32 %1, %9, %9\n\tv_mul_f32 %2, %10, %10\n\tv_mul_f32 %3, %11, %11\n\t"
        "v_mul_f32 %4, %12, %12\n\tv_mul_f32 %5, %13, %13\n\tv_mul_f32 %6, %14, %14\n\tv_mul_f32 %7, %15, %15"
        : "=&v"(q[0]), "=&v"(q[1]), "=&v"(q[2]), "=&v"(q[3]), "=&v"(q[4]), "=&v"(q[5]), "=&v"(q[6]), "=&v"(q[7])
        : "v"(y[0]), "v"(y[1]), "v"(y[2]), "v"(y[3]), "v"(y[4]), "v"(y[5]), "v"(y[6]), "v"(y[7]));
    const v4u s = {pack_bf(q[0], q[1]), pack_bf(q[2], q[3]), pack_bf(q[4], q[5]), pack_bf(q[6], q[7])};
    v4f d = {0, 0, 0, 0};
    d = __builtin_amdgcn_mfma_f32_16x16x32_bf16(__builtin_bit_cast(v8bf, a), __builtin_bit_cast(v8bf, sel_lo), d, 0, 0, 0);
    d = __builtin_amdgcn_mfma_f32_16x16x32_bf16(__builtin_bit_cast(v8bf, s), __builtin_bit_cast(v8bf, sel_hi), d, 0, 0, 0);
    u32 t0 = u_of((d.x + d.z) + (d.y + d.w)), t1 = t0;
    {
        auto r = __builtin_amdgcn_permlane16_swap(t0, t1, false, false);
        const u32 r0 = r[0], r1 = r[1];
        t0 = u_of(f_of(r0) + f_of(r1)); t1 = t0;
    }
    {
        auto r = __builtin_amdgcn_permlane32_swap(t0, t1, false, false);
        const u32 r0 = r[0], r1 = r[1];
        t0 = u_of(f_of(r0) + f_of(r1));
    }
    *(u32*)(lds + red_wr) = t0;
    __syncthreads();
    after_barrier();
    const v4f p0 = *(const v4f*)(lds + red_rd), p1 = *(const v4f*)(lds + red_rd + 16);
    const v4f pp = p0 + p1;
    const float tot = (pp.x + pp.z) + (pp.y + pp.w);
    const float s2 = dppf<0x128>(tot);
    dmean = tot * (1.0f / D_);
    const float var = fmaxf(s2 * (1.0f / D_) - dmean * dmean, 0.0f);
    rstd = __builtin_amdgcn_rsqf(var + 1e-5f);
}
__device__ __forceinline__ float wave_sum(float v) {
    asm("s_nop 1" : "+v"(v));
    v += dppf<0x128>(v);
    v += dppf<0x141>(v);
    v += dppf<0xB1>(v);
    v += dppf<0x4E>(v);
    u32 a = u_of(v), b = a;
    {
        auto r = __builtin_amdgcn_permlane16_swap(a, b, false, false);
        const u32 r0 = r[0], r1 = r[1];
        a = u_of(f_of(r0) + f_of(r1)); b = a;
    }
    auto r = __builtin_amdgcn_permlane32_swap(a, b, false, false);
    const u32 r0 = r[0], r1 = r[1];
    return f_of(r0) + f_of(r1);
}

__device__ __forceinline__ void load_afrags(v4u (&A)[8][4], unsigned char* lds, int region, int ch, int h, int l) {
    const int b = l & 15, g = l >> 4, par = (1 + b) & 1;
    const unsigned char* base = lds + region + ch * KC_CH + par * KC_COPY + 256 * h + 16 * g + 2 * (1 + b - par);
#pragma unroll
    for (int p = 0; p < 8; ++p)
#pragma unroll
        for (int r = 0; r < 4; ++r) {
            const u32* q = (const u32*)(base + 512 * p + 64 * r);
            A[p][r] = (v4u){q[0], q[1], q[2], q[3]};
        }
}

__device__ __forceinline__ v2f cexp_pow(float al, float be, float m) {
    const float e = __builtin_amdgcn_exp2f(al * m);
    const float ph = __builtin_amdgcn_fractf(be * m);
    return (v2f){e * __builtin_amdgcn_cosf(ph), e * __builtin_amdgcn_sinf(ph)};
}
__device__ __forceinline__ v2f cmul(v2f a, v2f b) {
    v2f t;
    asm("v_pk_mul_f32 %0, %1, %2 op_sel:[0,0] op_sel_hi:[1,0]\n\t"
        "v_pk_fma_f32 %0, %1, %2, %0 op_sel:[1,1,0] op_sel_hi:[0,1,1] neg_lo:[1,0,0]"
        : "=&v"(t) : "v"(a), "v"(b));
    return t;
}
__device__ __forceinline__ v4f mfma_bf(v4u a, v4u b, v4f c) {
    return __builtin_amdgcn_mfma_f32_16x16x32_bf16(__builtin_bit_cast(v8bf, a), __builtin_bit_cast(v8bf, b), c, 0, 0, 0);
}

extern "C" __global__ void __launch_bounds__(512, 2)
h3s4_kernel(const u16* __restrict__ q_, const u16* __restrict__ k_, const u16* __restrict__ v_, const u16* __restrict__ shk_,
            const u16* __restrict__ ssmk_, const u16* __restrict__ ssmD_, const u16* __restrict__ Are_, const u16* __restrict__ Aim_,
            const u16* __restrict__ Bre_, const u16* __restrict__ Bim_, const u16* __restrict__ Cre_, const u16* __restrict__ Cim_,
            const u16* __restrict__ logdt_, const u16* __restrict__ Dskip_, u16* __restrict__ out_, unsigned char* ws_) {
    __shared__ __attribute__((aligned(16))) unsigned char lds[LDS_BYTES];
    asm volatile("" ::"s"(q_), "s"(k_), "s"(v_), "s"(shk_), "s"(ssmk_), "s"(ssmD_), "s"(Are_), "s"(Aim_), "s"(Bre_), "s"(Bim_),
                 "s"(Cre_), "s"(Cim_), "s"(logdt_), "s"(Dskip_), "s"(out_), "s"(ws_));
    const int i = threadIdx.x, l = i & 63;
    const int w = __builtin_amdgcn_readfirstlane(i >> 6);
    u32 code_touch = 0;
    if (w == 0) {
        unsigned long long pc;
        u32 left;
        asm volatile("s_getpc_b64 %0\n.Lh3s4_pc:\n\ts_mov_b32 %1, .Lh3s4_end-.Lh3s4_pc+1536" : "=s"(pc), "=s"(left));
        const u32 nl = (left >> 12) + 1, off = ((blockIdx.x >> 3) * nl + l) * 128;
        if ((u32)l < nl && off + 4 <= left) code_touch = *(const __attribute__((address_space(1))) u32*)((pc & ~3ull) + off);
    }
    const int xcd = blockIdx.x & 7, idx = blockIdx.x >> 3;
    const int me = xcd * 32 + idx;
    const int mytile = idx >> 2;
    const int myr = 32 * mytile + 4 * xcd + (idx & 3);
    const __amdgpu_buffer_rsrc_t rs = __builtin_amdgcn_make_buffer_rsrc(ws_, 0, WS_TOTAL, 0x00020000);
    const v4u poison4 = {POISON, POISON, POISON, POISON};
    STAMP(0);

    const int cp = i & 31, rb = i >> 5;
    const int pcg = (xcd + idx) & 7;
    const int R0 = 64 * idx + 4 * rb, pch = 64 * pcg + 2 * cp;
    u32 kd[7], vd[4];
#pragma unroll
    for (int j = 0; j < 7; ++j) {
        const int Rj = R0 - 3 + j;
        kd[j] = *(const u32*)(k_ + (Rj < 0 ? 0 : Rj) * D_ + pch);
    }
#pragma unroll
    for (int j = 0; j < 4; ++j) vd[j] = *(const u32*)(v_ + (R0 + j) * D_ + pch);
    const v4u wd = *(const v4u*)(shk_ + pch * 4);
    u32 epoch = 0;
    if (w == 0) epoch = __builtin_amdgcn_raw_buffer_load_b32(rs, 0, WS_EPOCH, 16);
    v4u xq[4];

    if (i >= 256 && i < 256 + 64) *(v4u*)(lds + L_UR + ((i >> 5) & 1) * UR_PITCH + 4096 + (i & 31) * 16) = (v4u){0, 0, 0, 0};
    if (i >= 320 && i < 320 + 16) {
        const int e = i - 320;
        *(v4u*)(lds + L_KA + (e >> 1) * KC_COPY + (e & 1) * 16) = (v4u){0, 0, 0, 0};
    }
    if (i >= 384 && i < 384 + 56) *(u32*)(lds + L_SYNC + (i - 384) * 4) = 0;

    STAMP(1);
    {
#pragma unroll
        for (int j = 0; j < 3; ++j) if (R0 - 3 + j < 0) kd[j] = 0;
#pragma unroll
        for (int c = 0; c < 2; ++c) {
            const u32 sel = c ? 0x07060302u : 0x05040100u;
            const u32 w01 = wd[2 * c], w23 = wd[2 * c + 1];
            u32 P[7];
#pragma unroll
            for (int j = 1; j < 7; ++j) P[j] = __builtin_amdgcn_perm(kd[j - 1], kd[j], sel);
            float res[4];
#pragma unroll
            for (int t = 0; t < 4; ++t) {
                float a = __builtin_amdgcn_fdot2_f32_bf16(__builtin_bit_cast(v2bf, P[t + 3]), __builtin_bit_cast(v2bf, w01), 0.0f, false);
                a = __builtin_amdgcn_fdot2_f32_bf16(__builtin_bit_cast(v2bf, P[t + 1]), __builtin_bit_cast(v2bf, w23), a, false);
                res[t] = a * (c ? bf_hi(vd[t]) : bf_lo(vd[t]));
            }
            typedef u32 v2u __attribute__((ext_vector_type(2)));
            const int half = (1 - (rb & 1)) ^ ((cp >> 3) & 1);
            *(v2u*)(lds + L_T + (2 * cp + c) * 128 + (((rb >> 1) ^ (cp & 7)) * 16) + half * 8) = (v2u){pack_bf(res[3], res[2]), pack_bf(res[1], res[0])};
        }
    }
    SCHED_FENCE();
    if (w < 4) {
#pragma unroll
        for (int j = 0; j < 2; ++j) {
            const int e = i + 256 * j;
            store16(poison4, rs, (e >> 3) * C_PITCH + me * 128 + (e & 7) * 16, WS_C);
        }
    }
    u32 pp[11], dA, dB;
    if (w >= 4) {
        const int pc = 2 * me + ((w - 4) >> 1), pi = pc * NS_ + (i & 127), pj = pi ^ 64;
        pp[0] = Are_[pi]; pp[1] = Aim_[pi]; pp[2] = Bre_[pi]; pp[3] = Bim_[pi]; pp[4] = Cre_[pi]; pp[5] = Cim_[pi];
        pp[6] = Bre_[pj]; pp[7] = Bim_[pj]; pp[8] = Cre_[pj]; pp[9] = Cim_[pj];
        pp[10] = logdt_[pc];
        dA = ssmD_[2 * me + (l & 1)]; dB = Dskip_[2 * me + (l & 1)];
        if (w >= 6) {
#pragma unroll
            for (int j = 0; j < 4; ++j) xq[j] = *(const v4u*)(q_ + (8 * myr + 4 * (w & 1) + j) * D_ + l * 8);
        }
    }
    STAMP(2);
    __syncthreads();
    STAMP(3);

    v4u A[8][4];
    if (w < 4) {
#pragma unroll
        for (int j = 0; j < 2; ++j) {
            typedef u32 v2u __attribute__((ext_vector_type(2)));
            const int e = i + 256 * j, ch = e >> 3, chunk = e & 7, sw = (ch >> 4) & 1;
            const unsigned char* src = lds + L_T + ch * 128 + ((chunk ^ ((ch >> 1) & 7)) * 16);
            const v2u d0 = *(const v2u*)(src + 8 * sw), d1 = *(const v2u*)(src + 8 * (1 - sw));
            store16((v4u){d0.x, d0.y, d1.x, d1.y}, rs, (64 * pcg + ch) * KV_PITCH + (8 * idx + chunk) * 16, WS_KV);
        }
        SCHED_FENCE();
        asm volatile("" ::: "memory");
        v4u sk[3];
        u32 sknext[3];
#pragma unroll
        for (int j = 0; j < 3; ++j) {
            const int e = (i - 64) + 192 * j, ec = (w == 0 || e > 511) ? 0 : e;
            const u16* src = ssmk_ + (2 * me + (ec >> 8)) * S_ + (ec & 255) * 8;
            if (w != 0) {
                sk[j] = *(const v4u*)src;
                sknext[j] = *(const u32*)(src + ((ec & 255) < 255 ? 8 : 0));
            }
        }
        SCHED_FENCE();
        if (w == 0) asm volatile("s_waitcnt vmcnt(0)" ::: "memory");
        else asm volatile("s_waitcnt vmcnt(6)" ::: "memory");
        lds_flag_inc(SY_KV);
        STAMP(4);
        u32 tag = 0, tflag = 0;
        if (w == 0) {
            lds_wait_ge(lds, SY_KV, 4);
            tag = __builtin_amdgcn_readfirstlane(epoch) + 1u;
            if (l == 0) __builtin_amdgcn_raw_buffer_store_b32(tag, rs, me * 128, WS_FLAGS, 16);
            tflag = now_ticks();
            lds_flag_set(SY_TAG, tag);
            lds_flag_set(SY_TAGSET, 1u);
        }
        if (w != 0) {
#pragma unroll
            for (int j = 0; j < 3; ++j) {
                const int e = (i - 64) + 192 * j, sm = e & 255;
                if (e < 512) {
                    unsigned char* c0 = lds + L_KA + (e >> 8) * KC_CH;
                    const u32 nx = sm < 255 ? sknext[j] : 0u;
                    *(v4u*)(c0 + 32 + sm * 16) = sk[j];
                    *(v4u*)(c0 + KC_COPY + 32 + sm * 16) =
                        (v4u){__builtin_amdgcn_alignbit(sk[j].y, sk[j].x, 16), __builtin_amdgcn_alignbit(sk[j].z, sk[j].y, 16),
                              __builtin_amdgcn_alignbit(sk[j].w, sk[j].z, 16), __builtin_amdgcn_alignbit(nx, sk[j].w, 16)};
                    if (sm == 0) *(u32*)(c0 + KC_COPY + 28) = sk[j].x << 16;
                }
            }
            lds_flag_inc(SY_KA);
        }
        if (w == 0) {
            const u32 foff = (((xcd - (l & 31)) & 7) * 32 + (l & 31)) * 128;
            wait_until(tflag, KV_DELAY);
            for (u32 it = 0;; ++it) {
                asm volatile("" ::: "memory");
                if (__all(__builtin_amdgcn_raw_buffer_load_b32(rs, foff, WS_FLAGS, 16) == tag)) break;
                if (spin_give_up(lds, it * 4 + 3, GLB_SPIN_CAP * 4)) break;
            }
            lds_flag_set(SY_GO, 1u);
        } else {
            lds_wait_ge(lds, SY_KA, 3);
            load_afrags(A, lds, L_KA, w >> 1, w & 1, l);
            lds_wait_ge(lds, SY_GO, 1u);
        }
        STAMP(5);
        v4u g0, g1;
        {
            const u32 off = (2 * me + (l >> 5)) * KV_PITCH + (64 * w + (l & 31)) * 16;
            g0 = __builtin_amdgcn_raw_buffer_load_b128(rs, off, WS_KV, 16);
            g1 = __builtin_amdgcn_raw_buffer_load_b128(rs, off + 512, WS_KV, 16);
        }
        SCHED_FENCE();
        if (w == 0) {
            lds_wait_ge(lds, SY_KA, 3);
            load_afrags(A, lds, L_KA, 0, 0, l);
        }
        STAMP(6);
        unsigned char* dst = lds + L_UR + (l >> 5) * UR_PITCH + 2 * (2040 - 8 * (64 * w + (l & 31)));
        *(v4u*)dst = g0;
        *(v4u*)(dst - 512) = g1;
        lds_flag_set(SY_INTILE + 8 * w, 1u);
        lds_flag_set(SY_INTILE + 8 * w + 4, 1u);
        WAIT_VM0();

    } else {
        __builtin_amdgcn_s_setprio(2);
        const int wa = w - 4, ch = wa >> 1, n = (i - 256) & 127;
#pragma unroll
        for (int j = 0; j < 11; ++j) asm volatile("" : "+v"(pp[j]));
        asm volatile("" : "+v"(dA), "+v"(dB));
        const float ex = expf(f_of(pp[10] << 16));
        const float dt = bf_lo(pack_bf(ex, ex));
        const float al = dt * f_of(pp[0] << 16) * 1.44269504088896341f, be = dt * f_of(pp[1] << 16) * 0.15915494309189535f;
        v2f W;
        float inv;
        {
            const float br = dt * f_of(pp[2] << 16), bi = dt * f_of(pp[3] << 16), cr = f_of(pp[4] << 16), ci = f_of(pp[5] << 16);
            const float br2 = dt * f_of(pp[6] << 16), bi2 = dt * f_of(pp[7] << 16), cr2 = f_of(pp[8] << 16), ci2 = f_of(pp[9] << 16);
            W = (v2f){cr * br - ci * bi, cr * bi + ci * br};
            float mx = fmaxf(fmaxf(fabsf(W.x), fabsf(W.y)), fmaxf(fabsf(cr2 * br2 - ci2 * bi2), fabsf(cr2 * bi2 + ci2 * br2)));
            mx = fmaxf(mx, dppf<0x128>(mx));
            mx = fmaxf(mx, dppf<0x141>(mx));
            mx = fmaxf(mx, dppf<0xB1>(mx));
            mx = fmaxf(mx, dppf<0x4E>(mx));
            float m0 = f_of((u32)__builtin_amdgcn_readlane((int)u_of(mx), 0)), m1 = f_of((u32)__builtin_amdgcn_readlane((int)u_of(mx), 16));
            float m2 = f_of((u32)__builtin_amdgcn_readlane((int)u_of(mx), 32)), m3 = f_of((u32)__builtin_amdgcn_readlane((int)u_of(mx), 48));
            const int e = __builtin_amdgcn_frexp_expf(fmaxf(fmaxf(m0, m1), fmaxf(m2, m3)));
            W = (v2f){__builtin_amdgcn_ldexpf(W.x, 14 - e), __builtin_amdgcn_ldexpf(W.y, 14 - e)};
            inv = __builtin_amdgcn_ldexpf(1.0f, e - 14);
        }
        unsigned char* tab = lds + L_TAB + ch * TAB_CH + n * 4;
#define TAB_PUT(row, val) (*(u32*)(tab + (row) * TAB_ROW) = pack_h(val))
        const v2f E1 = cexp_pow(al, be, 1.0f), E8 = cexp_pow(al, be, 8.0f), E128 = cexp_pow(al, be, 128.0f);
        const v2f E256 = cmul(E128, E128), E384 = cmul(E256, E128), E512 = cmul(E256, E256), E1024 = cmul(E512, E512);
        {
            const v2f E8c = {E8.x, -E8.y};
            v2f B = {W.x, -W.y}, P = {1.0f, 0.0f}, Q = E512, Rr = E1024, P4 = cmul(cmul(E1, E1), cmul(E1, E1));
#pragma unroll
            for (int r = 0; r < 8; ++r) {
                TAB_PUT(41 + 2 * r, B);
                B = cmul(B, E8c);
                TAB_PUT(42 + 2 * r, B);
                if (r < 7) B = cmul(B, E8c);
                TAB_PUT(r, P);
                TAB_PUT(8 + r, r ? cmul(P, E128) : E128);
                if ((r & 1) == 0) {
                    TAB_PUT(16 + (r >> 1), r ? cmul(P, E256) : E256);
                    TAB_PUT(20 + (r >> 1), r ? cmul(P, E384) : E384);
                }
                if (r < 7) P = r ? cmul(P, E1) : E1;
                TAB_PUT(32 + r, Rr);
                if (r < 7) Rr = cmul(Rr, E128);
                if (r < 4) {
                    TAB_PUT(24 + 2 * r, Q);
                    TAB_PUT(25 + 2 * r, cmul(Q, P4));
                    if (r < 3) Q = cmul(Q, E128);
                }
            }
            TAB_PUT(40, cmul(E1024, E1024));
        }
#undef TAB_PUT
        if (l == 0) *(float*)(lds + L_DCO + 16 + ch * 4) = inv;
        lds_flag_inc(SY_TAB + 4 * ch);
        if (l < 2) { *(u32*)(lds + L_DCO + l * 4) = dA << 16; *(u32*)(lds + L_DCO + 8 + l * 4) = dB << 16; }
        STAMP(4);
        if (w < 6) {
            const int kc = w & 1;
            lds_wait_ge(lds, SY_TAB + 4 * kc, 2);
            {
                const int row = l & 15, g = l >> 4;
                const unsigned char* tb = lds + L_TAB + kc * TAB_CH + row * TAB_ROW + g * 16;
                float* kt = (float*)(lds + L_KT + kc * KT_CH);
                const float scale = *(const float*)(lds + L_DCO + 16 + kc * 4);
                v4u fb[8], fa[8];
                v4f acc[3];
#pragma unroll
                for (int s2 = 0; s2 < 8; ++s2) fb[s2] = *(const v4u*)(tb + 41 * TAB_ROW + s2 * 64);
#pragma unroll
                for (int tile = 0; tile < 3; ++tile) {
#pragma unroll
                    for (int s2 = 0; s2 < 8; ++s2) fa[s2] = *(const v4u*)(tb + tile * 16 * TAB_ROW + s2 * 64);
                    acc[tile] = (v4f){0, 0, 0, 0};
#pragma unroll
                    for (int s2 = 0; s2 < 8; ++s2)
                        acc[tile] = __builtin_amdgcn_mfma_f32_16x16x32_f16(__builtin_bit_cast(v8h, fa[s2]), __builtin_bit_cast(v8h, fb[s2]), acc[tile], 0, 0, 0);
                }
                asm volatile("s_nop 7\n\ts_nop 1" : "+v"(acc[0]), "+v"(acc[1]), "+v"(acc[2]));
#pragma unroll
                for (int r = 0; r < 4; ++r) {
                    const int rw = 4 * g + r;
                    kt[128 * (rw >> 3) + (rw & 7) + 8 * row] = vmul(acc[0][r], scale);
                    kt[rw < 8 ? 256 + 64 * (rw >> 2) + (rw & 3) + 4 * row : 384 + 32 * ((rw - 8) >> 1) + (rw & 1) + 2 * row] = vmul(acc[1][r], scale);
                    kt[rw < 8 ? 512 + 16 * rw + row : ((rw == 8 && row == 0) ? 640 : 644 + (l & 3))] = vmul(acc[2][r], scale);
                }
            }
            asm volatile("s_waitcnt lgkmcnt(0)" ::: "memory");
            {
                const float* t = (const float*)(lds + L_KT + kc * KT_CH);
                unsigned char* c0 = lds + L_KB + kc * KC_CH;
                const float a2 = t[512 + l], b2 = t[513 + l], a3 = t[576 + l], b3 = t[577 + l];
                const float v0 = t[384 + 2 * l], v1 = t[385 + 2 * l], v2 = t[386 + 2 * l];
                float kvv[9];
#pragma unroll
                for (int j = 0; j < 9; ++j) {
                    const int lag = 8 * l + j;
                    const u32 bb = pack_bf((float)lag, 0.0f) & 0xFFFFu;
                    kvv[j] = t[l < 32 ? lag : (int)(bb - 0x4280u)];
                }
                {
                    const u32 e0 = pack_bf(kvv[0], kvv[1]);
                    *(v4u*)(c0 + 32 + l * 16) = (v4u){e0, pack_bf(kvv[2], kvv[3]), pack_bf(kvv[4], kvv[5]), pack_bf(kvv[6], kvv[7])};
                    *(v4u*)(c0 + KC_COPY + 32 + l * 16) = (v4u){pack_bf(kvv[1], kvv[2]), pack_bf(kvv[3], kvv[4]), pack_bf(kvv[5], kvv[6]), pack_bf(kvv[7], kvv[8])};
                    if (l == 0) *(u32*)(c0 + KC_COPY + 28) = e0 << 16;
                }
                {
                    const u32 p00 = pack_bf(v0, v0), p01 = pack_bf(v0, v1), p11 = pack_bf(v1, v1), p12 = pack_bf(v1, v2), p22 = pack_bf(v2, v2);
                    *(v4u*)(c0 + 32 + (64 + l) * 16) = (v4u){p00, p01, p11, p22};
                    *(v4u*)(c0 + KC_COPY + 32 + (64 + l) * 16) = (v4u){p00, p11, p12, p22};
                }
#pragma unroll
                for (int it = 0; it < 2; ++it) {
                    const float a = it ? a3 : a2, b = it ? b3 : b2, c4 = (l & 1) ? b : a;
                    const u32 paa = pack_bf(a, a), pbb = pack_bf(b, b);
                    *(v4u*)(c0 + 32 + (128 + 64 * it + l) * 16) = (v4u){paa, paa, pack_bf(c4, b), pbb};
                    *(v4u*)(c0 + KC_COPY + 32 + (128 + 64 * it + l) * 16) = (v4u){paa, pack_bf(a, c4), pbb, pbb};
                }
            }
            lds_flag_inc(SY_KREADY);
            STAMP(5);
            if (w == 4) {
                lds_wait_ge(lds, SY_TAGSET, 1u);
                const u32 tag = __builtin_amdgcn_readfirstlane(lds_ld(lds, SY_TAG));
                lds_wait_ge_slow(lds, SY_GO, 1u);
                wait_until(now_ticks(), ALLF_DELAY);
                for (u32 it = 0;; ++it) {
                    asm volatile("" ::: "memory");
                    if (it) __builtin_amdgcn_s_sleep(8);
                    const u32 f0 = __builtin_amdgcn_raw_buffer_load_b32(rs, l * 128, WS_FLAGS, 16);
                    const u32 f1 = __builtin_amdgcn_raw_buffer_load_b32(rs, (l + 64) * 128, WS_FLAGS, 16);
                    const u32 f2 = __builtin_amdgcn_raw_buffer_load_b32(rs, (l + 128) * 128, WS_FLAGS, 16);
                    const u32 f3 = __builtin_amdgcn_raw_buffer_load_b32(rs, (l + 192) * 128, WS_FLAGS, 16);
                    if (__all(f0 == tag && f1 == tag && f2 == tag && f3 == tag)) break;
                    if (spin_give_up(lds, it * 4 + 3, GLB_SPIN_CAP * 4)) break;
                }
                if (me == 0 && l == 0) __builtin_amdgcn_raw_buffer_store_b32(tag, rs, 0, WS_EPOCH, 16);
                lds_flag_set(SY_ALLF, 1u);
            }
            WAIT_VM0();
        } else {
            const int kr = w & 1;
            lds_wait_ge(lds, SY_KV, 4);
#pragma unroll
            for (int j = 0; j < 4; ++j) store16(poison4, rs, myr * U_PITCH + ((i & 127) + 128 * j) * 16, WS_U);
            STAMP(5);
#pragma unroll
            for (int j = 0; j < 4; ++j) asm volatile("" : "+v"(xq[j]));
            {
                float f[4][8], mean[4], rstd[4];
#pragma unroll
                for (int rr = 0; rr < 4; ++rr) {
                    const v4u xr = xq[rr];
                    f[rr][0] = bf_lo(xr.x); f[rr][1] = bf_hi(xr.x); f[rr][2] = bf_lo(xr.y); f[rr][3] = bf_hi(xr.y);
                    f[rr][4] = bf_lo(xr.z); f[rr][5] = bf_hi(xr.z); f[rr][6] = bf_lo(xr.w); f[rr][7] = bf_hi(xr.w);
                    mean[rr] = vadd(vadd(vadd(f[rr][0], f[rr][1]), vadd(f[rr][2], f[rr][3])), vadd(vadd(f[rr][4], f[rr][5]), vadd(f[rr][6], f[rr][7])));
                }
#pragma unroll
                for (int rr = 0; rr < 4; ++rr) mean[rr] = vmul(wave_sum(mean[rr]), -1.0f / D_);
#pragma unroll
                for (int rr = 0; rr < 4; ++rr) {
#pragma unroll
                    for (int j = 0; j < 8; ++j) f[rr][j] = vadd(f[rr][j], mean[rr]);
                    float q2 = vmul(f[rr][0], f[rr][0]);
#pragma unroll
                    for (int j = 1; j < 8; ++j) q2 = vfma(f[rr][j], f[rr][j], q2);
                    rstd[rr] = q2;
                }
#pragma unroll
                for (int rr = 0; rr < 4; ++rr) rstd[rr] = __builtin_amdgcn_rsqf(vfma(wave_sum(rstd[rr]), 1.0f / D_, 1e-5f));
                asm volatile("s_nop 1" : "+v"(rstd[0]), "+v"(rstd[1]), "+v"(rstd[2]), "+v"(rstd[3]));
#pragma unroll
                for (int rr = 0; rr < 4; ++rr) {
                    const v4u xr = xq[rr];
                    u32 o[8];
#pragma unroll
                    for (int j = 0; j < 4; ++j) {
                        const u32 np = pack_bf(vmul(f[rr][2 * j], rstd[rr]), vmul(f[rr][2 * j + 1], rstd[rr]));
                        o[2 * j] = __builtin_amdgcn_perm(np, xr[j], 0x05040100u);
                        o[2 * j + 1] = __builtin_amdgcn_perm(np, xr[j], 0x07060302u);
                    }
                    unsigned char* dstx = lds + L_XN + (4 * kr + rr) * 2048 + l * 32;
                    *(v4u*)dstx = (v4u){o[0], o[1], o[2], o[3]};
                    *(v4u*)(dstx + 16) = (v4u){o[4], o[5], o[6], o[7]};
                    if (l == 0) *(float*)(lds + L_PIV + (4 * kr + rr) * 4) = -mean[rr];
                }
            }
            lds_flag_inc(SY_X0);
            WAIT_VM0();
            lds_flag_inc(SY_POIS);
            lds_wait_ge(lds, SY_POIS, 2);
            STAMP(6);
        }
    }

    asm volatile("" ::"v"(code_touch));
    WAIT_VM0();
    const int i_ = i;
    const __amdgpu_buffer_rsrc_t ro = __builtin_amdgcn_make_buffer_rsrc(out_, 0, S_ * D_ * 2, 0x00020000);

#pragma unroll 1
    for (int k = 0; k < 3; ++k) {
        int i = i_;
        asm volatile("" : "+v"(i));
        const int l = i & 63;
        STAMP(7 + 8 * k);
        const u32 cvoff = (myr >> 2) * C_PITCH + (i >> 1) * 128 + (myr & 3) * 32 + (i & 1) * 16, csoff = WS_C + k * C_SIZE;
        v4u cg = poison4;
        bool cissued = false;
        if (w < 4) {
            const int ch = w >> 1, h = w & 1, a = l & 15, g = l >> 4;
            const unsigned char* ub = lds + L_UR + ch * UR_PITCH + 2 * (2032 + 128 * h - 16 * a + 8 * g) - 3584;
            const u32 ready = (u32)(k + 1);
            if (k == 1 && mytile < KLOAD_SPLIT) load_afrags(A, lds, L_KB, ch, h, l);
            v4f acc[8];
#pragma unroll
            for (int c = 0; c < 8; ++c) acc[c] = (v4f){0, 0, 0, 0};
#pragma unroll
            for (int c = 0; c < 8; ++c) asm volatile("" : "+v"(acc[c]));
            v4u B[2][4];
            __builtin_amdgcn_s_setprio(1);
            for (u32 it = 0;; ++it) {
                asm volatile("" ::: "memory");
                const u32 f0 = lds_ld(lds, SY_INTILE);
                asm volatile("" ::: "memory");
#pragma unroll
                for (int r = 0; r < 4; ++r) B[0][r] = *(const v4u*)(ub + 512 * 7 + 64 * r);
                if (__builtin_expect((u32)__builtin_amdgcn_readfirstlane(f0) >= ready, 1)) break;
                if (spin_give_up(lds, it, LDS_SPIN_CAP)) break;
            }
            STAMP(8 + 8 * k);
#define MFA "v_mfma_f32_16x16x32_bf16 "
#define MF1(cur, c, p, r)                                                                                     \
    asm volatile(MFA "%0, %1, %2, %0" : "+v"(acc[c]) : "v"(A[p][r]), "v"(B[cur][r]));                          \
    SCHED_FENCE();
#define MF4(cur, c, p)                                                                                        \
    asm volatile(MFA "%0, %1, %5, %0\n\t" MFA "%0, %2, %6, %0\n\t" MFA "%0, %3, %7, %0\n\t" MFA "%0, %4, %8, %0" \
                 : "+v"(acc[c]) : "v"(A[p][0]), "v"(A[p][1]), "v"(A[p][2]), "v"(A[p][3]),                      \
                   "v"(B[cur][0]), "v"(B[cur][1]), "v"(B[cur][2]), "v"(B[cur][3]));                             \
    SCHED_FENCE();
            u32 vflag = SY_PART + 4 * w, vready = ready, vpart = L_PART + ch * PART_CH + h * 1024 + 16 * (16 * g + ((a + 8 * (g >> 1)) & 15));
            asm volatile("" : "+v"(vflag), "+v"(vready), "+v"(vpart));
            u32 fl = 0;
            MF1(0, 0, 0, 0)
            fl = lds_ld(lds, SY_INTILE + 4); SCHED_FENCE();
            MF1(0, 0, 0, 1)
            B[1][0] = *(const v4u*)(ub + 3072); SCHED_FENCE();
            MF1(0, 0, 0, 2)
            B[1][1] = *(const v4u*)(ub + 3136); SCHED_FENCE();
            MF1(0, 0, 0, 3)
            B[1][2] = *(const v4u*)(ub + 3200); SCHED_FENCE();
            MF1(0, 1, 1, 0)
            B[1][3] = *(const v4u*)(ub + 3264); SCHED_FENCE();
            MF1(0, 1, 1, 1)
            MF1(0, 1, 1, 2)
            put_partial<0>(vpart, acc[0]); SCHED_FENCE();
            MF1(0, 1, 1, 3)
            MF1(0, 2, 2, 0)
            put_partial_flag<0>(vflag, vready); SCHED_FENCE();
            MF1(0, 2, 2, 1)
            const u32 fls0 = (u32)__builtin_amdgcn_readfirstlane(fl); SCHED_FENCE();
            MF1(0, 2, 2, 2)
            MF1(0, 2, 2, 3)
            MF4(0, 3, 3)
            MF4(0, 4, 4)
            MF4(0, 5, 5)
            MF4(0, 6, 6)
            MF4(0, 7, 7)
            if (__builtin_expect(fls0 < ready, 0)) {
                lds_wait_ge(lds, SY_INTILE + 4, ready);
#pragma unroll
                for (int r = 0; r < 4; ++r) B[1][r] = *(const v4u*)(ub + 3072 + 64 * r);
            }
            asm volatile("" ::: "memory");
            SCHED_FENCE();
            MF1(1, 1, 0, 0)
            fl = lds_ld(lds, SY_INTILE + 8); SCHED_FENCE();
            MF1(1, 1, 0, 1)
            B[0][0] = *(const v4u*)(ub + 2560); SCHED_FENCE();
            MF1(1, 1, 0, 2)
            B[0][1] = *(const v4u*)(ub + 2624); SCHED_FENCE();
            MF1(1, 1, 0, 3)
            B[0][2] = *(const v4u*)(ub + 2688); SCHED_FENCE();
            MF1(1, 2, 1, 0)
            B[0][3] = *(const v4u*)(ub + 2752); SCHED_FENCE();
            MF1(1, 2, 1, 1)
            MF1(1, 2, 1, 2)
            put_partial<1>(vpart, acc[1]); SCHED_FENCE();
            MF1(1, 2, 1, 3)
            MF1(1, 3, 2, 0)
            put_partial_flag<1>(vflag, vready); SCHED_FENCE();
            MF1(1, 3, 2, 1)
            const u32 fls1 = (u32)__builtin_amdgcn_readfirstlane(fl); SCHED_FENCE();
            MF1(1, 3, 2, 2)
            MF1(1, 3, 2, 3)
            MF4(1, 4, 3)
            MF4(1, 5, 4)
            MF4(1, 6, 5)
            MF4(1, 7, 6)
            if (__builtin_expect(fls1 < ready, 0)) {
                lds_wait_ge(lds, SY_INTILE + 8, ready);
#pragma unroll
                for (int r = 0; r < 4; ++r) B[0][r] = *(const v4u*)(ub + 2560 + 64 * r);
            }
            asm volatile("" ::: "memory");
            SCHED_FENCE();
            MF1(0, 2, 0, 0)
            fl = lds_ld(lds, SY_INTILE + 12); SCHED_FENCE();
            MF1(0, 2, 0, 1)
            B[1][0] = *(const v4u*)(ub + 2048); SCHED_FENCE();
            MF1(0, 2, 0, 2)
            B[1][1] = *(const v4u*)(ub + 2112); SCHED_FENCE();
            MF1(0, 2, 0, 3)
            B[1][2] = *(const v4u*)(ub + 2176); SCHED_FENCE();
            MF1(0, 3, 1, 0)
            B[1][3] = *(const v4u*)(ub + 2240); SCHED_FENCE();
            MF1(0, 3, 1, 1)
            MF1(0, 3, 1, 2)
            put_partial<2>(vpart, acc[2]); SCHED_FENCE();
            MF1(0, 3, 1, 3)
            MF1(0, 4, 2, 0)
            put_partial_flag<2>(vflag, vready); SCHED_FENCE();
            MF1(0, 4, 2, 1)
            const u32 fls2 = (u32)__builtin_amdgcn_readfirstlane(fl); SCHED_FENCE();
            MF1(0, 4, 2, 2)
            MF1(0, 4, 2, 3)
            MF4(0, 5, 3)
            MF4(0, 6, 4)
            MF4(0, 7, 5)
            if (__builtin_expect(fls2 < ready, 0)) {
                lds_wait_ge(lds, SY_INTILE + 12, ready);
#pragma unroll
                for (int r = 0; r < 4; ++r) B[1][r] = *(const v4u*)(ub + 2048 + 64 * r);
            }
            asm volatile("" ::: "memory");
            SCHED_FENCE();
            MF1(1, 3, 0, 0)
            fl = lds_ld(lds, SY_INTILE + 16); SCHED_FENCE();
            MF1(1, 3, 0, 1)
            B[0][0] = *(const v4u*)(ub + 1536); SCHED_FENCE();
            MF1(1, 3, 0, 2)
            B[0][1] = *(const v4u*)(ub + 1600); SCHED_FENCE();
            MF1(1, 3, 0, 3)
            B[0][2] = *(const v4u*)(ub + 1664); SCHED_FENCE();
            MF1(1, 4, 1, 0)
            B[0][3] = *(const v4u*)(ub + 1728); SCHED_FENCE();
            MF1(1, 4, 1, 1)
            MF1(1, 4, 1, 2)
            put_partial<3>(vpart, acc[3]); SCHED_FENCE();
            MF1(1, 4, 1, 3)
            MF1(1, 5, 2, 0)
            put_partial_flag<3>(vflag, vready); SCHED_FENCE();
            MF1(1, 5, 2, 1)
            const u32 fls3 = (u32)__builtin_amdgcn_readfirstlane(fl); SCHED_FENCE();
            MF1(1, 5, 2, 2)
            MF1(1, 5, 2, 3)
            MF4(1, 6, 3)
            MF4(1, 7, 4)
            if (__builtin_expect(fls3 < ready, 0)) {
                lds_wait_ge(lds, SY_INTILE + 16, ready);
#pragma unroll
                for (int r = 0; r < 4; ++r) B[0][r] = *(const v4u*)(ub + 1536 + 64 * r);
            }
            asm volatile("" ::: "memory");
            SCHED_FENCE();
            MF1(0, 4, 0, 0)
            fl = lds_ld(lds, SY_INTILE + 20); SCHED_FENCE();
            MF1(0, 4, 0, 1)
            B[1][0] = *(const v4u*)(ub + 1024); SCHED_FENCE();
            MF1(0, 4, 0, 2)
            B[1][1] = *(const v4u*)(ub + 1088); SCHED_FENCE();
            MF1(0, 4, 0, 3)
            B[1][2] = *(const v4u*)(ub + 1152); SCHED_FENCE();
            MF1(0, 5, 1, 0)
            B[1][3] = *(const v4u*)(ub + 1216); SCHED_FENCE();
            MF1(0, 5, 1, 1)
            MF1(0, 5, 1, 2)
            put_partial<4>(vpart, acc[4]); SCHED_FENCE();
            MF1(0, 5, 1, 3)
            MF1(0, 6, 2, 0)
            put_partial_flag<4>(vflag, vready); SCHED_FENCE();
            MF1(0, 6, 2, 1)
            const u32 fls4 = (u32)__builtin_amdgcn_readfirstlane(fl); SCHED_FENCE();
            MF1(0, 6, 2, 2)
            MF1(0, 6, 2, 3)
            MF4(0, 7, 3)
            if (__builtin_expect(fls4 < ready, 0)) {
                lds_wait_ge(lds, SY_INTILE + 20, ready);
#pragma unroll
                for (int r = 0; r < 4; ++r) B[1][r] = *(const v4u*)(ub + 1024 + 64 * r);
            }
            asm volatile("" ::: "memory");
            SCHED_FENCE();
            MF1(1, 5, 0, 0)
            fl = lds_ld(lds, SY_INTILE + 24); SCHED_FENCE();
            MF1(1, 5, 0, 1)
            B[0][0] = *(const v4u*)(ub + 512); SCHED_FENCE();
            MF1(1, 5, 0, 2)
            B[0][1] = *(const v4u*)(ub + 576); SCHED_FENCE();
            MF1(1, 5, 0, 3)
            B[0][2] = *(const v4u*)(ub + 640); SCHED_FENCE();
            MF1(1, 6, 1, 0)
            B[0][3] = *(const v4u*)(ub + 704); SCHED_FENCE();
            MF1(1, 6, 1, 1)
            MF1(1, 6, 1, 2)
            put_partial<5>(vpart, acc[5]); SCHED_FENCE();
            MF1(1, 6, 1, 3)
            MF1(1, 7, 2, 0)
            put_partial_flag<5>(vflag, vready); SCHED_FENCE();
            MF1(1, 7, 2, 1)
            const u32 fls5 = (u32)__builtin_amdgcn_readfirstlane(fl); SCHED_FENCE();
            MF1(1, 7, 2, 2)
            MF1(1, 7, 2, 3)
            if (__builtin_expect(fls5 < ready, 0)) {
                lds_wait_ge(lds, SY_INTILE + 24, ready);
#pragma unroll
                for (int r = 0; r < 4; ++r) B[0][r] = *(const v4u*)(ub + 512 + 64 * r);
            }
            asm volatile("" ::: "memory");
            SCHED_FENCE();
            MF1(0, 6, 0, 0)
            fl = lds_ld(lds, SY_INTILE + 28); SCHED_FENCE();
            MF1(0, 6, 0, 1)
            B[1][0] = *(const v4u*)(ub + 0); SCHED_FENCE();
            MF1(0, 6, 0, 2)
            B[1][1] = *(const v4u*)(ub + 64); SCHED_FENCE();
            MF1(0, 6, 0, 3)
            B[1][2] = *(const v4u*)(ub + 128); SCHED_FENCE();
            MF1(0, 7, 1, 0)
            B[1][3] = *(const v4u*)(ub + 192); SCHED_FENCE();
            MF1(0, 7, 1, 1)
            MF1(0, 7, 1, 2)
            put_partial<6>(vpart, acc[6]); SCHED_FENCE();
            MF1(0, 7, 1, 3)
            put_partial_flag<6>(vflag, vready); SCHED_FENCE();
            const u32 fls6 = (u32)__builtin_amdgcn_readfirstlane(fl); SCHED_FENCE();
            if (__builtin_expect(fls6 < ready, 0)) {
                lds_wait_ge(lds, SY_INTILE + 28, ready);
#pragma unroll
                for (int r = 0; r < 4; ++r) B[1][r] = *(const v4u*)(ub + 0 + 64 * r);
            }
            asm volatile("" ::: "memory");
            STAMP(9 + 8 * k); if (k == 0) STAMPC(30);
            SCHED_FENCE();
            MF4(1, 7, 0)
            asm volatile("s_nop 7" ::: "memory");
            put_partial<7>(vpart, acc[7]); put_partial_flag<7>(vflag, vready);
            {
                const u32 pub = __builtin_amdgcn_readfirstlane(lds_ld(lds, SY_PUB)), allf = __builtin_amdgcn_readfirstlane(lds_ld(lds, SY_ALLF));
                const u32 pubt = __builtin_amdgcn_readfirstlane(lds_ld(lds, SY_PUBT));
                if (pub >= ready && allf && now_ticks() - pubt >= C_DELAY) { cg = __builtin_amdgcn_raw_buffer_load_b128(rs, cvoff, csoff, 16); cissued = true; }
                SCHED_FENCE();
            }
#undef MF1
#undef MF4
#undef MFA
            __builtin_amdgcn_s_setprio(0);
            if (k == 0 && mytile >= KLOAD_SPLIT) {
                lds_wait_ge_slow(lds, SY_KREADY, 2);
                load_afrags(A, lds, L_KB, ch, h, l);
            }
        } else if (w >= 6) {
            const int ch = l & 1, tl = 32 * (l >> 3) + 8 * ((l >> 1) & 3);
            const float dco = *(const float*)(lds + L_DCO + (k == 0 ? 0 : 8) + ch * 4);
            __builtin_amdgcn_s_setprio(3);
#pragma unroll 1
            for (int c = w & 1; c < 8; c += 2) {
                if (c >= 6) {
                    const u32 pub = __builtin_amdgcn_readfirstlane(lds_ld(lds, SY_PUB)), allf = __builtin_amdgcn_readfirstlane(lds_ld(lds, SY_ALLF));
                    const u32 pubt = __builtin_amdgcn_readfirstlane(lds_ld(lds, SY_PUBT));
                    const u32 in6 = __builtin_amdgcn_readfirstlane(lds_ld(lds, SY_INTILE + 24)), in7 = __builtin_amdgcn_readfirstlane(lds_ld(lds, SY_INTILE + 28));
                    if (pub >= (u32)(k + 1) && allf && in6 >= (u32)(k + 1) && in7 >= (u32)(k + 1) && now_ticks() - pubt >= C_DELAY) { cg = __builtin_amdgcn_raw_buffer_load_b128(rs, cvoff, csoff, 16); cissued = true; }
                }
                const int sb = (l >> 1) & 3, pa = 2 * (l >> 3) + (sb >> 1);
                const unsigned char* pp2 = lds + L_PART + c * PART_TILE + ch * PART_CH + 16 * (32 * (sb & 1) + ((pa + 8 * (sb & 1)) & 15));
                v4f a0, a1, b0, b1;
                v4u u;
                for (u32 it = 0;; ++it) {
                    asm volatile("" ::: "memory");
                    const v4u f = *(const v4u*)(lds + SY_PART + 16 * c);
                    asm volatile("" ::: "memory");
                    a0 = *(const v4f*)pp2; a1 = *(const v4f*)(pp2 + 256);
                    b0 = *(const v4f*)(pp2 + 1024); b1 = *(const v4f*)(pp2 + 1280);
                    u = *(const v4u*)(lds + L_UR + ch * UR_PITCH + 2 * (2040 - (256 * c + tl)));
                    const u32 fa = f.x < f.y ? f.x : f.y, fb = f.z < f.w ? f.z : f.w;
                    if ((u32)__builtin_amdgcn_readfirstlane(fa < fb ? fa : fb) >= (u32)(k + 1)) break;
                    if (spin_give_up(lds, it, LDS_SPIN_CAP)) break;
                }
                v4u o;
                o.x = no_poison(pack_bf(vfma(dco, bf_hi(u.w), vadd(a0.x, b0.x)), vfma(dco, bf_lo(u.w), vadd(a0.y, b0.y))));
                o.y = no_poison(pack_bf(vfma(dco, bf_hi(u.z), vadd(a0.z, b0.z)), vfma(dco, bf_lo(u.z), vadd(a0.w, b0.w))));
                o.z = no_poison(pack_bf(vfma(dco, bf_hi(u.y), vadd(a1.x, b1.x)), vfma(dco, bf_lo(u.y), vadd(a1.y, b1.y))));
                o.w = no_poison(pack_bf(vfma(dco, bf_hi(u.x), vadd(a1.z, b1.z)), vfma(dco, bf_lo(u.x), vadd(a1.w, b1.w))));
                store16(o, rs, (8 * c + (l >> 3)) * C_PITCH + me * 128 + (l & 7) * 16, WS_C + k * C_SIZE);
                if (c == mytile) { lds_flag_set(SY_PUBT, now_ticks()); lds_flag_set(SY_PUB, (u32)(k + 1)); }
                if (c < 2) STAMP(8 + 8 * k);
                if (c >= 6) STAMP(9 + 8 * k);
            }
            __builtin_amdgcn_s_setprio(2);
        }

        asm volatile("" : "+v"(i));
        STAMP(10 + 8 * k);
        float x[8], y[8], dm, rsd;
        u32 n0[8];
        float piv;
        v4u yap = {0, 0, 0, 0};
        if (!cissued) {
            if (k == 0) lds_wait_ge(lds, SY_ALLF, 1u);
            lds_wait_ge(lds, SY_INTILE + 24, (u32)(k + 1));
            lds_wait_ge(lds, SY_INTILE + 28, (u32)(k + 1));
            lds_wait_ge(lds, SY_PUB, (u32)(k + 1));
            wait_until(__builtin_amdgcn_readfirstlane(lds_ld(lds, SY_PUBT)), C_DELAY);
            cg = __builtin_amdgcn_raw_buffer_load_b128(rs, cvoff, csoff, 16);
        }
        STAMP(11 + 8 * k);
        SCHED_FENCE();
        if (k == 0) {
            store16(poison4, rs, (i >> 3) * C_PITCH + me * 128 + (i & 7) * 16, WS_C + C_SIZE);
            store16(poison4, rs, myr * U_PITCH + i * 16, WS_U + U_SIZE);
            lds_wait_ge(lds, SY_X0, 2);
#pragma unroll
            for (int t = 0; t < 8; ++t) {
                const u32 xn = *(const u32*)(lds + L_XN + t * 2048 + i * 4);
                x[t] = bf_lo(xn);
                n0[t] = (t & 1) ? (xn & 0xFFFF0000u) : (xn >> 16);
            }
            piv = *(const float*)(lds + L_PIV + (l & 7) * 4);
        } else {
#pragma unroll
            for (int t = 0; t < 8; ++t) n0[t] = (t & 1) ? 0x3F800000u : 0x00003F80u;
            if (k == 1) store16(poison4, rs, (i >> 3) * C_PITCH + me * 128 + (i & 7) * 16, WS_C + 2 * C_SIZE);
            const v4f xa = *(const v4f*)(lds + L_PARK + i * 16), xb = *(const v4f*)(lds + L_PARK + 8192 + i * 16);
            x[0] = xa.x; x[1] = xa.y; x[2] = xa.z; x[3] = xa.w; x[4] = xb.x; x[5] = xb.y; x[6] = xb.z; x[7] = xb.w;
            yap = *(const v4u*)(lds + L_PARK + 16384 + i * 16);
            piv = *(const float*)(lds + L_PARK + 24576 + i * 4);
        }
        v4u sel_lo, sel_hi;
        {
            const u32 one = 0x3F80u << (16 * (l & 1)), dsel = (l & 7) >> 1;
            const v4u pat = {dsel == 0 ? one : 0u, dsel == 1 ? one : 0u, dsel == 2 ? one : 0u, dsel == 3 ? one : 0u};
            const v4u zero = {0, 0, 0, 0};
            sel_lo = (l & 8) ? zero : pat;
            sel_hi = (l & 8) ? pat : zero;
        }
        u32 red_rd0 = L_RED + (l & 15) * 32, red_wr0 = red_rd0 + w * 4, red_rd1 = red_rd0 + 512, red_wr1 = red_wr0 + 512;
        float ya[8] = {bf_lo(yap.x), bf_hi(yap.x), bf_lo(yap.y), bf_hi(yap.y), bf_lo(yap.z), bf_hi(yap.z), bf_lo(yap.w), bf_hi(yap.w)};
        asm volatile("" : "+v"(sel_lo), "+v"(sel_hi), "+v"(red_rd0), "+v"(red_wr0), "+v"(red_rd1), "+v"(red_wr1), "+v"(piv));
        asm volatile("" : "+v"(ya[0]), "+v"(ya[1]), "+v"(ya[2]), "+v"(ya[3]), "+v"(ya[4]), "+v"(ya[5]), "+v"(ya[6]), "+v"(ya[7]));
        asm volatile("" : "+v"(x[0]), "+v"(x[1]), "+v"(x[2]), "+v"(x[3]), "+v"(x[4]), "+v"(x[5]), "+v"(x[6]), "+v"(x[7]));
        asm volatile("" : "+v"(n0[0]), "+v"(n0[1]), "+v"(n0[2]), "+v"(n0[3]), "+v"(n0[4]), "+v"(n0[5]), "+v"(n0[6]), "+v"(n0[7]));
        SCHED_FENCE();
        {
            bool ok = valid16(cg);
            for (u32 it = 0; !__all(ok); ++it) {
                if (spin_give_up(lds, it * 4 + 3, GLB_SPIN_CAP * 4)) break;
                asm volatile("" ::: "memory");
                if (!ok) { cg = __builtin_amdgcn_raw_buffer_load_b128(rs, cvoff, csoff, 16); ok = valid16(cg); }
            }
        }
        STAMP(12 + 8 * k);
        const u32 cgp[4] = {cg.x, cg.y, cg.z, cg.w};
        dot_rows(x, cgp, n0);
        if (k == 2) {
#pragma unroll
            for (int t = 0; t < 8; t += 2) {
                const u32 o = pack_bf(x[t], x[t + 1]);
                __builtin_amdgcn_raw_buffer_store_b16((u16)o, ro, i * 2 + t * (D_ * 2), myr * (8 * D_ * 2), OUT_AUX);
                __builtin_amdgcn_raw_buffer_store_b16((u16)(o >> 16), ro, i * 2 + (t + 1) * (D_ * 2), myr * (8 * D_ * 2), OUT_AUX);
            }
            break;
        }
        if (k == 0) {
            yap = cg;
        } else {
            sub_rows(y, x, piv);
            row_stats(y, sel_lo, sel_hi, lds, red_rd0, red_wr0, dm, rsd, [] {});
            norm_rows(y, rsd, dm * rsd);
            fma_rows(x, y, ya);
            piv += dm;
        }
        sub_rows(y, x, piv);
        WAIT_VM0();
        auto tail = [&](auto npre_c) {
            constexpr int NPRE = decltype(npre_c)::value;
            const u32 gsoff = WS_U + k * U_SIZE;
            const u32 gvoff = (32 * (w & 1) + (l >> 1)) * U_PITCH + (2 * me + (l & 1)) * 16;
            v4u d[4];
            row_stats(y, sel_lo, sel_hi, lds, red_rd1, red_wr1, dm, rsd, [&] {
                if (NPRE >= 1) d[0] = __builtin_amdgcn_raw_buffer_load_b128(rs, gvoff, gsoff, 16);
                SCHED_FENCE();
            });
            norm_rows(y, rsd, dm * rsd);
            v4u uo;
            uo.x = no_poison(pack_bf(y[7], y[6]));
            uo.y = no_poison(pack_bf(y[5], y[4]));
            uo.z = no_poison(pack_bf(y[3], y[2]));
            uo.w = no_poison(pack_bf(y[1], y[0]));
            SCHED_FENCE();
#pragma unroll
            for (int t = 1; t < NPRE; ++t) d[t] = __builtin_amdgcn_raw_buffer_load_b128(rs, gvoff + t * 64 * U_PITCH, gsoff, 16);
            SCHED_FENCE();
            store16(uo, rs, myr * U_PITCH + i * 16, WS_U + k * U_SIZE);
            const u32 ustore_t = NPRE >= 0 ? now_ticks() : 0u;
            piv += dm;
            *(v4f*)(lds + L_PARK + i * 16) = (v4f){x[0], x[1], x[2], x[3]};
            *(v4f*)(lds + L_PARK + 8192 + i * 16) = (v4f){x[4], x[5], x[6], x[7]};
            *(v4u*)(lds + L_PARK + 16384 + i * 16) = yap;
            *(float*)(lds + L_PARK + 24576 + i * 4) = piv;
            STAMP(13 + 8 * k);
            if (NPRE >= 0) {
                __builtin_amdgcn_s_setprio(3);
                unsigned char* dst = lds + L_UR + (l & 1) * UR_PITCH + 2 * (2040 - 8 * (32 * (w & 1) + (l >> 1)));
#pragma unroll
                for (int t = 0; t < 4; ++t) {
                    const int q = 2 * t + (w & 1);
                    if (t == NPRE) {
                        wait_until(ustore_t, U_DELAY);
#pragma unroll
                        for (int u = t; u < 4; ++u) {
                            d[u] = poison4;
                            if (2 * u + (w & 1) <= mytile + GATHER_AHEAD) d[u] = __builtin_amdgcn_raw_buffer_load_b128(rs, gvoff + u * 64 * U_PITCH, gsoff, 16);
                        }
                    }
                    if (__builtin_expect(!__all(valid16(d[t])), 0)) {
                        for (u32 it = 0; !__all(valid16(d[t])); ++it) {
                            if (spin_give_up(lds, it * 4 + 3, GLB_SPIN_CAP * 4)) break;
                            asm volatile("" ::: "memory");
                            if (!valid16(d[t])) d[t] = __builtin_amdgcn_raw_buffer_load_b128(rs, gvoff + t * 64 * U_PITCH, gsoff, 16);
                        }
                    }
                    *(v4u*)(dst - t * 1024) = d[t];
                    lds_flag_set(SY_INTILE + 4 * q, (u32)(k + 2));
                    if (t == 0) STAMP(16 + 8 * k);
                    if (t == 3) STAMP(17 + 8 * k);
                }
                WAIT_VM0();
                __builtin_amdgcn_s_setprio(2);
            }
        };
        if (w == 4 || w == 5) {
            if (mytile >= 6) tail(std::integral_constant<int, 3>());
            else if (mytile >= 4) tail(std::integral_constant<int, 2>());
            else if (mytile >= 2) tail(std::integral_constant<int, 1>());
            else tail(std::integral_constant<int, 0>());
        } else {
            tail(std::integral_constant<int, -1>());
        }
    }
    asm volatile(".Lh3s4_end:");
}

extern "C" void kernel_launch(void* const* d_in, const int* in_sizes, int n_in, void* d_out, int out_size, void* d_ws, size_t ws_size,
                              hipStream_t stream) {
    (void)in_sizes; (void)n_in; (void)out_size; (void)ws_size;
    h3s4_kernel<<<256, 512, 0, stream>>>(
        (const u16*)d_in[0], (const u16*)d_in[1], (const u16*)d_in[2], (const u16*)d_in[3], (const u16*)d_in[4], (const u16*)d_in[5],
        (const u16*)d_in[7], (const u16*)d_in[8], (const u16*)d_in[9], (const u16*)d_in[10], (const u16*)d_in[11], (const u16*)d_in[12],
        (const u16*)d_in[13], (const u16*)d_in[14], (u16*)d_out, (unsigned char*)d_ws);
}
```

```cpp
#include <hip/hip_runtime.h>
#include <stdint.h>
#include <type_traits>

typedef unsigned short u16;
typedef unsigned u32;
typedef u32 v4u __attribute__((ext_vector_type(4)));
typedef float v2f __attribute__((ext_vector_type(2)));
typedef float v4f __attribute__((ext_vector_type(4)));
typedef __bf16 v2bf __attribute__((ext_vector_type(2)));
typedef __bf16 v8bf __attribute__((ext_vector_type(8)));
typedef _Float16 v2h __attribute__((ext_vector_type(2)));
typedef _Float16 v8h __attribute__((ext_vector_type(8)));

constexpr int S_ = 2048, D_ = 512, NS_ = 128;
constexpr u32 POISON = 0xFFFFFFFFu;

constexpr u32 WS_EPOCH = 0;
constexpr u32 WS_FLAGS = 4096;
constexpr u32 KV_PITCH = 4096 + 128;
constexpr u32 WS_KV = WS_FLAGS + 256 * 128 + 4096;
constexpr u32 C_PITCH = 32768 + 128;
constexpr u32 C_SIZE = 64 * C_PITCH;
constexpr u32 WS_C = WS_KV + 512 * KV_PITCH;
constexpr u32 U_PITCH = 8192 + 128;
constexpr u32 U_SIZE = 256 * U_PITCH;
constexpr u32 WS_U = WS_C + 3 * C_SIZE;
constexpr u32 WS_TOTAL = WS_U + 2 * U_SIZE;

constexpr int UR_PITCH = 4608;
constexpr int L_UR = 0;
constexpr int PART_CH = 2048 + 64;
constexpr int PART_TILE = 2 * PART_CH;
constexpr int L_PART = L_UR + 2 * UR_PITCH;
constexpr int KC_COPY = 4160;
constexpr int KC_CH = 2 * KC_COPY;
constexpr int L_KA = L_PART + 8 * PART_TILE;
constexpr int L_KB = L_KA + 2 * KC_CH;
constexpr int KT_CH = 648 * 4;
constexpr int L_KT = L_KB + 2 * KC_CH;
constexpr int TAB_ROW = 528;
constexpr int TAB_CH = 57 * TAB_ROW;
constexpr int L_TAB = L_KT + 2 * KT_CH;
constexpr int L_T = L_TAB + 2 * TAB_CH;
constexpr int L_XN = L_T;
constexpr int L_PARK = L_TAB;
constexpr int L_RED = L_T + 16384;
constexpr int L_SYNC = L_RED + 2 * 512;
constexpr int SY_PART = L_SYNC;
constexpr int SY_INTILE = L_SYNC + 128;
constexpr int SY_KV = L_SYNC + 160;
constexpr int SY_GO = L_SYNC + 164;
constexpr int SY_TAB = L_SYNC + 168;
constexpr int SY_KA = L_SYNC + 176;
constexpr int SY_TAGSET = L_SYNC + 180;
constexpr int SY_KREADY = L_SYNC + 184;
constexpr int SY_ABORT = L_SYNC + 188;
constexpr int SY_TAG = L_SYNC + 192;
constexpr int SY_ALLF = L_SYNC + 196;
constexpr int SY_PUB = L_SYNC + 200;
constexpr int SY_X0 = L_SYNC + 204;
constexpr int SY_POIS = L_SYNC + 208;
constexpr int SY_PUBT = L_SYNC + 212;
constexpr int L_DCO = L_SYNC + 224;
constexpr int L_PIV = L_DCO + 32;
constexpr int LDS_BYTES = L_PIV + 32;
static_assert(LDS_BYTES > 82 * 1024 && LDS_BYTES <= 160 * 1024, "one workgroup per CU");

constexpr u32 C_DELAY = 65;
constexpr u32 U_DELAY = 55;
constexpr u32 ALLF_DELAY = 30;
constexpr int GATHER_AHEAD = 1;
constexpr int KLOAD_SPLIT = 5;
constexpr u32 LDS_SPIN_CAP = 1u << 19;
constexpr u32 GLB_SPIN_CAP = 1u << 16;

#define SCHED_FENCE() __builtin_amdgcn_sched_barrier(0)
#define WAIT_VM0() do { asm volatile("" ::: "memory"); __builtin_amdgcn_s_waitcnt(0x0F70); asm volatile("" ::: "memory"); } while (0)

__device__ __forceinline__ float bf_lo(u32 x) { return __builtin_bit_cast(float, x << 16); }
__device__ __forceinline__ float bf_hi(u32 x) { return __builtin_bit_cast(float, x & 0xFFFF0000u); }
__device__ __forceinline__ u32 pack_bf(float lo, float hi) {
    v2f f = {lo, hi};
    return __builtin_bit_cast(u32, __builtin_convertvector(f, v2bf));
}
__device__ __forceinline__ u32 pack_h(v2f f) { return __builtin_bit_cast(u32, __builtin_convertvector(f, v2h)); }
__device__ __forceinline__ u32 no_poison(u32 x) { return x < 0xFFFFFFFEu ? x : 0xFFFFFFFEu; }
template <int CTRL> __device__ __forceinline__ float dppf(float x) {
    return __builtin_bit_cast(float, __builtin_amdgcn_mov_dpp(__builtin_bit_cast(int, x), CTRL, 0xf, 0xf, true));
}
template <int CTRL> __device__ __forceinline__ u32 dppu(u32 x) { return (u32)__builtin_amdgcn_mov_dpp((int)x, CTRL, 0xf, 0xf, true); }
__device__ __forceinline__ float f_of(u32 x) { return __builtin_bit_cast(float, x); }
__device__ __forceinline__ u32 u_of(float x) { return __builtin_bit_cast(u32, x); }

__device__ __forceinline__ u32 lds_ld(unsigned char* lds, int off) {
    return __hip_atomic_load((u32*)(lds + off), __ATOMIC_RELAXED, __HIP_MEMORY_SCOPE_WORKGROUP);
}
__device__ __forceinline__ void lds_st(unsigned char* lds, int off, u32 v) {
    __hip_atomic_store((u32*)(lds + off), v, __ATOMIC_RELAXED, __HIP_MEMORY_SCOPE_WORKGROUP);
}
__device__ __forceinline__ void lds_flag_set(int off, u32 v) {
    asm volatile("s_mov_b64 exec, 1\n\tds_write_b32 %0, %1\n\ts_mov_b64 exec, -1" ::"v"(off), "v"(v) : "memory");
}
template <int Q> __device__ __forceinline__ void put_partial(u32 vpart, const v4f& acc) {
    asm volatile("ds_write_b128 %0, %1 offset:%c2" ::"v"(vpart), "v"(acc), "i"(Q * PART_TILE) : "memory");
}
template <int Q> __device__ __forceinline__ void put_partial_flag(u32 vflag, u32 v) {
    asm volatile("ds_write_b32 %0, %1 offset:%c2" ::"v"(vflag), "v"(v), "i"(Q * 16) : "memory");
}
__device__ __forceinline__ void lds_flag_inc(int off) {
    asm volatile("s_mov_b64 exec, 1\n\tds_add_u32 %0, %1\n\ts_mov_b64 exec, -1" ::"v"(off), "v"(1u) : "memory");
}
__device__ __forceinline__ bool spin_give_up(unsigned char* lds, u32 it, u32 cap) {
    if ((it & 63u) != 63u) return false;
    if (it > cap) { lds_st(lds, SY_ABORT, 1u); return true; }
    return __builtin_amdgcn_readfirstlane(lds_ld(lds, SY_ABORT)) != 0;
}
__device__ __forceinline__ void lds_wait_ge(unsigned char* lds, int off, u32 target) {
    if (__builtin_expect((u32)__builtin_amdgcn_readfirstlane(lds_ld(lds, off)) < target, 0)) {
        for (u32 it = 0; (u32)__builtin_amdgcn_readfirstlane(lds_ld(lds, off)) < target; ++it)
            if (spin_give_up(lds, it, LDS_SPIN_CAP)) break;
    }
    asm volatile("" ::: "memory");
}
__device__ __forceinline__ void lds_wait_ge_slow(unsigned char* lds, int off, u32 target) {
    for (u32 it = 0; (u32)__builtin_amdgcn_readfirstlane(lds_ld(lds, off)) < target; ++it) {
        if (spin_give_up(lds, it, LDS_SPIN_CAP)) break;
        __builtin_amdgcn_s_sleep(1);
    }
    asm volatile("" ::: "memory");
}
__device__ __forceinline__ void store16(v4u v, __amdgpu_buffer_rsrc_t rs, u32 voff, u32 soff) {
    __builtin_amdgcn_raw_buffer_store_b128(v, rs, voff + soff, 0, 16);
}
__device__ __forceinline__ u32 now_ticks() { return (u32)__builtin_amdgcn_s_memrealtime(); }
__device__ __forceinline__ void wait_until(u32 t0, u32 delay) {
    for (u32 it = 0; now_ticks() - t0 < delay && it < 4096; ++it) __builtin_amdgcn_s_sleep(1);
}
__device__ __forceinline__ bool valid16(v4u r) {
    u32 m = r.x > r.y ? r.x : r.y;
    m = m > r.z ? m : r.z;
    m = m > r.w ? m : r.w;
    return m != POISON;
}
__device__ __forceinline__ float vmul(float a, float b) {
    float r;
    asm("v_mul_f32 %0, %1, %2" : "=v"(r) : "v"(a), "v"(b));
    return r;
}
#define DPPB(t) " row_newbcast:" #t " row_mask:0xf bank_mask:0xf\n\t"
__device__ __forceinline__ void sub_rows(float (&y)[8], const float (&x)[8], float p) {
    asm("s_nop 1\n\t"
        "v_subrev_f32_dpp %0, %16, %8" DPPB(0) "v_subrev_f32_dpp %1, %16, %9" DPPB(1) "v_subrev_f32_dpp %2, %16, %10" DPPB(2)
        "v_subrev_f32_dpp %3, %16, %11" DPPB(3) "v_subrev_f32_dpp %4, %16, %12" DPPB(4) "v_subrev_f32_dpp %5, %16, %13" DPPB(5)
        "v_subrev_f32_dpp %6, %16, %14" DPPB(6) "v_subrev_f32_dpp %7, %16, %15" DPPB(7)
        : "=&v"(y[0]), "=&v"(y[1]), "=&v"(y[2]), "=&v"(y[3]), "=&v"(y[4]), "=&v"(y[5]), "=&v"(y[6]), "=&v"(y[7])
        : "v"(x[0]), "v"(x[1]), "v"(x[2]), "v"(x[3]), "v"(x[4]), "v"(x[5]), "v"(x[6]), "v"(x[7]), "v"(p));
}
__device__ __forceinline__ void norm_rows(float (&y)[8], float r, float m) {
    asm("s_nop 1\n\t"
        "v_mul_f32_dpp %0, %8, %0" DPPB(0) "v_mul_f32_dpp %1, %8, %1" DPPB(1) "v_mul_f32_dpp %2, %8, %2" DPPB(2) "v_mul_f32_dpp %3, %8, %3" DPPB(3)
        "v_mul_f32_dpp %4, %8, %4" DPPB(4) "v_mul_f32_dpp %5, %8, %5" DPPB(5) "v_mul_f32_dpp %6, %8, %6" DPPB(6) "v_mul_f32_dpp %7, %8, %7" DPPB(7)
        "v_subrev_f32_dpp %0, %9, %0" DPPB(0) "v_subrev_f32_dpp %1, %9, %1" DPPB(1) "v_subrev_f32_dpp %2, %9, %2" DPPB(2)
        "v_subrev_f32_dpp %3, %9, %3" DPPB(3) "v_subrev_f32_dpp %4, %9, %4" DPPB(4) "v_subrev_f32_dpp %5, %9, %5" DPPB(5)
        "v_subrev_f32_dpp %6, %9, %6" DPPB(6) "v_subrev_f32_dpp %7, %9, %7" DPPB(7)
        : "+v"(y[0]), "+v"(y[1]), "+v"(y[2]), "+v"(y[3]), "+v"(y[4]), "+v"(y[5]), "+v"(y[6]), "+v"(y[7])
        : "v"(r), "v"(m));
}
#undef DPPB
__device__ __forceinline__ void dot_rows(float (&x)[8], const u32 (&p)[4], const u32 (&m)[8]) {
    asm("v_dot2_f32_bf16 %0, %8, %12, %0\n\tv_dot2_f32_bf16 %1, %8, %13, %1\n\t"
        "v_dot2_f32_bf16 %2, %9, %14, %2\n\tv_dot2_f32_bf16 %3, %9, %15, %3\n\t"
        "v_dot2_f32_bf16 %4, %10, %16, %4\n\tv_dot2_f32_bf16 %5, %10, %17, %5\n\t"
        "v_dot2_f32_bf16 %6, %11, %18, %6\n\tv_dot2_f32_bf16 %7, %11, %19, %7\n\ts_nop 2"
        : "+v"(x[0]), "+v"(x[1]), "+v"(x[2]), "+v"(x[3]), "+v"(x[4]), "+v"(x[5]), "+v"(x[6]), "+v"(x[7])
        : "v"(p[0]), "v"(p[1]), "v"(p[2]), "v"(p[3]), "v"(m[0]), "v"(m[1]), "v"(m[2]), "v"(m[3]), "v"(m[4]), "v"(m[5]), "v"(m[6]), "v"(m[7]));
}
__device__ __forceinline__ void fma_rows(float (&x)[8], const float (&a)[8], const float (&b)[8]) {
    asm("v_fmac_f32 %0, %8, %16\n\tv_fmac_f32 %1, %9, %17\n\tv_fmac_f32 %2, %10, %18\n\tv_fmac_f32 %3, %11, %19\n\t"
        "v_fmac_f32 %4, %12, %20\n\tv_fmac_f32 %5, %13, %21\n\tv_fmac_f32 %6, %14, %22\n\tv_fmac_f32 %7, %15, %23"
        : "+v"(x[0]), "+v"(x[1]), "+v"(x[2]), "+v"(x[3]), "+v"(x[4]), "+v"(x[5]), "+v"(x[6]), "+v"(x[7])
        : "v"(a[0]), "v"(a[1]), "v"(a[2]), "v"(a[3]), "v"(a[4]), "v"(a[5]), "v"(a[6]), "v"(a[7]),
          "v"(b[0]), "v"(b[1]), "v"(b[2]), "v"(b[3]), "v"(b[4]), "v"(b[5]), "v"(b[6]), "v"(b[7]));
}
__device__ __forceinline__ float vfma(float a, float b, float c) {
    float r;
    asm("v_fma_f32 %0, %1, %2, %3" : "=v"(r) : "v"(a), "v"(b), "v"(c));
    return r;
}
__device__ __forceinline__ float vadd(float a, float b) {
    float r;
    asm("v_add_f32 %0, %1, %2" : "=v"(r) : "v"(a), "v"(b));
    return r;
}

template <class Hook>
__device__ __forceinline__ void row_stats(const float (&y)[8], const v4u& sel_lo, const v4u& sel_hi, unsigned char* lds, u32 red_rd, u32 red_wr,
                                          float& dmean, float& rstd, Hook after_barrier) {
    const v4u a = {pack_bf(y[0], y[1]), pack_bf(y[2], y[3]), pack_bf(y[4], y[5]), pack_bf(y[6], y[7])};
    float q[8];
    asm("v_mul_f32 %0, %8, %8\n\tv_mul_f32 %1, %9, %9\n\tv_mul_f32 %2, %10, %10\n\tv_mul_f32 %3, %11, %11\n\t"
        "v_mul_f32 %4, %12, %12\n\tv_mul_f32 %5, %13, %13\n\tv_mul_f32 %6, %14, %14\n\tv_mul_f32 %7, %15, %15"
        : "=&v"(q[0]), "=&v"(q[1]), "=&v"(q[2]), "=&v"(q[3]), "=&v"(q[4]), "=&v"(q[5]), "=&v"(q[6]), "=&v"(q[7])
        : "v"(y[0]), "v"(y[1]), "v"(y[2]), "v"(y[3]), "v"(y[4]), "v"(y[5]), "v"(y[6]), "v"(y[7]));
    const v4u s = {pack_bf(q[0], q[1]), pack_bf(q[2], q[3]), pack_bf(q[4], q[5]), pack_bf(q[6], q[7])};
    v4f d = {0, 0, 0, 0};
    d = __builtin_amdgcn_mfma_f32_16x16x32_bf16(__builtin_bit_cast(v8bf, a), __builtin_bit_cast(v8bf, sel_lo), d, 0, 0, 0);
    d = __builtin_amdgcn_mfma_f32_16x16x32_bf16(__builtin_bit_cast(v8bf, s), __builtin_bit_cast(v8bf, sel_hi), d, 0, 0, 0);
    u32 t0 = u_of((d.x + d.z) + (d.y + d.w)), t1 = t0;
    {
        auto r = __builtin_amdgcn_permlane16_swap(t0, t1, false, false);
        const u32 r0 = r[0], r1 = r[1];
        t0 = u_of(f_of(r0) + f_of(r1)); t1 = t0;
    }
    {
        auto r = __builtin_amdgcn_permlane32_swap(t0, t1, false, false);
        const u32 r0 = r[0], r1 = r[1];
        t0 = u_of(f_of(r0) + f_of(r1));
    }
    *(u32*)(lds + red_wr) = t0;
    __syncthreads();
    after_barrier();
    const v4f p0 = *(const v4f*)(lds + red_rd), p1 = *(const v4f*)(lds + red_rd + 16);
    const v4f pp = p0 + p1;
    const float tot = (pp.x + pp.z) + (pp.y + pp.w);
    const float s2 = dppf<0x128>(tot);
    dmean = tot * (1.0f / D_);
    const float var = fmaxf(s2 * (1.0f / D_) - dmean * dmean, 0.0f);
    rstd = __builtin_amdgcn_rsqf(var + 1e-5f);
}
__device__ __forceinline__ float wave_sum(float v) {
    asm("s_nop 1" : "+v"(v));
    v += dppf<0x128>(v);
    v += dppf<0x141>(v);
    v += dppf<0xB1>(v);
    v += dppf<0x4E>(v);
    u32 a = u_of(v), b = a;
    {
        auto r = __builtin_amdgcn_permlane16_swap(a, b, false, false);
        const u32 r0 = r[0], r1 = r[1];
        a = u_of(f_of(r0) + f_of(r1)); b = a;
    }
    auto r = __builtin_amdgcn_permlane32_swap(a, b, false, false);
    const u32 r0 = r[0], r1 = r[1];
    return f_of(r0) + f_of(r1);
}

__device__ __forceinline__ void load_afrags(v4u (&A)[8][4], unsigned char* lds, int region, int ch, int h, int l) {
    const int b = l & 15, g = l >> 4, par = (1 + b) & 1;
    const unsigned char* base = lds + region + ch * KC_CH + par * KC_COPY + 256 * h + 16 * g + 2 * (1 + b - par);
#pragma unroll
    for (int p = 0; p < 8; ++p)
#pragma unroll
        for (int r = 0; r < 4; ++r) {
            const u32* q = (const u32*)(base + 512 * p + 64 * r);
            A[p][r] = (v4u){q[0], q[1], q[2], q[3]};
        }
}

__device__ __forceinline__ v2f cexp_pow(float al, float be, float m) {
    const float e = __builtin_amdgcn_exp2f(al * m);
    const float ph = __builtin_amdgcn_fractf(be * m);
    return (v2f){e * __builtin_amdgcn_cosf(ph), e * __builtin_amdgcn_sinf(ph)};
}
__device__ __forceinline__ v2f cmul(v2f a, v2f b) {
    v2f t;
    asm("v_pk_mul_f32 %0, %1, %2 op_sel:[0,0] op_sel_hi:[1,0]\n\t"
        "v_pk_fma_f32 %0, %1, %2, %0 op_sel:[1,1,0] op_sel_hi:[0,1,1] neg_lo:[1,0,0]"
        : "=&v"(t) : "v"(a), "v"(b));
    return t;
}
extern "C" __global__ void __launch_bounds__(512, 2)
h3s4_kernel(const u16* __restrict__ q_, const u16* __restrict__ k_, const u16* __restrict__ v_, const u16* __restrict__ shk_,
            const u16* __restrict__ ssmk_, const u16* __restrict__ ssmD_, const u16* __restrict__ Are_, const u16* __restrict__ Aim_,
            const u16* __restrict__ Bre_, const u16* __restrict__ Bim_, const u16* __restrict__ Cre_, const u16* __restrict__ Cim_,
            const u16* __restrict__ logdt_, const u16* __restrict__ Dskip_, u16* __restrict__ out_, unsigned char* ws_) {
    __shared__ __attribute__((aligned(16))) unsigned char lds[LDS_BYTES];
    asm volatile("" ::"s"(q_), "s"(k_), "s"(v_), "s"(shk_), "s"(ssmk_), "s"(ssmD_), "s"(Are_), "s"(Aim_), "s"(Bre_), "s"(Bim_),
                 "s"(Cre_), "s"(Cim_), "s"(logdt_), "s"(Dskip_), "s"(out_), "s"(ws_));
    const int i = threadIdx.x, l = i & 63;
    const int w = __builtin_amdgcn_readfirstlane(i >> 6);
    u32 code_touch = 0;
    if (w == 0) {
        unsigned long long pc;
        u32 left;
        asm volatile("s_getpc_b64 %0\n.Lh3s4_pc:\n\ts_mov_b32 %1, .Lh3s4_end-.Lh3s4_pc+1280" : "=s"(pc), "=s"(left));
        const u32 nl = (left >> 12) + 1, off = ((blockIdx.x >> 3) * nl + l) * 128;
        if ((u32)l < nl && off + 4 <= left) code_touch = *(const __attribute__((address_space(1))) u32*)((pc & ~3ull) + off);
    }
    const int xcd = blockIdx.x & 7, idx = blockIdx.x >> 3;
    const int me = xcd * 32 + idx;
    const int mytile = idx >> 2;
    const int myr = 32 * mytile + 4 * xcd + (idx & 3);
    const __amdgpu_buffer_rsrc_t rs = __builtin_amdgcn_make_buffer_rsrc(ws_, 0, WS_TOTAL, 0x00020000);
    const v4u poison4 = {POISON, POISON, POISON, POISON};

    const int cp = i & 31, rb = i >> 5;
    const int pcg = (xcd + idx) & 7;
    const int R0 = 64 * idx + 4 * rb, pch = 64 * pcg + 2 * cp;
    u32 kd[7], vd[4];
#pragma unroll
    for (int j = 0; j < 7; ++j) {
        const int Rj = R0 - 3 + j;
        kd[j] = *(const u32*)(k_ + (Rj < 0 ? 0 : Rj) * D_ + pch);
    }
#pragma unroll
    for (int j = 0; j < 4; ++j) vd[j] = *(const u32*)(v_ + (R0 + j) * D_ + pch);
    const v4u wd = *(const v4u*)(shk_ + pch * 4);
    u32 epoch = 0;
    if (w == 0) epoch = __builtin_amdgcn_raw_buffer_load_b32(rs, 0, WS_EPOCH, 16);
    v4u xq[4];

    if (i >= 256 && i < 256 + 64) *(v4u*)(lds + L_UR + ((i >> 5) & 1) * UR_PITCH + 4096 + (i & 31) * 16) = (v4u){0, 0, 0, 0};
    if (i >= 320 && i < 320 + 16) {
        const int e = i - 320;
        *(v4u*)(lds + L_KA + (e >> 1) * KC_COPY + (e & 1) * 16) = (v4u){0, 0, 0, 0};
    }
    if (i >= 384 && i < 384 + 56) *(u32*)(lds + L_SYNC + (i - 384) * 4) = 0;

    {
#pragma unroll
        for (int j = 0; j < 3; ++j) if (R0 - 3 + j < 0) kd[j] = 0;
#pragma unroll
        for (int c = 0; c < 2; ++c) {
            const u32 sel = c ? 0x07060302u : 0x05040100u;
            const u32 w01 = wd[2 * c], w23 = wd[2 * c + 1];
            u32 P[7];
#pragma unroll
            for (int j = 1; j < 7; ++j) P[j] = __builtin_amdgcn_perm(kd[j - 1], kd[j], sel);
            float res[4];
#pragma unroll
            for (int t = 0; t < 4; ++t) {
                float a = __builtin_amdgcn_fdot2_f32_bf16(__builtin_bit_cast(v2bf, P[t + 3]), __builtin_bit_cast(v2bf, w01), 0.0f, false);
                a = __builtin_amdgcn_fdot2_f32_bf16(__builtin_bit_cast(v2bf, P[t + 1]), __builtin_bit_cast(v2bf, w23), a, false);
                res[t] = a * (c ? bf_hi(vd[t]) : bf_lo(vd[t]));
            }
            typedef u32 v2u __attribute__((ext_vector_type(2)));
            const int half = (1 - (rb & 1)) ^ ((cp >> 3) & 1);
            *(v2u*)(lds + L_T + (2 * cp + c) * 128 + (((rb >> 1) ^ (cp & 7)) * 16) + half * 8) = (v2u){pack_bf(res[3], res[2]), pack_bf(res[1], res[0])};
        }
    }
    SCHED_FENCE();
    if (w < 4) {
#pragma unroll
        for (int j = 0; j < 2; ++j) {
            const int e = i + 256 * j;
            store16(poison4, rs, (e >> 3) * C_PITCH + me * 128 + (e & 7) * 16, WS_C);
        }
    }
    u32 pp[11], dA, dB;
    if (w >= 4) {
        const int pc = 2 * me + ((w - 4) >> 1), pi = pc * NS_ + (i & 127), pj = pi ^ 64;
        pp[0] = Are_[pi]; pp[1] = Aim_[pi]; pp[2] = Bre_[pi]; pp[3] = Bim_[pi]; pp[4] = Cre_[pi]; pp[5] = Cim_[pi];
        pp[6] = Bre_[pj]; pp[7] = Bim_[pj]; pp[8] = Cre_[pj]; pp[9] = Cim_[pj];
        pp[10] = logdt_[pc];
        dA = ssmD_[2 * me + (l & 1)]; dB = Dskip_[2 * me + (l & 1)];
        if (w >= 6) {
#pragma unroll
            for (int j = 0; j < 4; ++j) xq[j] = *(const v4u*)(q_ + (8 * myr + 4 * (w & 1) + j) * D_ + l * 8);
        }
    }
    __syncthreads();

    v4u A[8][4];
    if (w < 4) {
#pragma unroll
        for (int j = 0; j < 2; ++j) {
            typedef u32 v2u __attribute__((ext_vector_type(2)));
            const int e = i + 256 * j, ch = e >> 3, chunk = e & 7, sw = (ch >> 4) & 1;
            const unsigned char* src = lds + L_T + ch * 128 + ((chunk ^ ((ch >> 1) & 7)) * 16);
            const v2u d0 = *(const v2u*)(src + 8 * sw), d1 = *(const v2u*)(src + 8 * (1 - sw));
            store16((v4u){d0.x, d0.y, d1.x, d1.y}, rs, (64 * pcg + ch) * KV_PITCH + (8 * idx + chunk) * 16, WS_KV);
        }
        SCHED_FENCE();
        asm volatile("" ::: "memory");
        v4u sk[3];
        u32 sknext[3];
#pragma unroll
        for (int j = 0; j < 3; ++j) {
            const int e = (i - 64) + 192 * j, ec = (w == 0 || e > 511) ? 0 : e;
            const u16* src = ssmk_ + (2 * me + (ec >> 8)) * S_ + (ec & 255) * 8;
            if (w != 0) {
                sk[j] = *(const v4u*)src;
                sknext[j] = *(const u32*)(src + ((ec & 255) < 255 ? 8 : 0));
            }
        }
        SCHED_FENCE();
        if (w == 0) asm volatile("s_waitcnt vmcnt(0)" ::: "memory");
        else asm volatile("s_waitcnt vmcnt(6)" ::: "memory");
        lds_flag_inc(SY_KV);
        u32 tag = 0;
        if (w == 0) {
            lds_wait_ge(lds, SY_KV, 4);
            tag = __builtin_amdgcn_readfirstlane(epoch) + 1u;
            if (l == 0) __builtin_amdgcn_raw_buffer_store_b32(tag, rs, me * 128, WS_FLAGS, 16);
            lds_flag_set(SY_TAG, tag);
            lds_flag_set(SY_TAGSET, 1u);
        }
        if (w != 0) {
#pragma unroll
            for (int j = 0; j < 3; ++j) {
                const int e = (i - 64) + 192 * j, sm = e & 255;
                if (e < 512) {
                    unsigned char* c0 = lds + L_KA + (e >> 8) * KC_CH;
                    const u32 nx = sm < 255 ? sknext[j] : 0u;
                    *(v4u*)(c0 + 32 + sm * 16) = sk[j];
                    *(v4u*)(c0 + KC_COPY + 32 + sm * 16) =
                        (v4u){__builtin_amdgcn_alignbit(sk[j].y, sk[j].x, 16), __builtin_amdgcn_alignbit(sk[j].z, sk[j].y, 16),
                              __builtin_amdgcn_alignbit(sk[j].w, sk[j].z, 16), __builtin_amdgcn_alignbit(nx, sk[j].w, 16)};
                    if (sm == 0) *(u32*)(c0 + KC_COPY + 28) = sk[j].x << 16;
                }
            }
            lds_flag_inc(SY_KA);
        }
        if (w == 0) {
            const u32 foff = (((xcd - (l & 31)) & 7) * 32 + (l & 31)) * 128;
            for (u32 it = 0;; ++it) {
                asm volatile("" ::: "memory");
                if (__all(__builtin_amdgcn_raw_buffer_load_b32(rs, foff, WS_FLAGS, 16) == tag)) break;
                if (spin_give_up(lds, it * 4 + 3, GLB_SPIN_CAP * 4)) break;
            }
            lds_flag_set(SY_GO, 1u);
        } else {
            lds_wait_ge(lds, SY_KA, 3);
            load_afrags(A, lds, L_KA, w >> 1, w & 1, l);
            lds_wait_ge(lds, SY_GO, 1u);
        }
        v4u g0, g1;
        {
            const u32 off = (2 * me + (l >> 5)) * KV_PITCH + (64 * w + (l & 31)) * 16;
            g0 = __builtin_amdgcn_raw_buffer_load_b128(rs, off, WS_KV, 16);
            g1 = __builtin_amdgcn_raw_buffer_load_b128(rs, off + 512, WS_KV, 16);
        }
        SCHED_FENCE();
        if (w == 0) {
            lds_wait_ge(lds, SY_KA, 3);
            load_afrags(A, lds, L_KA, 0, 0, l);
        }
        unsigned char* dst = lds + L_UR + (l >> 5) * UR_PITCH + 2 * (2040 - 8 * (64 * w + (l & 31)));
        *(v4u*)dst = g0;
        *(v4u*)(dst - 512) = g1;
        lds_flag_set(SY_INTILE + 8 * w, 1u);
        lds_flag_set(SY_INTILE + 8 * w + 4, 1u);
        WAIT_VM0();

    } else {
        __builtin_amdgcn_s_setprio(2);
        const int wa = w - 4, ch = wa >> 1, n = (i - 256) & 127;
#pragma unroll
        for (int j = 0; j < 11; ++j) asm volatile("" : "+v"(pp[j]));
        asm volatile("" : "+v"(dA), "+v"(dB));
        const float ex = expf(f_of(pp[10] << 16));
        const float dt = bf_lo(pack_bf(ex, ex));
        const float al = dt * f_of(pp[0] << 16) * 1.44269504088896341f, be = dt * f_of(pp[1] << 16) * 0.15915494309189535f;
        v2f W;
        float inv;
        {
            const float br = dt * f_of(pp[2] << 16), bi = dt * f_of(pp[3] << 16), cr = f_of(pp[4] << 16), ci = f_of(pp[5] << 16);
            const float br2 = dt * f_of(pp[6] << 16), bi2 = dt * f_of(pp[7] << 16), cr2 = f_of(pp[8] << 16), ci2 = f_of(pp[9] << 16);
            W = (v2f){cr * br - ci * bi, cr * bi + ci * br};
            float mx = fmaxf(fmaxf(fabsf(W.x), fabsf(W.y)), fmaxf(fabsf(cr2 * br2 - ci2 * bi2), fabsf(cr2 * bi2 + ci2 * br2)));
            mx = fmaxf(mx, dppf<0x128>(mx));
            mx = fmaxf(mx, dppf<0x141>(mx));
            mx = fmaxf(mx, dppf<0xB1>(mx));
            mx = fmaxf(mx, dppf<0x4E>(mx));
            float m0 = f_of((u32)__builtin_amdgcn_readlane((int)u_of(mx), 0)), m1 = f_of((u32)__builtin_amdgcn_readlane((int)u_of(mx), 16));
            float m2 = f_of((u32)__builtin_amdgcn_readlane((int)u_of(mx), 32)), m3 = f_of((u32)__builtin_amdgcn_readlane((int)u_of(mx), 48));
            const int e = __builtin_amdgcn_frexp_expf(fmaxf(fmaxf(m0, m1), fmaxf(m2, m3)));
            W = (v2f){__builtin_amdgcn_ldexpf(W.x, 14 - e), __builtin_amdgcn_ldexpf(W.y, 14 - e)};
            inv = __builtin_amdgcn_ldexpf(1.0f, e - 14);
        }
        unsigned char* tab = lds + L_TAB + ch * TAB_CH + n * 4;
#define TAB_PUT(row, val) (*(u32*)(tab + (row) * TAB_ROW) = pack_h(val))
        const v2f E1 = cexp_pow(al, be, 1.0f), E8 = cexp_pow(al, be, 8.0f), E128 = cexp_pow(al, be, 128.0f);
        const v2f E256 = cmul(E128, E128), E384 = cmul(E256, E128), E512 = cmul(E256, E256), E1024 = cmul(E512, E512);
        {
            const v2f E8c = {E8.x, -E8.y};
            v2f B = {W.x, -W.y}, P = {1.0f, 0.0f}, Q = E512, Rr = E1024, P4 = cmul(cmul(E1, E1), cmul(E1, E1));
#pragma unroll
            for (int r = 0; r < 8; ++r) {
                TAB_PUT(41 + 2 * r, B);
                B = cmul(B, E8c);
                TAB_PUT(42 + 2 * r, B);
                if (r < 7) B = cmul(B, E8c);
                TAB_PUT(r, P);
                TAB_PUT(8 + r, r ? cmul(P, E128) : E128);
                if ((r & 1) == 0) {
                    TAB_PUT(16 + (r >> 1), r ? cmul(P, E256) : E256);
                    TAB_PUT(20 + (r >> 1), r ? cmul(P, E384) : E384);
                }
                if (r < 7) P = r ? cmul(P, E1) : E1;
                TAB_PUT(32 + r, Rr);
                if (r < 7) Rr = cmul(Rr, E128);
                if (r < 4) {
                    TAB_PUT(24 + 2 * r, Q);
                    TAB_PUT(25 + 2 * r, cmul(Q, P4));
                    if (r < 3) Q = cmul(Q, E128);
                }
            }
            TAB_PUT(40, cmul(E1024, E1024));
        }
#undef TAB_PUT
        if (l == 0) *(float*)(lds + L_DCO + 16 + ch * 4) = inv;
        lds_flag_inc(SY_TAB + 4 * ch);
        if (l < 2) { *(u32*)(lds + L_DCO + l * 4) = dA << 16; *(u32*)(lds + L_DCO + 8 + l * 4) = dB << 16; }
        if (w < 6) {
            const int kc = w & 1;
            lds_wait_ge(lds, SY_TAB + 4 * kc, 2);
            {
                const int row = l & 15, g = l >> 4;
                const unsigned char* tb = lds + L_TAB + kc * TAB_CH + row * TAB_ROW + g * 16;
                float* kt = (float*)(lds + L_KT + kc * KT_CH);
                const float scale = *(const float*)(lds + L_DCO + 16 + kc * 4);
                v4u fb[8], fa[8];
                v4f acc[3];
#pragma unroll
                for (int s2 = 0; s2 < 8; ++s2) fb[s2] = *(const v4u*)(tb + 41 * TAB_ROW + s2 * 64);
#pragma unroll
                for (int tile = 0; tile < 3; ++tile) {
#pragma unroll
                    for (int s2 = 0; s2 < 8; ++s2) fa[s2] = *(const v4u*)(tb + tile * 16 * TAB_ROW + s2 * 64);
                    acc[tile] = (v4f){0, 0, 0, 0};
#pragma unroll
                    for (int s2 = 0; s2 < 8; ++s2)
                        acc[tile] = __builtin_amdgcn_mfma_f32_16x16x32_f16(__builtin_bit_cast(v8h, fa[s2]), __builtin_bit_cast(v8h, fb[s2]), acc[tile], 0, 0, 0);
                }
                asm volatile("s_nop 7\n\ts_nop 1" : "+v"(acc[0]), "+v"(acc[1]), "+v"(acc[2]));
#pragma unroll
                for (int r = 0; r < 4; ++r) {
                    const int rw = 4 * g + r;
                    kt[128 * (rw >> 3) + (rw & 7) + 8 * row] = vmul(acc[0][r], scale);
                    kt[rw < 8 ? 256 + 64 * (rw >> 2) + (rw & 3) + 4 * row : 384 + 32 * ((rw - 8) >> 1) + (rw & 1) + 2 * row] = vmul(acc[1][r], scale);
                    kt[rw < 8 ? 512 + 16 * rw + row : ((rw == 8 && row == 0) ? 640 : 644 + (l & 3))] = vmul(acc[2][r], scale);
                }
            }
            asm volatile("s_waitcnt lgkmcnt(0)" ::: "memory");
            {
                const float* t = (const float*)(lds + L_KT + kc * KT_CH);
                unsigned char* c0 = lds + L_KB + kc * KC_CH;
                const float a2 = t[512 + l], b2 = t[513 + l], a3 = t[576 + l], b3 = t[577 + l];
                const float v0 = t[384 + 2 * l], v1 = t[385 + 2 * l], v2 = t[386 + 2 * l];
                float kvv[9];
#pragma unroll
                for (int j = 0; j < 9; ++j) {
                    const int lag = 8 * l + j;
                    const u32 bb = pack_bf((float)lag, 0.0f) & 0xFFFFu;
                    kvv[j] = t[l < 32 ? lag : (int)(bb - 0x4280u)];
                }
                {
                    const u32 e0 = pack_bf(kvv[0], kvv[1]);
                    *(v4u*)(c0 + 32 + l * 16) = (v4u){e0, pack_bf(kvv[2], kvv[3]), pack_bf(kvv[4], kvv[5]), pack_bf(kvv[6], kvv[7])};
                    *(v4u*)(c0 + KC_COPY + 32 + l * 16) = (v4u){pack_bf(kvv[1], kvv[2]), pack_bf(kvv[3], kvv[4]), pack_bf(kvv[5], kvv[6]), pack_bf(kvv[7], kvv[8])};
                    if (l == 0) *(u32*)(c0 + KC_COPY + 28) = e0 << 16;
                }
                {
                    const u32 p00 = pack_bf(v0, v0), p01 = pack_bf(v0, v1), p11 = pack_bf(v1, v1), p12 = pack_bf(v1, v2), p22 = pack_bf(v2, v2);
                    *(v4u*)(c0 + 32 + (64 + l) * 16) = (v4u){p00, p01, p11, p22};
                    *(v4u*)(c0 + KC_COPY + 32 + (64 + l) * 16) = (v4u){p00, p11, p12, p22};
                }
#pragma unroll
                for (int it = 0; it < 2; ++it) {
                    const float a = it ? a3 : a2, b = it ? b3 : b2, c4 = (l & 1) ? b : a;
                    const u32 paa = pack_bf(a, a), pbb = pack_bf(b, b);
                    *(v4u*)(c0 + 32 + (128 + 64 * it + l) * 16) = (v4u){paa, paa, pack_bf(c4, b), pbb};
                    *(v4u*)(c0 + KC_COPY + 32 + (128 + 64 * it + l) * 16) = (v4u){paa, pack_bf(a, c4), pbb, pbb};
                }
            }
            lds_flag_inc(SY_KREADY);
            if (w == 4) {
                lds_wait_ge(lds, SY_TAGSET, 1u);
                const u32 tag = __builtin_amdgcn_readfirstlane(lds_ld(lds, SY_TAG));
                lds_wait_ge_slow(lds, SY_GO, 1u);
                wait_until(now_ticks(), ALLF_DELAY);
                for (u32 it = 0;; ++it) {
                    asm volatile("" ::: "memory");
                    if (it) __builtin_amdgcn_s_sleep(8);
                    const u32 f0 = __builtin_amdgcn_raw_buffer_load_b32(rs, l * 128, WS_FLAGS, 16);
                    const u32 f1 = __builtin_amdgcn_raw_buffer_load_b32(rs, (l + 64) * 128, WS_FLAGS, 16);
                    const u32 f2 = __builtin_amdgcn_raw_buffer_load_b32(rs, (l + 128) * 128, WS_FLAGS, 16);
                    const u32 f3 = __builtin_amdgcn_raw_buffer_load_b32(rs, (l + 192) * 128, WS_FLAGS, 16);
                    if (__all(f0 == tag && f1 == tag && f2 == tag && f3 == tag)) break;
                    if (spin_give_up(lds, it * 4 + 3, GLB_SPIN_CAP * 4)) break;
                }
                if (me == 0 && l == 0) __builtin_amdgcn_raw_buffer_store_b32(tag, rs, 0, WS_EPOCH, 16);
                lds_flag_set(SY_ALLF, 1u);
            }
            WAIT_VM0();
        } else {
            const int kr = w & 1;
            lds_wait_ge(lds, SY_KV, 4);
#pragma unroll
            for (int j = 0; j < 4; ++j) store16(poison4, rs, myr * U_PITCH + ((i & 127) + 128 * j) * 16, WS_U);
#pragma unroll
            for (int j = 0; j < 4; ++j) asm volatile("" : "+v"(xq[j]));
            {
                float f[4][8], mean[4], rstd[4];
#pragma unroll
                for (int rr = 0; rr < 4; ++rr) {
                    const v4u xr = xq[rr];
                    f[rr][0] = bf_lo(xr.x); f[rr][1] = bf_hi(xr.x); f[rr][2] = bf_lo(xr.y); f[rr][3] = bf_hi(xr.y);
                    f[rr][4] = bf_lo(xr.z); f[rr][5] = bf_hi(xr.z); f[rr][6] = bf_lo(xr.w); f[rr][7] = bf_hi(xr.w);
                    mean[rr] = vadd(vadd(vadd(f[rr][0], f[rr][1]), vadd(f[rr][2], f[rr][3])), vadd(vadd(f[rr][4], f[rr][5]), vadd(f[rr][6], f[rr][7])));
                }
#pragma unroll
                for (int rr = 0; rr < 4; ++rr) mean[rr] = vmul(wave_sum(mean[rr]), -1.0f / D_);
#pragma unroll
                for (int rr = 0; rr < 4; ++rr) {
#pragma unroll
                    for (int j = 0; j < 8; ++j) f[rr][j] = vadd(f[rr][j], mean[rr]);
                    float q2 = vmul(f[rr][0], f[rr][0]);
#pragma unroll
                    for (int j = 1; j < 8; ++j) q2 = vfma(f[rr][j], f[rr][j], q2);
                    rstd[rr] = q2;
                }
#pragma unroll
                for (int rr = 0; rr < 4; ++rr) rstd[rr] = __builtin_amdgcn_rsqf(vfma(wave_sum(rstd[rr]), 1.0f / D_, 1e-5f));
                asm volatile("s_nop 1" : "+v"(rstd[0]), "+v"(rstd[1]), "+v"(rstd[2]), "+v"(rstd[3]));
#pragma unroll
                for (int rr = 0; rr < 4; ++rr) {
                    const v4u xr = xq[rr];
                    u32 o[8];
#pragma unroll
                    for (int j = 0; j < 4; ++j) {
                        const u32 np = pack_bf(vmul(f[rr][2 * j], rstd[rr]), vmul(f[rr][2 * j + 1], rstd[rr]));
                        o[2 * j] = __builtin_amdgcn_perm(np, xr[j], 0x05040100u);
                        o[2 * j + 1] = __builtin_amdgcn_perm(np, xr[j], 0x07060302u);
                    }
                    unsigned char* dstx = lds + L_XN + (4 * kr + rr) * 2048 + l * 32;
                    *(v4u*)dstx = (v4u){o[0], o[1], o[2], o[3]};
                    *(v4u*)(dstx + 16) = (v4u){o[4], o[5], o[6], o[7]};
                    if (l == 0) *(float*)(lds + L_PIV + (4 * kr + rr) * 4) = -mean[rr];
                }
            }
            lds_flag_inc(SY_X0);
            WAIT_VM0();
            lds_flag_inc(SY_POIS);
            lds_wait_ge(lds, SY_POIS, 2);
        }
    }

    asm volatile("" ::"v"(code_touch));
    WAIT_VM0();
    const int i_ = i;
    const __amdgpu_buffer_rsrc_t ro = __builtin_amdgcn_make_buffer_rsrc(out_, 0, S_ * D_ * 2, 0x00020000);

#pragma unroll 1
    for (int k = 0; k < 3; ++k) {
        int i = i_;
        asm volatile("" : "+v"(i));
        const int l = i & 63;
        const u32 cvoff = (myr >> 2) * C_PITCH + (i >> 1) * 128 + (myr & 3) * 32 + (i & 1) * 16, csoff = WS_C + k * C_SIZE;
        v4u cg = poison4;
        bool cissued = false;
        if (w < 4) {
            const int ch = w >> 1, h = w & 1, a = l & 15, g = l >> 4;
            const unsigned char* ub = lds + L_UR + ch * UR_PITCH + 2 * (2032 + 128 * h - 16 * a + 8 * g) - 3584;
            const u32 ready = (u32)(k + 1);
            if (k == 1 && mytile < KLOAD_SPLIT) load_afrags(A, lds, L_KB, ch, h, l);
            v4f acc[8];
#pragma unroll
            for (int c = 0; c < 8; ++c) acc[c] = (v4f){0, 0, 0, 0};
#pragma unroll
            for (int c = 0; c < 8; ++c) asm volatile("" : "+v"(acc[c]));
            v4u B[2][4];
            __builtin_amdgcn_s_setprio(1);
            for (u32 it = 0;; ++it) {
                asm volatile("" ::: "memory");
                const u32 f0 = lds_ld(lds, SY_INTILE);
                asm volatile("" ::: "memory");
#pragma unroll
                for (int r = 0; r < 4; ++r) B[0][r] = *(const v4u*)(ub + 512 * 7 + 64 * r);
                if (__builtin_expect((u32)__builtin_amdgcn_readfirstlane(f0) >= ready, 1)) break;
                if (spin_give_up(lds, it, LDS_SPIN_CAP)) break;
            }
#define MFA "v_mfma_f32_16x16x32_bf16 "
#define MF1(cur, c, p, r)                                                                                     \
    asm volatile(MFA "%0, %1, %2, %0" : "+v"(acc[c]) : "v"(A[p][r]), "v"(B[cur][r]));                          \
    SCHED_FENCE();
#define MF4(cur, c, p)                                                                                        \
    asm volatile(MFA "%0, %1, %5, %0\n\t" MFA "%0, %2, %6, %0\n\t" MFA "%0, %3, %7, %0\n\t" MFA "%0, %4, %8, %0" \
                 : "+v"(acc[c]) : "v"(A[p][0]), "v"(A[p][1]), "v"(A[p][2]), "v"(A[p][3]),                      \
                   "v"(B[cur][0]), "v"(B[cur][1]), "v"(B[cur][2]), "v"(B[cur][3]));                             \
    SCHED_FENCE();
            u32 vflag = SY_PART + 4 * w, vready = ready, vpart = L_PART + ch * PART_CH + h * 1024 + 16 * (16 * g + ((a + 8 * (g >> 1)) & 15));
            asm volatile("" : "+v"(vflag), "+v"(vready), "+v"(vpart));
            u32 fl = 0;
            MF1(0, 0, 0, 0)
            fl = lds_ld(lds, SY_INTILE + 4); SCHED_FENCE();
            MF1(0, 0, 0, 1)
            B[1][0] = *(const v4u*)(ub + 3072); SCHED_FENCE();
            MF1(0, 0, 0, 2)
            B[1][1] = *(const v4u*)(ub + 3136); SCHED_FENCE();
            MF1(0, 0, 0, 3)
            B[1][2] = *(const v4u*)(ub + 3200); SCHED_FENCE();
            MF1(0, 1, 1, 0)
            B[1][3] = *(const v4u*)(ub + 3264); SCHED_FENCE();
            MF1(0, 1, 1, 1)
            MF1(0, 1, 1, 2)
            put_partial<0>(vpart, acc[0]); SCHED_FENCE();
            MF1(0, 1, 1, 3)
            MF1(0, 2, 2, 0)
            put_partial_flag<0>(vflag, vready); SCHED_FENCE();
            MF1(0, 2, 2, 1)
            const u32 fls0 = (u32)__builtin_amdgcn_readfirstlane(fl); SCHED_FENCE();
            MF1(0, 2, 2, 2)
            MF1(0, 2, 2, 3)
            MF4(0, 3, 3)
            MF4(0, 4, 4)
            MF4(0, 5, 5)
            MF4(0, 6, 6)
            MF4(0, 7, 7)
            if (__builtin_expect(fls0 < ready, 0)) {
                lds_wait_ge(lds, SY_INTILE + 4, ready);
#pragma unroll
                for (int r = 0; r < 4; ++r) B[1][r] = *(const v4u*)(ub + 3072 + 64 * r);
            }
            asm volatile("" ::: "memory");
            SCHED_FENCE();
            MF1(1, 1, 0, 0)
            fl = lds_ld(lds, SY_INTILE + 8); SCHED_FENCE();
            MF1(1, 1, 0, 1)
            B[0][0] = *(const v4u*)(ub + 2560); SCHED_FENCE();
            MF1(1, 1, 0, 2)
            B[0][1] = *(const v4u*)(ub + 2624); SCHED_FENCE();
            MF1(1, 1, 0, 3)
            B[0][2] = *(const v4u*)(ub + 2688); SCHED_FENCE();
            MF1(1, 2, 1, 0)
            B[0][3] = *(const v4u*)(ub + 2752); SCHED_FENCE();
            MF1(1, 2, 1, 1)
            MF1(1, 2, 1, 2)
            put_partial<1>(vpart, acc[1]); SCHED_FENCE();
            MF1(1, 2, 1, 3)
            MF1(1, 3, 2, 0)
            put_partial_flag<1>(vflag, vready); SCHED_FENCE();
            MF1(1, 3, 2, 1)
            const u32 fls1 = (u32)__builtin_amdgcn_readfirstlane(fl); SCHED_FENCE();
            MF1(1, 3, 2, 2)
            MF1(1, 3, 2, 3)
            MF4(1, 4, 3)
            MF4(1, 5, 4)
            MF4(1, 6, 5)
            MF4(1, 7, 6)
            if (__builtin_expect(fls1 < ready, 0)) {
                lds_wait_ge(lds, SY_INTILE + 8, ready);
#pragma unroll
                for (int r = 0; r < 4; ++r) B[0][r] = *(const v4u*)(ub + 2560 + 64 * r);
            }
            asm volatile("" ::: "memory");
            SCHED_FENCE();
            MF1(0, 2, 0, 0)
            fl = lds_ld(lds, SY_INTILE + 12); SCHED_FENCE();
            MF1(0, 2, 0, 1)
            B[1][0] = *(const v4u*)(ub + 2048); SCHED_FENCE();
            MF1(0, 2, 0, 2)
            B[1][1] = *(const v4u*)(ub + 2112); SCHED_FENCE();
            MF1(0, 2, 0, 3)
            B[1][2] = *(const v4u*)(ub + 2176); SCHED_FENCE();
            MF1(0, 3, 1, 0)
            B[1][3] = *(const v4u*)(ub + 2240); SCHED_FENCE();
            MF1(0, 3, 1, 1)
            MF1(0, 3, 1, 2)
            put_partial<2>(vpart, acc[2]); SCHED_FENCE();
            MF1(0, 3, 1, 3)
            MF1(0, 4, 2, 0)
            put_partial_flag<2>(vflag, vready); SCHED_FENCE();
            MF1(0, 4, 2, 1)
            const u32 fls2 = (u32)__builtin_amdgcn_readfirstlane(fl); SCHED_FENCE();
            MF1(0, 4, 2, 2)
            MF1(0, 4, 2, 3)
            MF4(0, 5, 3)
            MF4(0, 6, 4)
            MF4(0, 7, 5)
            if (__builtin_expect(fls2 < ready, 0)) {
                lds_wait_ge(lds, SY_INTILE + 12, ready);
#pragma unroll
                for (int r = 0; r < 4; ++r) B[1][r] = *(const v4u*)(ub + 2048 + 64 * r);
            }
            asm volatile("" ::: "memory");
            SCHED_FENCE();
            MF1(1, 3, 0, 0)
            fl = lds_ld(lds, SY_INTILE + 16); SCHED_FENCE();
            MF1(1, 3, 0, 1)
            B[0][0] = *(const v4u*)(ub + 1536); SCHED_FENCE();
            MF1(1, 3, 0, 2)
            B[0][1] = *(const v4u*)(ub + 1600); SCHED_FENCE();
            MF1(1, 3, 0, 3)
            B[0][2] = *(const v4u*)(ub + 1664); SCHED_FENCE();
            MF1(1, 4, 1, 0)
            B[0][3] = *(const v4u*)(ub + 1728); SCHED_FENCE();
            MF1(1, 4, 1, 1)
            MF1(1, 4, 1, 2)
            put_partial<3>(vpart, acc[3]); SCHED_FENCE();
            MF1(1, 4, 1, 3)
            MF1(1, 5, 2, 0)
            put_partial_flag<3>(vflag, vready); SCHED_FENCE();
            MF1(1, 5, 2, 1)
            const u32 fls3 = (u32)__builtin_amdgcn_readfirstlane(fl); SCHED_FENCE();
            MF1(1, 5, 2, 2)
            MF1(1, 5, 2, 3)
            MF4(1, 6, 3)
            MF4(1, 7, 4)
            if (__builtin_expect(fls3 < ready, 0)) {
                lds_wait_ge(lds, SY_INTILE + 16, ready);
#pragma unroll
                for (int r = 0; r < 4; ++r) B[0][r] = *(const v4u*)(ub + 1536 + 64 * r);
            }
            asm volatile("" ::: "memory");
            SCHED_FENCE();
            MF1(0, 4, 0, 0)
            fl = lds_ld(lds, SY_INTILE + 20); SCHED_FENCE();
            MF1(0, 4, 0, 1)
            B[1][0] = *(const v4u*)(ub + 1024); SCHED_FENCE();
            MF1(0, 4, 0, 2)
            B[1][1] = *(const v4u*)(ub + 1088); SCHED_FENCE();
            MF1(0, 4, 0, 3)
            B[1][2] = *(const v4u*)(ub + 1152); SCHED_FENCE();
            MF1(0, 5, 1, 0)
            B[1][3] = *(const v4u*)(ub + 1216); SCHED_FENCE();
            MF1(0, 5, 1, 1)
            MF1(0, 5, 1, 2)
            put_partial<4>(vpart, acc[4]); SCHED_FENCE();
            MF1(0, 5, 1, 3)
            MF1(0, 6, 2, 0)
            put_partial_flag<4>(vflag, vready); SCHED_FENCE();
            MF1(0, 6, 2, 1)
            const u32 fls4 = (u32)__builtin_amdgcn_readfirstlane(fl); SCHED_FENCE();
            MF1(0, 6, 2, 2)
            MF1(0, 6, 2, 3)
            MF4(0, 7, 3)
            if (__builtin_expect(fls4 < ready, 0)) {
                lds_wait_ge(lds, SY_INTILE + 20, ready);
#pragma unroll
                for (int r = 0; r < 4; ++r) B[1][r] = *(const v4u*)(ub + 1024 + 64 * r);
            }
            asm volatile("" ::: "memory");
            SCHED_FENCE();
            MF1(1, 5, 0, 0)
            fl = lds_ld(lds, SY_INTILE + 24); SCHED_FENCE();
            MF1(1, 5, 0, 1)
            B[0][0] = *(const v4u*)(ub + 512); SCHED_FENCE();
            MF1(1, 5, 0, 2)
            B[0][1] = *(const v4u*)(ub + 576); SCHED_FENCE();
            MF1(1, 5, 0, 3)
            B[0][2] = *(const v4u*)(ub + 640); SCHED_FENCE();
            MF1(1, 6, 1, 0)
            B[0][3] = *(const v4u*)(ub + 704); SCHED_FENCE();
            MF1(1, 6, 1, 1)
            MF1(1, 6, 1, 2)
            put_partial<5>(vpart, acc[5]); SCHED_FENCE();
            MF1(1, 6, 1, 3)
            MF1(1, 7, 2, 0)
            put_partial_flag<5>(vflag, vready); SCHED_FENCE();
            MF1(1, 7, 2, 1)
            const u32 fls5 = (u32)__builtin_amdgcn_readfirstlane(fl); SCHED_FENCE();
            MF1(1, 7, 2, 2)
            MF1(1, 7, 2, 3)
            if (__builtin_expect(fls5 < ready, 0)) {
                lds_wait_ge(lds, SY_INTILE + 24, ready);
#pragma unroll
                for (int r = 0; r < 4; ++r) B[0][r] = *(const v4u*)(ub + 512 + 64 * r);
            }
            asm volatile("" ::: "memory");
            SCHED_FENCE();
            MF1(0, 6, 0, 0)
            fl = lds_ld(lds, SY_INTILE + 28); SCHED_FENCE();
            MF1(0, 6, 0, 1)
            B[1][0] = *(const v4u*)(ub + 0); SCHED_FENCE();
            MF1(0, 6, 0, 2)
            B[1][1] = *(const v4u*)(ub + 64); SCHED_FENCE();
            MF1(0, 6, 0, 3)
            B[1][2] = *(const v4u*)(ub + 128); SCHED_FENCE();
            MF1(0, 7, 1, 0)
            B[1][3] = *(const v4u*)(ub + 192); SCHED_FENCE();
            MF1(0, 7, 1, 1)
            MF1(0, 7, 1, 2)
            put_partial<6>(vpart, acc[6]); SCHED_FENCE();
            MF1(0, 7, 1, 3)
            put_partial_flag<6>(vflag, vready); SCHED_FENCE();
            const u32 fls6 = (u32)__builtin_amdgcn_readfirstlane(fl); SCHED_FENCE();
            if (__builtin_expect(fls6 < ready, 0)) {
                lds_wait_ge(lds, SY_INTILE + 28, ready);
#pragma unroll
                for (int r = 0; r < 4; ++r) B[1][r] = *(const v4u*)(ub + 0 + 64 * r);
            }
            asm volatile("" ::: "memory");
            SCHED_FENCE();
            MF4(1, 7, 0)
            asm volatile("s_nop 7" ::: "memory");
            put_partial<7>(vpart, acc[7]); put_partial_flag<7>(vflag, vready);
            {
                const u32 pub = __builtin_amdgcn_readfirstlane(lds_ld(lds, SY_PUB)), allf = __builtin_amdgcn_readfirstlane(lds_ld(lds, SY_ALLF));
                const u32 pubt = __builtin_amdgcn_readfirstlane(lds_ld(lds, SY_PUBT));
                if (pub >= ready && allf && now_ticks() - pubt >= C_DELAY) { cg = __builtin_amdgcn_raw_buffer_load_b128(rs, cvoff, csoff, 16); cissued = true; }
                SCHED_FENCE();
            }
#undef MF1
#undef MF4
#undef MFA
            __builtin_amdgcn_s_setprio(0);
            if (k == 0 && mytile >= KLOAD_SPLIT) {
                lds_wait_ge_slow(lds, SY_KREADY, 2);
                load_afrags(A, lds, L_KB, ch, h, l);
            }
        } else if (w >= 6) {
            const int ch = l & 1, tl = 32 * (l >> 3) + 8 * ((l >> 1) & 3);
            const float dco = *(const float*)(lds + L_DCO + (k == 0 ? 0 : 8) + ch * 4);
            __builtin_amdgcn_s_setprio(3);
#pragma unroll 1
            for (int c = w & 1; c < 8; c += 2) {
                if (c >= 6) {
                    const u32 pub = __builtin_amdgcn_readfirstlane(lds_ld(lds, SY_PUB)), allf = __builtin_amdgcn_readfirstlane(lds_ld(lds, SY_ALLF));
                    const u32 pubt = __builtin_amdgcn_readfirstlane(lds_ld(lds, SY_PUBT));
                    const u32 in6 = __builtin_amdgcn_readfirstlane(lds_ld(lds, SY_INTILE + 24)), in7 = __builtin_amdgcn_readfirstlane(lds_ld(lds, SY_INTILE + 28));
                    if (pub >= (u32)(k + 1) && allf && in6 >= (u32)(k + 1) && in7 >= (u32)(k + 1) && now_ticks() - pubt >= C_DELAY) { cg = __builtin_amdgcn_raw_buffer_load_b128(rs, cvoff, csoff, 16); cissued = true; }
                }
                const int sb = (l >> 1) & 3, pa = 2 * (l >> 3) + (sb >> 1);
                const unsigned char* pp2 = lds + L_PART + c * PART_TILE + ch * PART_CH + 16 * (32 * (sb & 1) + ((pa + 8 * (sb & 1)) & 15));
                v4f a0, a1, b0, b1;
                v4u u;
                for (u32 it = 0;; ++it) {
                    asm volatile("" ::: "memory");
                    const v4u f = *(const v4u*)(lds + SY_PART + 16 * c);
                    asm volatile("" ::: "memory");
                    a0 = *(const v4f*)pp2; a1 = *(const v4f*)(pp2 + 256);
                    b0 = *(const v4f*)(pp2 + 1024); b1 = *(const v4f*)(pp2 + 1280);
                    u = *(const v4u*)(lds + L_UR + ch * UR_PITCH + 2 * (2040 - (256 * c + tl)));
                    const u32 fa = f.x < f.y ? f.x : f.y, fb = f.z < f.w ? f.z : f.w;
                    if ((u32)__builtin_amdgcn_readfirstlane(fa < fb ? fa : fb) >= (u32)(k + 1)) break;
                    if (spin_give_up(lds, it, LDS_SPIN_CAP)) break;
                }
                v4u o;
                o.x = no_poison(pack_bf(vfma(dco, bf_hi(u.w), vadd(a0.x, b0.x)), vfma(dco, bf_lo(u.w), vadd(a0.y, b0.y))));
                o.y = no_poison(pack_bf(vfma(dco, bf_hi(u.z), vadd(a0.z, b0.z)), vfma(dco, bf_lo(u.z), vadd(a0.w, b0.w))));
                o.z = no_poison(pack_bf(vfma(dco, bf_hi(u.y), vadd(a1.x, b1.x)), vfma(dco, bf_lo(u.y), vadd(a1.y, b1.y))));
                o.w = no_poison(pack_bf(vfma(dco, bf_hi(u.x), vadd(a1.z, b1.z)), vfma(dco, bf_lo(u.x), vadd(a1.w, b1.w))));
                store16(o, rs, (8 * c + (l >> 3)) * C_PITCH + me * 128 + (l & 7) * 16, WS_C + k * C_SIZE);
                if (c == mytile) { lds_flag_set(SY_PUBT, now_ticks()); lds_flag_set(SY_PUB, (u32)(k + 1)); }
            }
            __builtin_amdgcn_s_setprio(2);
        }

        asm volatile("" : "+v"(i));
        float x[8], y[8], dm, rsd;
        u32 n0[8];
        float piv;
        v4u yap = {0, 0, 0, 0};
        if (!cissued) {
            if (k == 0) lds_wait_ge(lds, SY_ALLF, 1u);
            lds_wait_ge(lds, SY_INTILE + 24, (u32)(k + 1));
            lds_wait_ge(lds, SY_INTILE + 28, (u32)(k + 1));
            lds_wait_ge(lds, SY_PUB, (u32)(k + 1));
            wait_until(__builtin_amdgcn_readfirstlane(lds_ld(lds, SY_PUBT)), C_DELAY);
            cg = __builtin_amdgcn_raw_buffer_load_b128(rs, cvoff, csoff, 16);
        }
        SCHED_FENCE();
        if (k == 0) {
            store16(poison4, rs, (i >> 3) * C_PITCH + me * 128 + (i & 7) * 16, WS_C + C_SIZE);
            store16(poison4, rs, myr * U_PITCH + i * 16, WS_U + U_SIZE);
            lds_wait_ge(lds, SY_X0, 2);
#pragma unroll
            for (int t = 0; t < 8; ++t) {
                const u32 xn = *(const u32*)(lds + L_XN + t * 2048 + i * 4);
                x[t] = bf_lo(xn);
                n0[t] = (t & 1) ? (xn & 0xFFFF0000u) : (xn >> 16);
            }
            piv = *(const float*)(lds + L_PIV + (l & 7) * 4);
        } else {
#pragma unroll
            for (int t = 0; t < 8; ++t) n0[t] = (t & 1) ? 0x3F800000u : 0x00003F80u;
            if (k == 1) store16(poison4, rs, (i >> 3) * C_PITCH + me * 128 + (i & 7) * 16, WS_C + 2 * C_SIZE);
            const v4f xa = *(const v4f*)(lds + L_PARK + i * 16), xb = *(const v4f*)(lds + L_PARK + 8192 + i * 16);
            x[0] = xa.x; x[1] = xa.y; x[2] = xa.z; x[3] = xa.w; x[4] = xb.x; x[5] = xb.y; x[6] = xb.z; x[7] = xb.w;
            yap = *(const v4u*)(lds + L_PARK + 16384 + i * 16);
            piv = *(const float*)(lds + L_PARK + 24576 + i * 4);
        }
        v4u sel_lo, sel_hi;
        {
            const u32 one = 0x3F80u << (16 * (l & 1)), dsel = (l & 7) >> 1;
            const v4u pat = {dsel == 0 ? one : 0u, dsel == 1 ? one : 0u, dsel == 2 ? one : 0u, dsel == 3 ? one : 0u};
            const v4u zero = {0, 0, 0, 0};
            sel_lo = (l & 8) ? zero : pat;
            sel_hi = (l & 8) ? pat : zero;
        }
        u32 red_rd0 = L_RED + (l & 15) * 32, red_wr0 = red_rd0 + w * 4, red_rd1 = red_rd0 + 512, red_wr1 = red_wr0 + 512;
        float ya[8] = {bf_lo(yap.x), bf_hi(yap.x), bf_lo(yap.y), bf_hi(yap.y), bf_lo(yap.z), bf_hi(yap.z), bf_lo(yap.w), bf_hi(yap.w)};
        asm volatile("" : "+v"(sel_lo), "+v"(sel_hi), "+v"(red_rd0), "+v"(red_wr0), "+v"(red_rd1), "+v"(red_wr1), "+v"(piv));
        asm volatile("" : "+v"(ya[0]), "+v"(ya[1]), "+v"(ya[2]), "+v"(ya[3]), "+v"(ya[4]), "+v"(ya[5]), "+v"(ya[6]), "+v"(ya[7]));
        asm volatile("" : "+v"(x[0]), "+v"(x[1]), "+v"(x[2]), "+v"(x[3]), "+v"(x[4]), "+v"(x[5]), "+v"(x[6]), "+v"(x[7]));
        asm volatile("" : "+v"(n0[0]), "+v"(n0[1]), "+v"(n0[2]), "+v"(n0[3]), "+v"(n0[4]), "+v"(n0[5]), "+v"(n0[6]), "+v"(n0[7]));
        SCHED_FENCE();
        {
            bool ok = valid16(cg);
            for (u32 it = 0; !__all(ok); ++it) {
                if (spin_give_up(lds, it * 4 + 3, GLB_SPIN_CAP * 4)) break;
                asm volatile("" ::: "memory");
                if (!ok) { cg = __builtin_amdgcn_raw_buffer_load_b128(rs, cvoff, csoff, 16); ok = valid16(cg); }
            }
        }
        const u32 cgp[4] = {cg.x, cg.y, cg.z, cg.w};
        dot_rows(x, cgp, n0);
        if (k == 2) {
#pragma unroll
            for (int t = 0; t < 8; t += 2) {
                const u32 o = pack_bf(x[t], x[t + 1]);
                __builtin_amdgcn_raw_buffer_store_b16((u16)o, ro, i * 2 + t * (D_ * 2), myr * (8 * D_ * 2), 16);
                __builtin_amdgcn_raw_buffer_store_b16((u16)(o >> 16), ro, i * 2 + (t + 1) * (D_ * 2), myr * (8 * D_ * 2), 16);
            }
            break;
        }
        if (k == 0) {
            yap = cg;
        } else {
            sub_rows(y, x, piv);
            row_stats(y, sel_lo, sel_hi, lds, red_rd0, red_wr0, dm, rsd, [] {});
            norm_rows(y, rsd, dm * rsd);
            fma_rows(x, y, ya);
            piv += dm;
        }
        sub_rows(y, x, piv);
        WAIT_VM0();
        auto tail = [&](auto npre_c) {
            constexpr int NPRE = decltype(npre_c)::value;
            const u32 gsoff = WS_U + k * U_SIZE;
            const u32 gvoff = (32 * (w & 1) + (l >> 1)) * U_PITCH + (2 * me + (l & 1)) * 16;
            v4u d[4];
            row_stats(y, sel_lo, sel_hi, lds, red_rd1, red_wr1, dm, rsd, [&] {
                if (NPRE >= 1) d[0] = __builtin_amdgcn_raw_buffer_load_b128(rs, gvoff, gsoff, 16);
                SCHED_FENCE();
            });
            norm_rows(y, rsd, dm * rsd);
            v4u uo;
            uo.x = no_poison(pack_bf(y[7], y[6]));
            uo.y = no_poison(pack_bf(y[5], y[4]));
            uo.z = no_poison(pack_bf(y[3], y[2]));
            uo.w = no_poison(pack_bf(y[1], y[0]));
            SCHED_FENCE();
#pragma unroll
            for (int t = 1; t < NPRE; ++t) d[t] = __builtin_amdgcn_raw_buffer_load_b128(rs, gvoff + t * 64 * U_PITCH, gsoff, 16);
            SCHED_FENCE();
            store16(uo, rs, myr * U_PITCH + i * 16, WS_U + k * U_SIZE);
            const u32 ustore_t = NPRE >= 0 ? now_ticks() : 0u;
            piv += dm;
            *(v4f*)(lds + L_PARK + i * 16) = (v4f){x[0], x[1], x[2], x[3]};
            *(v4f*)(lds + L_PARK + 8192 + i * 16) = (v4f){x[4], x[5], x[6], x[7]};
            *(v4u*)(lds + L_PARK + 16384 + i * 16) = yap;
            *(float*)(lds + L_PARK + 24576 + i * 4) = piv;
            if (NPRE >= 0) {
                __builtin_amdgcn_s_setprio(3);
                unsigned char* dst = lds + L_UR + (l & 1) * UR_PITCH + 2 * (2040 - 8 * (32 * (w & 1) + (l >> 1)));
#pragma unroll
                for (int t = 0; t < 4; ++t) {
                    const int q = 2 * t + (w & 1);
                    if (t == NPRE) {
                        wait_until(ustore_t, U_DELAY);
#pragma unroll
                        for (int u = t; u < 4; ++u) {
                            d[u] = poison4;
                            if (2 * u + (w & 1) <= mytile + GATHER_AHEAD) d[u] = __builtin_amdgcn_raw_buffer_load_b128(rs, gvoff + u * 64 * U_PITCH, gsoff, 16);
                        }
                    }
                    if (__builtin_expect(!__all(valid16(d[t])), 0)) {
                        for (u32 it = 0; !__all(valid16(d[t])); ++it) {
                            if (spin_give_up(lds, it * 4 + 3, GLB_SPIN_CAP * 4)) break;
                            asm volatile("" ::: "memory");
                            if (!valid16(d[t])) d[t] = __builtin_amdgcn_raw_buffer_load_b128(rs, gvoff + t * 64 * U_PITCH, gsoff, 16);
                        }
                    }
                    *(v4u*)(dst - t * 1024) = d[t];
                    lds_flag_set(SY_INTILE + 4 * q, (u32)(k + 2));
                }
                WAIT_VM0();
                __builtin_amdgcn_s_setprio(2);
            }
        };
        if (w == 4 || w == 5) {
            if (mytile >= 6) tail(std::integral_constant<int, 3>());
            else if (mytile >= 4) tail(std::integral_constant<int, 2>());
            else if (mytile >= 2) tail(std::integral_constant<int, 1>());
            else tail(std::integral_constant<int, 0>());
        } else {
            tail(std::integral_constant<int, -1>());
        }
    }
    asm volatile(".Lh3s4_end:");
}

extern "C" void kernel_launch(void* const* d_in, const int* in_sizes, int n_in, void* d_out, int out_size, void* d_ws, size_t ws_size,
                              hipStream_t stream) {
    (void)in_sizes; (void)n_in; (void)out_size; (void)ws_size;
    h3s4_kernel<<<256, 512, 0, stream>>>(
        (const u16*)d_in[0], (const u16*)d_in[1], (const u16*)d_in[2], (const u16*)d_in[3], (const u16*)d_in[4], (const u16*)d_in[5],
        (const u16*)d_in[7], (const u16*)d_in[8], (const u16*)d_in[9], (const u16*)d_in[10], (const u16*)d_in[11], (const u16*)d_in[12],
        (const u16*)d_in[13], (const u16*)d_in[14], (u16*)d_out, (unsigned char*)d_ws);
}
```
